```python
import torch

def _softmax(x, axis=-1):
    return torch.softmax(x, dim=axis)

def _gelu(x):
    return torch.nn.functional.gelu(x, approximate='tanh')

def _silu(x):
    return torch.nn.functional.silu(x)

def _sigmoid(x):
    return torch.sigmoid(x)

def _layernorm(x, axis=-1, eps=1e-05):
    m = x.mean(dim=axis, keepdim=True)
    v = x.var(dim=axis, keepdim=True, unbiased=False)
    return (x - m) / torch.sqrt(v + eps)

def _scatter_add(tgt, idx, src, axis=0):
    return tgt.clone().index_add_(axis, idx, src)

def _segment_sum(src, seg, n):
    out = torch.zeros((n,) + src.shape[1:], dtype=src.dtype)
    return out.index_add_(0, seg, src)

def _pad(x, pw):
    flat = tuple((v for pair in reversed(pw) for v in pair))
    return torch.nn.functional.pad(x, flat)
kernel_name = 'sequential_preLN__memory_aug_recurrent_mem_tr__dense_activation_silu__prompt'

def setup_inputs():

    _w = lambda a: (lambda t: t.to(torch.bfloat16) if t.is_floating_point() else t)(torch.as_tensor(a))

    def _A_setup():
        B, S, Dm = (1, 2048, 1024)
        H, Dh = (32, 128)
        M, seg = (8, 32)
        seg = min(seg, S)
        S = S // seg * seg
        return {'x': _w((lambda *s: torch.randn(*s, dtype=torch.bfloat16) * 0.05)(B, S, Dm)), 'mem0': _w((lambda *s: torch.randn(*s, dtype=torch.bfloat16) * 0.2)(B, M, Dm)), 'Wq': _w((lambda *s: torch.randn(*s, dtype=torch.bfloat16) * 1.6)(Dm, H, Dh) * Dm ** (-0.5)), 'Wk': _w((lambda *s: torch.randn(*s, dtype=torch.bfloat16) * 1.6)(Dm, H, Dh) * Dm ** (-0.5)), 'Wv': _w((lambda *s: torch.randn(*s, dtype=torch.bfloat16) * 0.2)(Dm, H, Dh) * Dm ** (-0.5)), 'Wo': _w((lambda *s: torch.randn(*s, dtype=torch.bfloat16) * 0.2)(H, Dh, Dm) * (H * Dh) ** (-0.5)), 'seg_len': seg}
    _A_in = _A_setup()

    def _B_setup():
        B, S, D, F = (1, 2048, 1024, 3584)
        return {'x': _w((lambda *s: torch.randn(*s, dtype=torch.bfloat16) * 0.05)(B, S, D)), 'w1': _w((lambda *s: torch.randn(*s, dtype=torch.bfloat16) * 0.8)(D, F) * D ** (-0.5)), 'w2': _w((lambda *s: torch.randn(*s, dtype=torch.bfloat16) * 0.1)(F, D) * F ** (-0.5))}
    _B_in = _B_setup()
    return {'A_x': _A_in['x'], 'A_mem0': _A_in['mem0'], 'A_Wq': _A_in['Wq'], 'A_Wk': _A_in['Wk'], 'A_Wv': _A_in['Wv'], 'A_Wo': _A_in['Wo'], 'A_seg_len': _A_in['seg_len'], 'B_x': _B_in['x'], 'B_w1': _B_in['w1'], 'B_w2': _B_in['w2']}

def reference(A_x, A_mem0, A_Wq, A_Wk, A_Wv, A_Wo, A_seg_len, B_x, B_w1, B_w2):

    def _A_ref(x, mem0, Wq, Wk, Wv, Wo, seg_len):
        B, S, Dm = x.shape
        H, Dh = (Wq.shape[1], Wq.shape[2])
        M = mem0.shape[1]
        scale = Dh ** (-0.5)
        n_seg = S // seg_len
        L = M + seg_len
        idx = torch.arange(L)
        mask = (idx[:, None] < M) | (idx[:, None] >= idx[None, :])
        mem = mem0
        outs = []
        for s in range(n_seg):
            xs = x[:, s * seg_len:(s + 1) * seg_len, :].clone()
            h = torch.concatenate([mem, xs], axis=1)
            q = torch.einsum('btd,dhe->bhte', h, Wq)
            k = torch.einsum('btd,dhe->bhte', h, Wk)
            v = torch.einsum('btd,dhe->bhte', h, Wv)
            scores = torch.einsum('bhqe,bhke->bhqk', q, k) * scale
            scores = torch.where(mask[None, None], scores, -1000000000.0)
            p = _softmax(scores, axis=-1)
            o = torch.einsum('bhqk,bhke->bhqe', p, v)
            o = torch.einsum('bhte,hed->btd', o, Wo)
            mem = o[:, :M, :].clone()
            outs.append(o[:, M:, :])
        return torch.concatenate(outs, axis=1)

    def _B_ref(x, w1, w2):
        h = torch.einsum('bsd,df->bsf', x, w1)
        h = _silu(h)
        out = torch.einsum('bsf,fd->bsd', h, w2)
        return out
    x = A_x
    h = x + _A_ref(_layernorm(x, axis=-1), A_mem0, A_Wq, A_Wk, A_Wv, A_Wo, A_seg_len)
    h2 = h + _B_ref(_layernorm(h, axis=-1), B_w1, B_w2)
    return h2
```

```cpp
#include <hip/hip_runtime.h>
#include <stdint.h>
#include <type_traits>

typedef unsigned short u16;
typedef __bf16 bf16x2 __attribute__((ext_vector_type(2)));
typedef __bf16 bf16x4 __attribute__((ext_vector_type(4)));
typedef __bf16 bf16x8 __attribute__((ext_vector_type(8)));
typedef __bf16 bf16x16 __attribute__((ext_vector_type(16)));
typedef short s16x4 __attribute__((ext_vector_type(4)));
typedef float f32x2 __attribute__((ext_vector_type(2)));
typedef float f32x4 __attribute__((ext_vector_type(4)));
typedef unsigned v2u __attribute__((ext_vector_type(2)));
typedef unsigned v4u __attribute__((ext_vector_type(4)));
typedef unsigned v8u __attribute__((ext_vector_type(8)));
#define LAS __attribute__((address_space(3)))
#define GAS __attribute__((address_space(1)))
#define DEVI __device__ __forceinline__
#define RLX_AGENT __ATOMIC_RELAXED, __HIP_MEMORY_SCOPE_AGENT

constexpr int S_TOK = 2048, DM = 1024, NH = 32, DH = 128, HD = NH * DH, MEMT = 8, SEG = 32, NSEG = S_TOK / SEG, FF = 3584;
constexpr int QKV_LD = 3 * HD;

typedef __amdgpu_buffer_rsrc_t rsrc_t;
constexpr int C_TICK = 0;
constexpr int C_VOTE = 896;
constexpr int C_PROG = 1024;
constexpr int C_SLOT = C_PROG + 96 * 128;
constexpr int C_CNT = C_SLOT + 8 * 128;
constexpr int C_QKVF = C_CNT + 256 * 128;
constexpr int C_END = C_QKVF + 768 * 64;
constexpr size_t WS_RING = 131072;
constexpr size_t WS_VB = WS_RING + ((size_t)4 << 20);
constexpr size_t WS_KM = WS_VB + (size_t)64 * 131072;
constexpr size_t WS_XN = WS_KM + (size_t)64 * 131072;
constexpr size_t WS_QKV = WS_XN + (size_t)S_TOK * DM * 2;
constexpr size_t WS_O = WS_QKV + (size_t)S_TOK * QKV_LD * 2;
constexpr size_t WS_H = WS_O + (size_t)S_TOK * HD * 2;
constexpr size_t WS_HN = WS_H + (size_t)S_TOK * DM * 2;
constexpr size_t WS_H1 = WS_HN + (size_t)S_TOK * DM * 2;
constexpr size_t WS_PART = WS_H1 + (size_t)S_TOK * FF * 2;
constexpr size_t WS_DBG = WS_PART + (size_t)(S_TOK + 128) * DM * 8 * 2;
constexpr size_t WS_END = WS_DBG + (1 << 20);
static_assert(C_END <= (int)WS_RING && WS_END < ((size_t)256 << 20), "workspace");
constexpr int RING = 4, R_MB = 0, R_OB = RING * 2 * 16384, R_VR = R_OB + RING * 8 * 16384, R_FLAG = R_VR + RING * 16 * 16384, R_PROBE = R_FLAG + 16384, R_END = R_PROBE + 16 * 16384;
static_assert(R_END <= (2 << 20), "ring window");
__host__ __device__ inline unsigned ring_base(const void* ws) { return (unsigned)WS_RING + (unsigned)((0 - ((uintptr_t)ws + WS_RING)) & ((2u << 20) - 1)); }
constexpr unsigned SENT = 0xFFFFFFFFu;
constexpr int SPIN_CAP = 1 << 20;
constexpr float SCALE = 0.08838834764831845f;
constexpr float LOG2E = 1.4426950408889634f;

DEVI float bf2f(unsigned short b) { return __builtin_bit_cast(float, (unsigned)b << 16); }
DEVI float bflo(unsigned w) { return __builtin_bit_cast(float, w << 16); }
DEVI float bfhi(unsigned w) { return __builtin_bit_cast(float, w & 0xffff0000u); }
DEVI unsigned pk2(float lo, float hi) { f32x2 v = {lo, hi}; return __builtin_bit_cast(unsigned, __builtin_convertvector(v, bf16x2)); }
DEVI float rbf(float x) { return (float)(__bf16)x; }
DEVI unsigned nosent(unsigned w) { return w < 0xFFFFFFFEu ? w : 0xFFFFFFFEu; }
DEVI v2u trread(LAS const unsigned char* p) {
  return __builtin_bit_cast(v2u, __builtin_amdgcn_ds_read_tr16_b64_v4i16((LAS s16x4*)p)); }
DEVI f32x4 mfma32(v4u a, v4u b, f32x4 c) {
  return __builtin_amdgcn_mfma_f32_16x16x32_bf16(__builtin_bit_cast(bf16x8, a), __builtin_bit_cast(bf16x8, b), c, 0, 0, 0); }
DEVI f32x4 mfma16(v2u a, v2u b, f32x4 c) {
  return mfma32((v4u){a[0], a[1], 0u, 0u}, (v4u){b[0], b[1], 0u, 0u}, c); }
DEVI f32x4 smfma(v4u a, v8u b, f32x4 c, int idx) {
  return __builtin_amdgcn_smfmac_f32_16x16x64_bf16(__builtin_bit_cast(bf16x8, a), __builtin_bit_cast(bf16x16, b), c, idx, 0, 0); }
DEVI void glds16(const void* g, LAS unsigned char* l) { __builtin_amdgcn_global_load_lds((const GAS unsigned*)g, (LAS unsigned*)l, 16, 0, 0); }
#define WAIT_V(n) asm volatile("s_waitcnt vmcnt(" #n ")" ::: "memory")
#define WAIT_L(n) asm volatile("s_waitcnt lgkmcnt(" #n ")" ::: "memory")
#define BAR() do { asm volatile("" ::: "memory"); __builtin_amdgcn_s_barrier(); asm volatile("" ::: "memory"); } while (0)
template <int N> DEVI void wait_v() { asm volatile("s_waitcnt vmcnt(%0)" :: "i"(N) : "memory"); }

DEVI float xmax16_32(float x) {
  unsigned u = __builtin_bit_cast(unsigned, x);
  auto r = __builtin_amdgcn_permlane16_swap(u, u, false, false);
  x = fmaxf(__builtin_bit_cast(float, (unsigned)r[0]), __builtin_bit_cast(float, (unsigned)r[1]));
  u = __builtin_bit_cast(unsigned, x);
  auto q = __builtin_amdgcn_permlane32_swap(u, u, false, false);
  return fmaxf(__builtin_bit_cast(float, (unsigned)q[0]), __builtin_bit_cast(float, (unsigned)q[1]));
}
DEVI float xsum16_32(float x) {
  unsigned u = __builtin_bit_cast(unsigned, x);
  auto r = __builtin_amdgcn_permlane16_swap(u, u, false, false);
  x = __builtin_bit_cast(float, (unsigned)r[0]) + __builtin_bit_cast(float, (unsigned)r[1]);
  u = __builtin_bit_cast(unsigned, x);
  auto q = __builtin_amdgcn_permlane32_swap(u, u, false, false);
  return __builtin_bit_cast(float, (unsigned)q[0]) + __builtin_bit_cast(float, (unsigned)q[1]);
}
DEVI float fold32(float a, float b) {
  auto r = __builtin_amdgcn_permlane32_swap(__builtin_bit_cast(unsigned, a), __builtin_bit_cast(unsigned, b), false, false);
  return __builtin_bit_cast(float, (unsigned)r[0]) + __builtin_bit_cast(float, (unsigned)r[1]);
}

DEVI int aimg(int r, int ch) { return r * 128 + ((ch ^ ((r >> 1) & 7)) << 4); }
template <int I, int N, class F> DEVI void sfor(F&& f) { if constexpr (I < N) { f(std::integral_constant<int, I>{}); sfor<I + 1, N>(f); } }
DEVI v4u ld16(rsrc_t rs, int voff, int soff) { return __builtin_amdgcn_raw_buffer_load_b128(rs, voff, soff, 16); }
DEVI v2u ld8(rsrc_t rs, int voff, int soff) { return __builtin_amdgcn_raw_buffer_load_b64(rs, voff, soff, 16); }
DEVI void st16(v4u v, rsrc_t rs, int voff, int soff) {
  __builtin_amdgcn_raw_buffer_store_b128(v, rs, voff, soff, 16); asm volatile("s_nop 1" :: "v"(v) : "memory"); }
template <bool SC> DEVI void dma16(rsrc_t rs, unsigned lds, int voff, int soff) {
  int so;
  if (SC) asm volatile("s_mov_b32 m0, %1\n\ts_mov_b32 %0, %4\n\tbuffer_load_dwordx4 %2, %3, %0 offen sc1 lds" : "=&s"(so) : "s"(lds), "v"(voff), "s"(rs), "s"(soff) : "memory");
  else asm volatile("s_mov_b32 m0, %1\n\ts_mov_b32 %0, %4\n\tbuffer_load_dwordx4 %2, %3, %0 offen lds" : "=&s"(so) : "s"(lds), "v"(voff), "s"(rs), "s"(soff) : "memory");
}
#define PH(k) do {} while (0)
DEVI int fresh_lane() { int l; asm volatile("v_mbcnt_lo_u32_b32 %0, -1, 0\n\tv_mbcnt_hi_u32_b32 %0, -1, %0" : "=v"(l)); return l; }
DEVI v4u lds16(unsigned a) { return *(LAS const v4u*)(uintptr_t)a; }
DEVI v2u ldstr(unsigned a) { return trread((LAS const unsigned char*)(uintptr_t)a); }
DEVI v4u pack_pair(f32x4 a, f32x4 b) {
  auto r0 = __builtin_amdgcn_permlane16_swap(pk2(a[0], a[1]), pk2(b[0], b[1]), false, false);
  auto r1 = __builtin_amdgcn_permlane16_swap(pk2(a[2], a[3]), pk2(b[2], b[3]), false, false);
  return (v4u){(unsigned)r0[0], (unsigned)r1[0], (unsigned)r0[1], (unsigned)r1[1]};
}

__global__ void __launch_bounds__(256) k_init(const u16* __restrict__ x, const u16* __restrict__ mem0, unsigned char* __restrict__ ws) {
  const int tid = threadIdx.x, lane = tid & 63, wave = tid >> 6;
  const int row = blockIdx.x * 4 + wave;
  {
    const u16* src = x + (size_t)row * DM;
    const v4u a = *(const v4u*)(src + lane * 8), b = *(const v4u*)(src + 512 + lane * 8);
    float v[16];
#pragma unroll
    for (int j = 0; j < 4; ++j) { v[2 * j] = bflo(a[j]); v[2 * j + 1] = bfhi(a[j]); v[8 + 2 * j] = bflo(b[j]); v[8 + 2 * j + 1] = bfhi(b[j]); }
    float s = 0.f;
#pragma unroll
    for (int j = 0; j < 16; ++j) s += v[j];
#pragma unroll
    for (int o = 1; o < 64; o <<= 1) s += __shfl_xor(s, o);
    const float mean = s * (1.0f / DM);
    float q = 0.f;
#pragma unroll
    for (int j = 0; j < 16; ++j) { const float d = v[j] - mean; q += d * d; }
#pragma unroll
    for (int o = 1; o < 64; o <<= 1) q += __shfl_xor(q, o);
    const float mb = rbf(mean), sd = rbf(sqrtf(rbf(rbf(q * (1.0f / DM)) + 1e-5f)));
    v4u oa, ob;
#pragma unroll
    for (int j = 0; j < 4; ++j) {
      oa[j] = pk2(rbf(v[2 * j] - mb) / sd, rbf(v[2 * j + 1] - mb) / sd);
      ob[j] = pk2(rbf(v[8 + 2 * j] - mb) / sd, rbf(v[8 + 2 * j + 1] - mb) / sd);
    }
    unsigned char* d = ws + WS_XN + (size_t)(row >> 7) * (16 * 16384) + (lane >> 3) * 16384 + aimg(row & 127, lane & 7);
    *(v4u*)d = oa; *(v4u*)(d + 8 * 16384) = ob;
  }
  const int gt = blockIdx.x * 256 + tid;
  unsigned char* ring = ws + ring_base(ws);
  if (gt + 2048 < R_PROBE / 16) ((v4u*)ring)[gt + 2048] = (v4u){SENT, SENT, SENT, SENT};
  if (gt < C_END / 16) ((v4u*)ws)[gt] = (v4u){0u, 0u, 0u, 0u};
  if (gt < 1024) {
    const int q = gt, j = q >> 5, r = (q >> 4) & 1, a1 = (q >> 3) & 1, hh = (q >> 2) & 1, i3 = q & 3;
    const unsigned* src = (const unsigned*)(mem0 + (4 * r + i3) * DM + 32 * j + 16 * a1 + 2 * hh);
    v4u v = {nosent(src[0]), nosent(src[2]), nosent(src[4]), nosent(src[6])};
    unsigned char* d = ring + R_MB + (q >> 9) * 16384 + (q & 511) * 16;
    *(v4u*)d = v; *(v4u*)(d + 8192) = v;
  }
}
static_assert(R_PROBE / 16 <= 2048 + 512 * 256 && C_END % 16 == 0, "k_init covers the rings");

template <int BN> DEVI int bswz(int k) { return BN >= 128 ? ((k & 3) | (((k >> 3) & 1) << 2)) : (((k >> 1) & 1) | (((k >> 3) & 1) << 1)); }
template <int MODE> struct Gemm {
  static constexpr bool BIG = MODE == 0;
  static constexpr int BM = MODE == 2 ? 64 : 128, BN = BIG ? 256 : BM, WN = BIG ? 4 : 2, ST = BIG ? 3 : (MODE == 1 ? 4 : 8), T = MODE == 2 ? 2 : 4;
  static constexpr int ABYTES = BM * 128, STB = (BM + BN) * 128, LA = BM / 64, LB = BN / 64, LPT = LA + LB, NRD = 3 * T, NM = T * T, BR = 4096 / BN;
  struct Frag { v4u a[T]; v2u lo[T], hi[T]; };
  rsrc_t rA, rB; int aoffs, boffs, ldb2, nt, wave, av, bv;
  DEVI void init(rsrc_t rA_, rsrc_t rB_, int aoffs_, int boffs_, int ldb2_, int nt_, int wave_, int lane) {
    rA = rA_; rB = rB_; aoffs = aoffs_; boffs = boffs_; ldb2 = ldb2_; nt = nt_; wave = wave_;
    const int p = wave * 64 + lane, k = p / (BN / 8), cc = p % (BN / 8);
    av = p * 16; bv = k * ldb2 + ((((cc >> 1) ^ bswz<BN>(k)) << 1) | (cc & 1)) * 16;
  }
  template <int i> DEVI void piece(int t, int slot) const {
    const unsigned base = slot * STB + wave * 1024;
    if constexpr (i < LA) dma16<true>(rA, base + i * 8192, av, t < nt ? aoffs + t * 16384 + i * 8192 : 0x7ff00000);
    else dma16<false>(rB, base + ABYTES + (i - LA) * 8192, bv, t < nt ? boffs + (t * 64 + (i - LA) * BR) * ldb2 : 0x7ff00000);
  }
  DEVI void prefetch_b() const {
#pragma unroll
    for (int s = 0; s < (BIG ? ST - 1 : ST); ++s) sfor<LA, LPT>([&](auto I) { piece<I>(s, s); });
    if constexpr (BIG) piece<LA>(ST - 1, ST - 1);
  }
  template <class F> DEVI void run(f32x4 (&acc)[T][T], int lane, F&& after_loop) const {
    const int grp = BIG ? 0 : wave >> 2, wq = BIG ? wave : wave & 3;
    const int wm = wq / WN, wn = wq % WN, c = lane & 15, g = lane >> 4;
    const unsigned a0 = aimg(wm * (16 * T) + c, g);
    const int krow = 8 * g + (c >> 2);
    unsigned b0[T];
#pragma unroll
    for (int n = 0; n < T; ++n) b0[n] = ABYTES + krow * (BN * 2) + (c & 3) * 8 + (((wn * T + n) ^ bswz<BN>(krow)) << 5);
    auto rd1 = [&](auto R, Frag& f, unsigned aa, unsigned bo) {
      constexpr int r = R;
      if constexpr (r < 2 * T) { if constexpr (r & 1) f.hi[r >> 1] = ldstr(bo + b0[r >> 1] + 8 * BN); else f.lo[r >> 1] = ldstr(bo + b0[r >> 1]); }
      else f.a[r - 2 * T] = lds16(aa + (r - 2 * T) * 2048);
    };
    auto half = [&](auto P0, auto NP, Frag& fc, Frag& fn, unsigned sb, int ks, int tdma, int sdma) {
      const unsigned aa = sb + (a0 ^ (ks << 6)), bo = sb + ks * (64 * BN);
      sfor<0, NM>([&](auto J) {
        constexpr int j = J, m = j / T, n = j % T;
        const v4u bf = {fc.lo[n][0], fc.lo[n][1], fc.hi[n][0], fc.hi[n][1]};
        asm volatile("v_mfma_f32_16x16x32_bf16 %0, %1, %2, %0" : "+v"(acc[m][n]) : "v"(bf), "v"(fc.a[m]) : "memory");
        constexpr int RW = NM - (T == 4 ? 3 : 1), r0 = j < RW ? j * NRD / RW : NRD, r1 = j < RW ? (j + 1) * NRD / RW : NRD;
        constexpr int d0 = (j * NP + NM - 1) / NM, d1 = ((j + 1) * NP + NM - 1) / NM;
        sfor<d0, d1>([&](auto S) { piece<P0 + S>(tdma, sdma); });
        sfor<r0, r1>([&](auto S) { rd1(S, fn, aa, bo); });
      });
    };
    using I0 = std::integral_constant<int, 0>; using I3 = std::integral_constant<int, 3>;
#pragma unroll
    for (int m = 0; m < T; ++m)
#pragma unroll
      for (int n = 0; n < T; ++n) { acc[m][n] = (f32x4){0.f, 0.f, 0.f, 0.f}; asm volatile("" : "+v"(acc[m][n])); }
    Frag f0, f1;
    int slot = 0;
#pragma unroll
    for (int s = 0; s < ST; ++s) sfor<0, LA>([&](auto I) { piece<I>(s, s); });
    PH(0);
    wait_v<(ST - 1) * LA>(); BAR();
    PH(1);
    if constexpr (BIG) {
      sfor<0, NRD>([&](auto R) { rd1(R, f0, a0, 0); });
      int pslot = ST - 1;
      for (int t = 0; t < nt; ++t) {
        half(I3{}, I3{}, f0, f1, slot * STB, 1, t + ST - 1, pslot);
        asm volatile("s_waitcnt vmcnt(5) lgkmcnt(0)" ::: "memory");
        __builtin_amdgcn_s_barrier();
        pslot = slot; if (++slot == ST) slot = 0;
        half(I0{}, I3{}, f1, f0, slot * STB, 0, t + ST, pslot);
      }
    } else {
      using IL = std::integral_constant<int, LPT>;
      sfor<0, NRD>([&](auto R) { rd1(R, f0, a0 ^ (grp << 6), grp * (64 * BN)); });
      sfor<0, (ST - 2) / 2>([&](auto TT) { constexpr int t = 2 * TT;
        asm volatile("s_waitcnt vmcnt(%0) lgkmcnt(0)" :: "i"((ST - 2 - t) * LA + t * LPT) : "memory"); __builtin_amdgcn_s_barrier();
        half(I0{}, IL{}, f0, f1, (t + 1) * STB, grp, t + ST, t);
        asm volatile("s_waitcnt vmcnt(%0) lgkmcnt(0)" :: "i"((ST - 3 - t) * LA + (t + 1) * LPT) : "memory"); __builtin_amdgcn_s_barrier();
        half(I0{}, IL{}, f1, f0, (t + 2) * STB, grp, t + 1 + ST, t + 1); });
      slot = ST - 2;
      for (int t = ST - 2; t < nt; t += 2) {
        asm volatile("s_waitcnt vmcnt(%0) lgkmcnt(0)" :: "i"((ST - 2) * LPT) : "memory"); __builtin_amdgcn_s_barrier();
        int ps = slot; slot = (slot + 1) & (ST - 1);
        half(I0{}, IL{}, f0, f1, slot * STB, grp, t + ST, ps);
        asm volatile("s_waitcnt vmcnt(%0) lgkmcnt(0)" :: "i"((ST - 2) * LPT) : "memory"); __builtin_amdgcn_s_barrier();
        ps = slot; slot = (slot + 1) & (ST - 1);
        half(I0{}, IL{}, f1, f0, slot * STB, grp, t + 1 + ST, ps);
      }
    }
    asm volatile("s_nop 7\n\ts_nop 1\n\ts_waitcnt vmcnt(0) lgkmcnt(0)" ::: "memory");
    PH(2);
    after_loop();
#pragma unroll
    for (int m = 0; m < T; ++m)
#pragma unroll
      for (int n = 0; n < T; ++n) asm volatile("" : "+v"(acc[m][n]));
    if constexpr (!BIG) {
      constexpr int H = T / 2, XB = MODE == 2 ? ST * STB : 0;
      if constexpr (MODE == 1) BAR();
      LAS unsigned char* X = (LAS unsigned char*)(uintptr_t)XB + lane * 16;
#pragma unroll
      for (int m = 0; m < H; ++m)
#pragma unroll
        for (int n = 0; n < T; ++n) *(LAS f32x4*)(X + (wave * H * T + m * T + n) * 1024) = grp ? acc[m][n] : acc[m + H][n];
      WAIT_L(0); BAR();
#pragma unroll
      for (int m = 0; m < H; ++m)
#pragma unroll
        for (int n = 0; n < T; ++n) { const f32x4 o = *(LAS f32x4*)(X + ((wave ^ 4) * H * T + m * T + n) * 1024); acc[m][n] = grp ? o + acc[m + H][n] : acc[m][n] + o; }
    }
    PH(3);
  }
};
DEVI v4u silu8(v4u w) {
#pragma unroll
  for (int j = 0; j < 4; ++j) { const float u = bflo(w[j]), v = bfhi(w[j]);
    w[j] = pk2(u * __builtin_amdgcn_rcpf(1.0f + __builtin_amdgcn_exp2f(-LOG2E * u)), v * __builtin_amdgcn_rcpf(1.0f + __builtin_amdgcn_exp2f(-LOG2E * v))); }
  return w;
}

DEVI void attn_item(rsrc_t rs, GAS unsigned* ctl, int s, int h, int wave, int lane, int& budget) {
  const int c = lane & 15, g = lane >> 4, l16 = lane * 16;
  const unsigned vl = wave * 8192;
  const int blk = (int)WS_QKV + (s * NH + h) * (3 * 8192);
#pragma unroll
  for (int i = 0; i < 8; ++i) dma16<true>(rs, vl + i * 1024, l16, blk + 16384 + i * 1024);
  v4u qf[2][4], kf[3][4]; v2u vm[8];
#pragma unroll
  for (int ks = 0; ks < 4; ++ks) {
#pragma unroll
    for (int t = 0; t < 2; ++t) { qf[t][ks] = ld16(rs, l16, blk + t * 4096 + ks * 1024); kf[1 + t][ks] = ld16(rs, l16, blk + 8192 + t * 4096 + ks * 1024); }
  }
  f32x4 sc[3][2];
#pragma unroll
  for (int kt = 1; kt < 3; ++kt)
#pragma unroll
    for (int t = 0; t < 2; ++t) { sc[kt][t] = (f32x4){0.f, 0.f, 0.f, 0.f};
      if (kt == 2 && t == 0) continue;
#pragma unroll
      for (int ks = 0; ks < 4; ++ks) sc[kt][t] = mfma32(kf[kt][ks], qf[t][ks], sc[kt][t]); }
  PH(0);
  { GAS unsigned* w = ctl + (C_PROG + ((lane < 3 ? lane : 0) * 32 + h) * 128) / 4;
    while (__builtin_amdgcn_ballot_w64(__hip_atomic_load(w, RLX_AGENT) <= (unsigned)s) != 0ull && --budget > 0) __builtin_amdgcn_s_sleep(4);
    asm volatile("" ::: "memory"); }
  PH(1);
#pragma unroll
  for (int ks = 0; ks < 4; ++ks) kf[0][ks] = ld16(rs, l16, (int)WS_KM + (s * NH + h) * 4096 + ks * 1024);
#pragma unroll
  for (int T = 0; T < 8; ++T) vm[T] = ld8(rs, lane * 8, (int)WS_VB + (s * NH + h) * 4096 + T * 512);
#pragma unroll
  for (int t = 0; t < 2; ++t) { sc[0][t] = (f32x4){0.f, 0.f, 0.f, 0.f};
#pragma unroll
    for (int ks = 0; ks < 4; ++ks) sc[0][t] = mfma32(kf[0][ks], qf[t][ks], sc[0][t]); }
  v4u px[2]; v2u pm[2];
#pragma unroll
  for (int t = 0; t < 2; ++t) {
    float v[12]; float mx = -INFINITY;
#pragma unroll
    for (int kt = 0; kt < 3; ++kt)
#pragma unroll
      for (int r = 0; r < 4; ++r) {
        const bool ok = kt == 0 ? true : (16 * (kt - 1) + 4 * g + r <= 16 * t + c);
        const float z = rbf(rbf(sc[kt][t][r]) * SCALE);
        v[kt * 4 + r] = ok ? z : -INFINITY; mx = fmaxf(mx, v[kt * 4 + r]); }
    mx = xmax16_32(mx);
    float sum = 0.f;
#pragma unroll
    for (int j = 0; j < 12; ++j) { v[j] = __builtin_amdgcn_exp2f((v[j] - mx) * LOG2E); sum += j < 4 ? 0.5f * v[j] : v[j]; }
    sum = xsum16_32(sum);
    const float inv = __builtin_amdgcn_rcpf(sum);
    pm[t] = (v2u){pk2(v[0] * inv, v[1] * inv), pk2(v[2] * inv, v[3] * inv)};
    px[t] = (v4u){pk2(v[4] * inv, v[5] * inv), pk2(v[6] * inv, v[7] * inv), pk2(v[8] * inv, v[9] * inv), pk2(v[10] * inv, v[11] * inv)};
  }
  WAIT_V(0);
  PH(2);
  const unsigned va = vl + (lane >> 2) * 64 + (lane & 3) * 8;
  const int obase = (int)WS_O + ((s >> 2) * 64 + 2 * h) * 16384, gp = (g & 1) * 2 + (g >> 1);
#pragma unroll
  for (int T = 0; T < 8; T += 2) {
    f32x4 o[2][2];
#pragma unroll
    for (int u = 0; u < 2; ++u) {
      const v2u lo = ldstr(va + (T >> 1) * 2048 + u * 32), hi = ldstr(va + (T >> 1) * 2048 + u * 32 + 1024);
      const v4u vf = {lo[0], lo[1], hi[0], hi[1]};
#pragma unroll
      for (int t = 0; t < 2; ++t) { o[u][t] = mfma16(vm[T + u], pm[t], (f32x4){0.f, 0.f, 0.f, 0.f}); o[u][t] = mfma32(vf, px[t], o[u][t]); }
    }
#pragma unroll
    for (int t = 0; t < 2; ++t) st16(pack_pair(o[0][t], o[1][t]), rs, aimg(32 * (s & 3) + 16 * t + c, (T & 2) * 2 + gp), obase + (T >> 2) * 16384);
  }
}

DEVI float wave_sum(float s) {
  s += __builtin_bit_cast(float, __builtin_amdgcn_mov_dpp(__builtin_bit_cast(int, s), 0xB1, 0xF, 0xF, true));
  s += __builtin_bit_cast(float, __builtin_amdgcn_mov_dpp(__builtin_bit_cast(int, s), 0x4E, 0xF, 0xF, true));
  s += __builtin_bit_cast(float, __builtin_amdgcn_mov_dpp(__builtin_bit_cast(int, s), 0x141, 0xF, 0xF, true));
  s += __builtin_bit_cast(float, __builtin_amdgcn_mov_dpp(__builtin_bit_cast(int, s), 0x140, 0xF, 0xF, true));
  return xsum16_32(s);
}
template <bool FIN>
DEVI void reduce_row(rsrc_t rs, const u16* __restrict__ x, u16* __restrict__ out, int row, int nch, int lane) {
  const int l16 = lane * 16, pb = (int)WS_PART + (row >> 7) * (8 * 262144) + (row & 127) * 2048, hb = (int)WS_H + row * 2048;
  v4u res[2];
#pragma unroll
  for (int q = 0; q < 2; ++q) res[q] = FIN ? ld16(rs, l16, hb + q * 1024) : *(const v4u*)(x + (size_t)row * DM + q * 512 + lane * 8);
  float sum[16];
  if (nch <= 4) {
    v4u pt[4][2];
#pragma unroll
    for (int k = 0; k < 4; ++k)
#pragma unroll
      for (int q = 0; q < 2; ++q) pt[k][q] = ld16(rs, l16, k < nch ? pb + k * 262144 + q * 1024 : 0x7ff00000);
#pragma unroll
    for (int j = 0; j < 8; ++j) {
      sum[2 * j] = (bflo(pt[0][j >> 2][j & 3]) + bflo(pt[1][j >> 2][j & 3])) + (bflo(pt[2][j >> 2][j & 3]) + bflo(pt[3][j >> 2][j & 3]));
      sum[2 * j + 1] = (bfhi(pt[0][j >> 2][j & 3]) + bfhi(pt[1][j >> 2][j & 3])) + (bfhi(pt[2][j >> 2][j & 3]) + bfhi(pt[3][j >> 2][j & 3])); }
  } else {
    v4u pt[16][2];
#pragma unroll
    for (int k = 0; k < 16; ++k)
#pragma unroll
      for (int q = 0; q < 2; ++q) pt[k][q] = ld16(rs, l16, k < nch ? pb + k * 262144 + q * 1024 : 0x7ff00000);
#pragma unroll
    for (int j = 0; j < 16; ++j) sum[j] = 0.f;
#pragma unroll
    for (int k = 0; k < 16; ++k)
#pragma unroll
      for (int j = 0; j < 8; ++j) { sum[2 * j] += bflo(pt[k][j >> 2][j & 3]); sum[2 * j + 1] += bfhi(pt[k][j >> 2][j & 3]); }
  }
  v4u h[2];
#pragma unroll
  for (int j = 0; j < 8; ++j) h[j >> 2][j & 3] = pk2(bflo(res[j >> 2][j & 3]) + rbf(sum[2 * j]), bfhi(res[j >> 2][j & 3]) + rbf(sum[2 * j + 1]));
  if (FIN) {
#pragma unroll
    for (int q = 0; q < 2; ++q) *(v4u*)(out + (size_t)row * DM + q * 512 + lane * 8) = h[q];
    return;
  }
  float s = 0.f;
#pragma unroll
  for (int q = 0; q < 2; ++q)
#pragma unroll
    for (int j = 0; j < 4; ++j) s += bflo(h[q][j]) + bfhi(h[q][j]);
  const float mean = wave_sum(s) * (1.0f / DM);
  s = 0.f;
#pragma unroll
  for (int q = 0; q < 2; ++q)
#pragma unroll
    for (int j = 0; j < 4; ++j) { const float d0 = bflo(h[q][j]) - mean, d1 = bfhi(h[q][j]) - mean; s += d0 * d0 + d1 * d1; }
  const float var = wave_sum(s) * (1.0f / DM);
  const float mb = rbf(mean), inv = 1.0f / rbf(sqrtf(rbf(rbf(var) + 1e-5f)));
#pragma unroll
  for (int q = 0; q < 2; ++q) { v4u o;
#pragma unroll
    for (int j = 0; j < 4; ++j) o[j] = pk2(rbf(bflo(h[q][j]) - mb) * inv, rbf(bfhi(h[q][j]) - mb) * inv);
    st16(o, rs, (lane >> 3) * 16384 + aimg(row & 127, lane & 7), (int)WS_HN + ((row >> 7) * 16 + q * 8) * 16384); }
#pragma unroll
  for (int q = 0; q < 2; ++q) st16(h[q], rs, l16, hb + q * 1024);
}

#define SLEEP_P 16
#define SLEEP_M 14
#define SLEEP_H 6
#define SLEEP_V 8
#define STAGGER 7
#define SPREAD 1
#define SLEEP_K 22
#define PLACE 1
#define STW_P 0
#define STW_M 0
#define FORCE_PAR -1
constexpr int CHAIN_LDS = 160 * 1024;

DEVI int probe_parity(unsigned char* ws, LAS unsigned char* L, int ring, int slot, int wave, int lane) {
  if (FORCE_PAR >= 0) return FORCE_PAR;
  const rsrc_t rs = __builtin_amdgcn_make_buffer_rsrc(ws, 0, (int)WS_END, 0x00020000);
  const int so = ring + R_PROBE + (wave * 2 + (slot >> 6)) * 16384 + (slot & 63) * 128;
  int best0 = 1 << 30, best1 = 1 << 30;
#pragma unroll 1
  for (int rep = 0; rep < 6; ++rep) {
    const int par = rep & 1;
    if (lane < 8) __builtin_amdgcn_raw_buffer_store_b128((v4u){0u, 0u, 0u, 0u}, rs, lane * 16, so + par * 8192, 16);
    WAIT_V(0);
    const unsigned long long t0 = __builtin_amdgcn_s_memtime();
    unsigned v = __builtin_amdgcn_raw_buffer_load_b32(rs, 0, so + par * 8192, 16);
    asm volatile("s_waitcnt vmcnt(0)" : "+v"(v) :: "memory");
    const int dt = (int)(__builtin_amdgcn_s_memtime() - t0);
    if (par) best1 = min(best1, dt); else best0 = min(best0, dt);
  }
  if (lane == 0) *(LAS int*)(L + wave * 4) = best1 < best0 ? 1 : 0;
  __syncthreads();
  const v4u x = *(LAS v4u*)L, y = *(LAS v4u*)(L + 16);
  const int votes = x[0] + x[1] + x[2] + x[3] + y[0] + y[1] + y[2] + y[3];
  __syncthreads();
  return __builtin_amdgcn_readfirstlane(votes > 4 ? 1 : 0);
}

#define CHAIN_CLOBBERS "v0", "v1", "v2", "v3", "v4", "v5", "v6", "v7", "v8", "v9", "v10", "v11", "v12", "v13", "v14", "v15", "v16", "v17", "v18", "v19", "v20", "v21", "v22", "v23", "v24", "v25", "v26", "v27", "v28", "v29", "v30", "v31", "v32", "v33", "v34", "v35", "v36", "v37", "v38", "v39", "v40", "v41", "v42", "v43", "v44", "v45", "v46", "v47", "v48", "v49", "v50", "v51", "v52", "v53", "v54", "v55", "v56", "v57", "v58", "v59", "v60", "v61", "v62", "v63", "v64", "v65", "v66", "v67", "v68", "v69", "v70", "v71", "v72", "v73", "v74", "v75", "v76", "v77", "v78", "v79", "v80", "v81", "v82", "v83", "v84", "v85", "v86", "v87", "v88", "v89", "v90", "v91", "v92", "v93", "v94", "v95", "v96", "v97", "v98", "v99", "v100", "v101", "v102", "v103", "v104", "v105", "v106", "v107", "v108", "v109", "v110", "v111", "v112", "v113", "v114", "v115", "v116", "v117", "v118", "v119", "v120", "v121", "v122", "v123", "v124", "v125", "v126", "v127", "v128", "v129", "v130", "v131", "v132", "v133", "v134", "v135", "v136", "v137", "v138", "v139", "v140", "v141", "v142", "v143", "v144", "v145", "v146", "v147", "v148", "v149", "v150", "v151", "v152", "v153", "v154", "v155", "v156", "v157", "v158", "v159", "v160", "v161", "v162", "v163", "v164", "v165", "v166", "v167", "v168", "v169", "v170", "v171", "v172", "v173", "v174", "v175", "v176", "v177", "v178", "v179", "v180", "v181", "v182", "v183", "v184", "v185", "v186", "v187", "v188", "v189", "v190", "v191", "v192", "v193", "v194", "v195", "v196", "v197", "v198", "v200", "v201", "v202", "v203", "v204", "v205", "v206", "v207", "v208", "v209", "v210", "v211", "v212", "v213", "v214", "v215", "v216", "v217", "v218", "v219", "v220", "v221", "v222", "v223", "v224", "v225", "v226", "v227", "v228", "v229", "v230", "v231", "v232", "v233", "v234", "v235", "v236", "v237", "v238", "v239", "v240", "v241", "v242", "v243", "v244", "v245", "v246", "v247", "v248", "v249", "v250", "v251", "v252", "v253", "v254", "v255", "s40", "s41", "s42", "s43", "s44", "s45", "s46", "s47", "s48", "s49", "s50", "s51", "s52", "s53", "s54", "s55", "s56", "s57", "s58", "s59", "s60", "s61", "s62", "s63", "s64", "s65", "s66", "s67", "s68", "s69", "s70", "s71", "s72", "s73", "s74", "s75", "s76", "s77", "s78", "s79", "s80", "s81", "s82", "s83", "s84", "s85", "s86", "s87", "s88", "s89", "s90", "s91", "s92", "s93", "s94", "s95", "s96", "s97", "s98", "s99"
DEVI void chain_P_asm(const unsigned char* ws, const u16* wa, const u16* wb, int wave, int idx, int rp, int half) {
  asm volatile(
        "s_mov_b64 s[40:41], %[ws]\n\t"
        "s_mov_b32 s42, %[wsn]\n\t"
        "s_mov_b32 s43, 0x20000\n\t"
        "s_mov_b64 s[44:45], %[wa]\n\t"
        "s_mov_b32 s46, 8388608\n\t"
        "s_mov_b32 s47, 0x20000\n\t"
        "s_mov_b64 s[48:49], %[wb]\n\t"
        "s_mov_b32 s50, 8388608\n\t"
        "s_mov_b32 s51, 0x20000\n\t"
        "s_mov_b32 s52, %[wave]\n\t"
        "s_mov_b32 s53, %[idx]\n\t"
        "s_mov_b32 s54, %[rp]\n\t"
        "s_mov_b32 s56, %[bud]\n\t"
        "s_mov_b32 s57, 0\n\t"
        "s_mov_b32 s70, 0x3e0293ee\n\t"
        "s_mov_b32 s71, 0x7ff00000\n\t"
        "v_mbcnt_lo_u32_b32 v250, -1, 0\n\t"
        "v_mbcnt_hi_u32_b32 v250, -1, v250\n\t"
        "s_lshl_b32 s58, s52, 8\n\t"
        "s_add_i32 s58, s58, 159744\n\t"
        "v_lshl_add_u32 v250, v250, 2, s58\n\t"
        "ds_write_b32 v250, v199\n\t"
        "s_waitcnt lgkmcnt(0)\n\t"
        "s_lshl_b32 s65, s52, 10\n\t"
        "s_mul_i32 s58, s53, 24576\n\t"
        "s_add_i32 s66, s58, %[kx]\n\t"
        "s_lshl_b32 s58, s53, 12\n\t"
        "s_add_i32 s68, s58, %[km]\n\t"
        "s_lshr_b32 s58, s53, 3\n\t"
        "s_lshl_b32 s58, s58, 14\n\t"
        "s_and_b32 s59, s53, 7\n\t"
        "s_lshl_b32 s59, s59, 10\n\t"
        "s_add_i32 s69, s58, s59\n\t"
        "s_add_i32 s69, s69, 131072\n\t"
        "s_lshr_b32 s58, s53, 1\n\t"
        "s_lshl_b32 s58, s58, 14\n\t"
        "s_and_b32 s59, s53, 1\n\t"
        "s_lshl_b32 s59, s59, 12\n\t"
        "s_add_i32 s67, s58, s59\n\t"
        "s_add_i32 s67, s67, 655360\n\t"
        "s_add_i32 s67, s67, s54\n\t"
        "s_add_i32 s67, s67, s65\n\t"
        "v_mbcnt_lo_u32_b32 v252, -1, 0\n\t"
        "v_mbcnt_hi_u32_b32 v252, -1, v252\n\t"
        "s_and_b32 s62, s52, 3\n\t"
        "s_lshl_b32 s62, s62, 1\n\t"
        "s_lshr_b32 s58, s52, 1\n\t"
        "s_and_b32 s58, s58, 1\n\t"
        "s_lshl_b32 s58, s58, 2\n\t"
        "v_lshrrev_b32 v255, 4, v252\n\t"
        "v_or_b32 v255, s58, v255\n\t"
        "v_bfe_u32 v253, v252, 1, 3\n\t"
        "v_xor_b32 v253, v253, v255\n\t"
        "v_lshlrev_b32 v253, 5, v253\n\t"
        "v_and_b32 v255, 1, v252\n\t"
        "v_lshl_add_u32 v253, v255, 4, v253\n\t"
        "v_lshrrev_b32 v255, 4, v252\n\t"
        "v_lshl_add_u32 v253, v255, 13, v253\n\t"
        "s_lshl_b32 s58, s52, 15\n\t"
        "s_lshl_b32 s59, s53, 8\n\t"
        "s_add_i32 s58, s58, s59\n\t"
        "v_add_u32 v253, s58, v253\n\t"
        "v_bfe_u32 v254, v252, 2, 3\n\t"
        "v_xor_b32 v254, s62, v254\n\t"
        "v_lshlrev_b32 v254, 5, v254\n\t"
        "v_bfe_u32 v255, v252, 4, 2\n\t"
        "v_lshl_add_u32 v254, v255, 11, v254\n\t"
        "v_bfe_u32 v255, v252, 2, 2\n\t"
        "v_lshl_add_u32 v254, v255, 8, v254\n\t"
        "v_bfe_u32 v255, v252, 0, 2\n\t"
        "v_lshl_add_u32 v254, v255, 3, v254\n\t"
        "v_xor_b32 v251, 32, v254\n\t"
        "s_lshr_b32 s62, s52, 2\n\t"
        "s_lshl_b32 s62, s62, 14\n\t"
        "v_add_u32 v254, s62, v254\n\t"
        "v_add_u32 v251, s62, v251\n\t"
        "s_add_i32 m0, s65, 16384\n\t"
        "s_mov_b32 s64, 0\n\t"
        "buffer_load_dwordx4 v253, s[44:47], s64 offen lds\n\t"
        "s_add_i32 m0, s65, 24576\n\t"
        "s_mov_b32 s64, 262144\n\t"
        "buffer_load_dwordx4 v253, s[44:47], s64 offen lds\n\t"
        "s_add_i32 m0, s65, 32768\n\t"
        "s_mov_b32 s64, 0\n\t"
        "buffer_load_dwordx4 v253, s[48:51], s64 offen lds\n\t"
        "s_add_i32 m0, s65, 40960\n\t"
        "s_mov_b32 s64, 262144\n\t"
        "buffer_load_dwordx4 v253, s[48:51], s64 offen lds\n\t"
        "s_add_i32 m0, s65, 49152\n\t"
        "s_mov_b32 s64, 524288\n\t"
        "buffer_load_dwordx4 v253, s[44:47], s64 offen lds\n\t"
        "s_add_i32 m0, s65, 57344\n\t"
        "s_mov_b32 s64, 786432\n\t"
        "buffer_load_dwordx4 v253, s[44:47], s64 offen lds\n\t"
        "s_add_i32 m0, s65, 65536\n\t"
        "s_mov_b32 s64, 524288\n\t"
        "buffer_load_dwordx4 v253, s[48:51], s64 offen lds\n\t"
        "s_add_i32 m0, s65, 73728\n\t"
        "s_mov_b32 s64, 786432\n\t"
        "buffer_load_dwordx4 v253, s[48:51], s64 offen lds\n\t"
        "s_add_i32 m0, s65, 81920\n\t"
        "s_mov_b32 s64, 1048576\n\t"
        "buffer_load_dwordx4 v253, s[44:47], s64 offen lds\n\t"
        "s_add_i32 m0, s65, 90112\n\t"
        "s_mov_b32 s64, 1310720\n\t"
        "buffer_load_dwordx4 v253, s[44:47], s64 offen lds\n\t"
        "s_add_i32 m0, s65, 98304\n\t"
        "s_mov_b32 s64, 1048576\n\t"
        "buffer_load_dwordx4 v253, s[48:51], s64 offen lds\n\t"
        "s_add_i32 m0, s65, 106496\n\t"
        "s_mov_b32 s64, 1310720\n\t"
        "buffer_load_dwordx4 v253, s[48:51], s64 offen lds\n\t"
        "s_add_i32 m0, s65, 114688\n\t"
        "s_mov_b32 s64, 1572864\n\t"
        "buffer_load_dwordx4 v253, s[44:47], s64 offen lds\n\t"
        "s_add_i32 m0, s65, 122880\n\t"
        "s_mov_b32 s64, 1835008\n\t"
        "buffer_load_dwordx4 v253, s[44:47], s64 offen lds\n\t"
        "s_add_i32 m0, s65, 131072\n\t"
        "s_mov_b32 s64, 1572864\n\t"
        "buffer_load_dwordx4 v253, s[48:51], s64 offen lds\n\t"
        "s_add_i32 m0, s65, 139264\n\t"
        "s_mov_b32 s64, 1835008\n\t"
        "buffer_load_dwordx4 v253, s[48:51], s64 offen lds\n\t"
        "s_waitcnt vmcnt(12)\n\t"
        "s_barrier\n\t"
        "v_add_u32 v255, 16384, v254\n\t"
        "ds_read_b64_tr_b16 v[0:1], v255 offset:0\n\t"
        "ds_read_b64_tr_b16 v[2:3], v255 offset:1024\n\t"
        "ds_read_b64_tr_b16 v[4:5], v255 offset:8192\n\t"
        "ds_read_b64_tr_b16 v[6:7], v255 offset:9216\n\t"
        "s_waitcnt lgkmcnt(0)\n\t"
        "v_add_u32 v255, 16384, v251\n\t"
        "ds_read_b64_tr_b16 v[128:129], v255 offset:0\n\t"
        "ds_read_b64_tr_b16 v[130:131], v255 offset:1024\n\t"
        "ds_read_b64_tr_b16 v[132:133], v255 offset:8192\n\t"
        "ds_read_b64_tr_b16 v[134:135], v255 offset:9216\n\t"
        "s_waitcnt lgkmcnt(0)\n\t"
        "s_barrier\n\t"
        "s_add_i32 m0, s65, 16384\n\t"
        "s_mov_b32 s64, 2097152\n\t"
        "buffer_load_dwordx4 v253, s[44:47], s64 offen lds\n\t"
        "s_add_i32 m0, s65, 24576\n\t"
        "s_mov_b32 s64, 2359296\n\t"
        "buffer_load_dwordx4 v253, s[44:47], s64 offen lds\n\t"
        "s_add_i32 m0, s65, 32768\n\t"
        "s_mov_b32 s64, 2097152\n\t"
        "buffer_load_dwordx4 v253, s[48:51], s64 offen lds\n\t"
        "s_add_i32 m0, s65, 40960\n\t"
        "s_mov_b32 s64, 2359296\n\t"
        "buffer_load_dwordx4 v253, s[48:51], s64 offen lds\n\t"
        "s_waitcnt vmcnt(12)\n\t"
        "s_barrier\n\t"
        "v_add_u32 v255, 49152, v254\n\t"
        "ds_read_b64_tr_b16 v[8:9], v255 offset:0\n\t"
        "ds_read_b64_tr_b16 v[10:11], v255 offset:1024\n\t"
        "ds_read_b64_tr_b16 v[12:13], v255 offset:8192\n\t"
        "ds_read_b64_tr_b16 v[14:15], v255 offset:9216\n\t"
        "s_waitcnt lgkmcnt(0)\n\t"
        "v_add_u32 v255, 49152, v251\n\t"
        "ds_read_b64_tr_b16 v[192:193], v255 offset:0\n\t"
        "ds_read_b64_tr_b16 v[194:195], v255 offset:1024\n\t"
        "ds_read_b64_tr_b16 v[196:197], v255 offset:8192\n\t"
        "ds_read_b64_tr_b16 v[198:199], v255 offset:9216\n\t"
        "s_waitcnt lgkmcnt(0)\n\t"
        "s_barrier\n\t"
        "s_add_i32 m0, s65, 49152\n\t"
        "s_mov_b32 s64, 2621440\n\t"
        "buffer_load_dwordx4 v253, s[44:47], s64 offen lds\n\t"
        "s_add_i32 m0, s65, 57344\n\t"
        "s_mov_b32 s64, 2883584\n\t"
        "buffer_load_dwordx4 v253, s[44:47], s64 offen lds\n\t"
        "s_add_i32 m0, s65, 65536\n\t"
        "s_mov_b32 s64, 2621440\n\t"
        "buffer_load_dwordx4 v253, s[48:51], s64 offen lds\n\t"
        "s_add_i32 m0, s65, 73728\n\t"
        "s_mov_b32 s64, 2883584\n\t"
        "buffer_load_dwordx4 v253, s[48:51], s64 offen lds\n\t"
        "s_waitcnt vmcnt(12)\n\t"
        "s_barrier\n\t"
        "v_add_u32 v255, 81920, v254\n\t"
        "ds_read_b64_tr_b16 v[16:17], v255 offset:0\n\t"
        "ds_read_b64_tr_b16 v[18:19], v255 offset:1024\n\t"
        "ds_read_b64_tr_b16 v[20:21], v255 offset:8192\n\t"
        "ds_read_b64_tr_b16 v[22:23], v255 offset:9216\n\t"
        "s_waitcnt lgkmcnt(0)\n\t"
        "v_add_u32 v255, 81920, v251\n\t"
        "ds_read_b64_tr_b16 v[136:137], v255 offset:0\n\t"
        "ds_read_b64_tr_b16 v[138:139], v255 offset:1024\n\t"
        "ds_read_b64_tr_b16 v[140:141], v255 offset:8192\n\t"
        "ds_read_b64_tr_b16 v[142:143], v255 offset:9216\n\t"
        "s_waitcnt lgkmcnt(0)\n\t"
        "s_barrier\n\t"
        "s_add_i32 m0, s65, 81920\n\t"
        "s_mov_b32 s64, 3145728\n\t"
        "buffer_load_dwordx4 v253, s[44:47], s64 offen lds\n\t"
        "s_add_i32 m0, s65, 90112\n\t"
        "s_mov_b32 s64, 3407872\n\t"
        "buffer_load_dwordx4 v253, s[44:47], s64 offen lds\n\t"
        "s_add_i32 m0, s65, 98304\n\t"
        "s_mov_b32 s64, 3145728\n\t"
        "buffer_load_dwordx4 v253, s[48:51], s64 offen lds\n\t"
        "s_add_i32 m0, s65, 106496\n\t"
        "s_mov_b32 s64, 3407872\n\t"
        "buffer_load_dwordx4 v253, s[48:51], s64 offen lds\n\t"
        "s_waitcnt vmcnt(12)\n\t"
        "s_barrier\n\t"
        "v_add_u32 v255, 114688, v254\n\t"
        "ds_read_b64_tr_b16 v[24:25], v255 offset:0\n\t"
        "ds_read_b64_tr_b16 v[26:27], v255 offset:1024\n\t"
        "ds_read_b64_tr_b16 v[28:29], v255 offset:8192\n\t"
        "ds_read_b64_tr_b16 v[30:31], v255 offset:9216\n\t"
        "s_waitcnt lgkmcnt(0)\n\t"
        "v_add_u32 v255, 114688, v251\n\t"
        "ds_read_b64_tr_b16 v[200:201], v255 offset:0\n\t"
        "ds_read_b64_tr_b16 v[202:203], v255 offset:1024\n\t"
        "ds_read_b64_tr_b16 v[204:205], v255 offset:8192\n\t"
        "ds_read_b64_tr_b16 v[206:207], v255 offset:9216\n\t"
        "s_waitcnt lgkmcnt(0)\n\t"
        "s_barrier\n\t"
        "s_add_i32 m0, s65, 114688\n\t"
        "s_mov_b32 s64, 3670016\n\t"
        "buffer_load_dwordx4 v253, s[44:47], s64 offen lds\n\t"
        "s_add_i32 m0, s65, 122880\n\t"
        "s_mov_b32 s64, 3932160\n\t"
        "buffer_load_dwordx4 v253, s[44:47], s64 offen lds\n\t"
        "s_add_i32 m0, s65, 131072\n\t"
        "s_mov_b32 s64, 3670016\n\t"
        "buffer_load_dwordx4 v253, s[48:51], s64 offen lds\n\t"
        "s_add_i32 m0, s65, 139264\n\t"
        "s_mov_b32 s64, 3932160\n\t"
        "buffer_load_dwordx4 v253, s[48:51], s64 offen lds\n\t"
        "s_waitcnt vmcnt(12)\n\t"
        "s_barrier\n\t"
        "v_add_u32 v255, 16384, v254\n\t"
        "ds_read_b64_tr_b16 v[32:33], v255 offset:0\n\t"
        "ds_read_b64_tr_b16 v[34:35], v255 offset:1024\n\t"
        "ds_read_b64_tr_b16 v[36:37], v255 offset:8192\n\t"
        "ds_read_b64_tr_b16 v[38:39], v255 offset:9216\n\t"
        "s_waitcnt lgkmcnt(0)\n\t"
        "v_add_u32 v255, 16384, v251\n\t"
        "ds_read_b64_tr_b16 v[144:145], v255 offset:0\n\t"
        "ds_read_b64_tr_b16 v[146:147], v255 offset:1024\n\t"
        "ds_read_b64_tr_b16 v[148:149], v255 offset:8192\n\t"
        "ds_read_b64_tr_b16 v[150:151], v255 offset:9216\n\t"
        "s_waitcnt lgkmcnt(0)\n\t"
        "s_barrier\n\t"
        "s_add_i32 m0, s65, 16384\n\t"
        "s_mov_b32 s64, 4194304\n\t"
        "buffer_load_dwordx4 v253, s[44:47], s64 offen lds\n\t"
        "s_add_i32 m0, s65, 24576\n\t"
        "s_mov_b32 s64, 4456448\n\t"
        "buffer_load_dwordx4 v253, s[44:47], s64 offen lds\n\t"
        "s_add_i32 m0, s65, 32768\n\t"
        "s_mov_b32 s64, 4194304\n\t"
        "buffer_load_dwordx4 v253, s[48:51], s64 offen lds\n\t"
        "s_add_i32 m0, s65, 40960\n\t"
        "s_mov_b32 s64, 4456448\n\t"
        "buffer_load_dwordx4 v253, s[48:51], s64 offen lds\n\t"
        "s_waitcnt vmcnt(12)\n\t"
        "s_barrier\n\t"
        "v_add_u32 v255, 49152, v254\n\t"
        "ds_read_b64_tr_b16 v[40:41], v255 offset:0\n\t"
        "ds_read_b64_tr_b16 v[42:43], v255 offset:1024\n\t"
        "ds_read_b64_tr_b16 v[44:45], v255 offset:8192\n\t"
        "ds_read_b64_tr_b16 v[46:47], v255 offset:9216\n\t"
        "s_waitcnt lgkmcnt(0)\n\t"
        "v_add_u32 v255, 49152, v251\n\t"
        "ds_read_b64_tr_b16 v[208:209], v255 offset:0\n\t"
        "ds_read_b64_tr_b16 v[210:211], v255 offset:1024\n\t"
        "ds_read_b64_tr_b16 v[212:213], v255 offset:8192\n\t"
        "ds_read_b64_tr_b16 v[214:215], v255 offset:9216\n\t"
        "s_waitcnt lgkmcnt(0)\n\t"
        "s_barrier\n\t"
        "s_add_i32 m0, s65, 49152\n\t"
        "s_mov_b32 s64, 4718592\n\t"
        "buffer_load_dwordx4 v253, s[44:47], s64 offen lds\n\t"
        "s_add_i32 m0, s65, 57344\n\t"
        "s_mov_b32 s64, 4980736\n\t"
        "buffer_load_dwordx4 v253, s[44:47], s64 offen lds\n\t"
        "s_add_i32 m0, s65, 65536\n\t"
        "s_mov_b32 s64, 4718592\n\t"
        "buffer_load_dwordx4 v253, s[48:51], s64 offen lds\n\t"
        "s_add_i32 m0, s65, 73728\n\t"
        "s_mov_b32 s64, 4980736\n\t"
        "buffer_load_dwordx4 v253, s[48:51], s64 offen lds\n\t"
        "s_waitcnt vmcnt(12)\n\t"
        "s_barrier\n\t"
        "v_add_u32 v255, 81920, v254\n\t"
        "ds_read_b64_tr_b16 v[48:49], v255 offset:0\n\t"
        "ds_read_b64_tr_b16 v[50:51], v255 offset:1024\n\t"
        "ds_read_b64_tr_b16 v[52:53], v255 offset:8192\n\t"
        "ds_read_b64_tr_b16 v[54:55], v255 offset:9216\n\t"
        "s_waitcnt lgkmcnt(0)\n\t"
        "v_add_u32 v255, 81920, v251\n\t"
        "ds_read_b64_tr_b16 v[152:153], v255 offset:0\n\t"
        "ds_read_b64_tr_b16 v[154:155], v255 offset:1024\n\t"
        "ds_read_b64_tr_b16 v[156:157], v255 offset:8192\n\t"
        "ds_read_b64_tr_b16 v[158:159], v255 offset:9216\n\t"
        "s_waitcnt lgkmcnt(0)\n\t"
        "s_barrier\n\t"
        "s_add_i32 m0, s65, 81920\n\t"
        "s_mov_b32 s64, 5242880\n\t"
        "buffer_load_dwordx4 v253, s[44:47], s64 offen lds\n\t"
        "s_add_i32 m0, s65, 90112\n\t"
        "s_mov_b32 s64, 5505024\n\t"
        "buffer_load_dwordx4 v253, s[44:47], s64 offen lds\n\t"
        "s_add_i32 m0, s65, 98304\n\t"
        "s_mov_b32 s64, 5242880\n\t"
        "buffer_load_dwordx4 v253, s[48:51], s64 offen lds\n\t"
        "s_add_i32 m0, s65, 106496\n\t"
        "s_mov_b32 s64, 5505024\n\t"
        "buffer_load_dwordx4 v253, s[48:51], s64 offen lds\n\t"
        "s_waitcnt vmcnt(12)\n\t"
        "s_barrier\n\t"
        "v_add_u32 v255, 114688, v254\n\t"
        "ds_read_b64_tr_b16 v[56:57], v255 offset:0\n\t"
        "ds_read_b64_tr_b16 v[58:59], v255 offset:1024\n\t"
        "ds_read_b64_tr_b16 v[60:61], v255 offset:8192\n\t"
        "ds_read_b64_tr_b16 v[62:63], v255 offset:9216\n\t"
        "s_waitcnt lgkmcnt(0)\n\t"
        "v_add_u32 v255, 114688, v251\n\t"
        "ds_read_b64_tr_b16 v[216:217], v255 offset:0\n\t"
        "ds_read_b64_tr_b16 v[218:219], v255 offset:1024\n\t"
        "ds_read_b64_tr_b16 v[220:221], v255 offset:8192\n\t"
        "ds_read_b64_tr_b16 v[222:223], v255 offset:9216\n\t"
        "s_waitcnt lgkmcnt(0)\n\t"
        "s_barrier\n\t"
        "s_add_i32 m0, s65, 114688\n\t"
        "s_mov_b32 s64, 5767168\n\t"
        "buffer_load_dwordx4 v253, s[44:47], s64 offen lds\n\t"
        "s_add_i32 m0, s65, 122880\n\t"
        "s_mov_b32 s64, 6029312\n\t"
        "buffer_load_dwordx4 v253, s[44:47], s64 offen lds\n\t"
        "s_add_i32 m0, s65, 131072\n\t"
        "s_mov_b32 s64, 5767168\n\t"
        "buffer_load_dwordx4 v253, s[48:51], s64 offen lds\n\t"
        "s_add_i32 m0, s65, 139264\n\t"
        "s_mov_b32 s64, 6029312\n\t"
        "buffer_load_dwordx4 v253, s[48:51], s64 offen lds\n\t"
        "s_waitcnt vmcnt(12)\n\t"
        "s_barrier\n\t"
        "v_add_u32 v255, 16384, v254\n\t"
        "ds_read_b64_tr_b16 v[64:65], v255 offset:0\n\t"
        "ds_read_b64_tr_b16 v[66:67], v255 offset:1024\n\t"
        "ds_read_b64_tr_b16 v[68:69], v255 offset:8192\n\t"
        "ds_read_b64_tr_b16 v[70:71], v255 offset:9216\n\t"
        "s_waitcnt lgkmcnt(0)\n\t"
        "v_add_u32 v255, 16384, v251\n\t"
        "ds_read_b64_tr_b16 v[160:161], v255 offset:0\n\t"
        "ds_read_b64_tr_b16 v[162:163], v255 offset:1024\n\t"
        "ds_read_b64_tr_b16 v[164:165], v255 offset:8192\n\t"
        "ds_read_b64_tr_b16 v[166:167], v255 offset:9216\n\t"
        "s_waitcnt lgkmcnt(0)\n\t"
        "s_barrier\n\t"
        "s_add_i32 m0, s65, 16384\n\t"
        "s_mov_b32 s64, 6291456\n\t"
        "buffer_load_dwordx4 v253, s[44:47], s64 offen lds\n\t"
        "s_add_i32 m0, s65, 24576\n\t"
        "s_mov_b32 s64, 6553600\n\t"
        "buffer_load_dwordx4 v253, s[44:47], s64 offen lds\n\t"
        "s_add_i32 m0, s65, 32768\n\t"
        "s_mov_b32 s64, 6291456\n\t"
        "buffer_load_dwordx4 v253, s[48:51], s64 offen lds\n\t"
        "s_add_i32 m0, s65, 40960\n\t"
        "s_mov_b32 s64, 6553600\n\t"
        "buffer_load_dwordx4 v253, s[48:51], s64 offen lds\n\t"
        "s_waitcnt vmcnt(12)\n\t"
        "s_barrier\n\t"
        "v_add_u32 v255, 49152, v254\n\t"
        "ds_read_b64_tr_b16 v[72:73], v255 offset:0\n\t"
        "ds_read_b64_tr_b16 v[74:75], v255 offset:1024\n\t"
        "ds_read_b64_tr_b16 v[76:77], v255 offset:8192\n\t"
        "ds_read_b64_tr_b16 v[78:79], v255 offset:9216\n\t"
        "s_waitcnt lgkmcnt(0)\n\t"
        "v_add_u32 v255, 49152, v251\n\t"
        "ds_read_b64_tr_b16 v[224:225], v255 offset:0\n\t"
        "ds_read_b64_tr_b16 v[226:227], v255 offset:1024\n\t"
        "ds_read_b64_tr_b16 v[228:229], v255 offset:8192\n\t"
        "ds_read_b64_tr_b16 v[230:231], v255 offset:9216\n\t"
        "s_waitcnt lgkmcnt(0)\n\t"
        "s_barrier\n\t"
        "s_add_i32 m0, s65, 49152\n\t"
        "s_mov_b32 s64, 6815744\n\t"
        "buffer_load_dwordx4 v253, s[44:47], s64 offen lds\n\t"
        "s_add_i32 m0, s65, 57344\n\t"
        "s_mov_b32 s64, 7077888\n\t"
        "buffer_load_dwordx4 v253, s[44:47], s64 offen lds\n\t"
        "s_add_i32 m0, s65, 65536\n\t"
        "s_mov_b32 s64, 6815744\n\t"
        "buffer_load_dwordx4 v253, s[48:51], s64 offen lds\n\t"
        "s_add_i32 m0, s65, 73728\n\t"
        "s_mov_b32 s64, 7077888\n\t"
        "buffer_load_dwordx4 v253, s[48:51], s64 offen lds\n\t"
        "s_waitcnt vmcnt(12)\n\t"
        "s_barrier\n\t"
        "v_add_u32 v255, 81920, v254\n\t"
        "ds_read_b64_tr_b16 v[80:81], v255 offset:0\n\t"
        "ds_read_b64_tr_b16 v[82:83], v255 offset:1024\n\t"
        "ds_read_b64_tr_b16 v[84:85], v255 offset:8192\n\t"
        "ds_read_b64_tr_b16 v[86:87], v255 offset:9216\n\t"
        "s_waitcnt lgkmcnt(0)\n\t"
        "v_add_u32 v255, 81920, v251\n\t"
        "ds_read_b64_tr_b16 v[168:169], v255 offset:0\n\t"
        "ds_read_b64_tr_b16 v[170:171], v255 offset:1024\n\t"
        "ds_read_b64_tr_b16 v[172:173], v255 offset:8192\n\t"
        "ds_read_b64_tr_b16 v[174:175], v255 offset:9216\n\t"
        "s_waitcnt lgkmcnt(0)\n\t"
        "s_barrier\n\t"
        "s_add_i32 m0, s65, 81920\n\t"
        "s_mov_b32 s64, 7340032\n\t"
        "buffer_load_dwordx4 v253, s[44:47], s64 offen lds\n\t"
        "s_add_i32 m0, s65, 90112\n\t"
        "s_mov_b32 s64, 7602176\n\t"
        "buffer_load_dwordx4 v253, s[44:47], s64 offen lds\n\t"
        "s_add_i32 m0, s65, 98304\n\t"
        "s_mov_b32 s64, 7340032\n\t"
        "buffer_load_dwordx4 v253, s[48:51], s64 offen lds\n\t"
        "s_add_i32 m0, s65, 106496\n\t"
        "s_mov_b32 s64, 7602176\n\t"
        "buffer_load_dwordx4 v253, s[48:51], s64 offen lds\n\t"
        "s_waitcnt vmcnt(12)\n\t"
        "s_barrier\n\t"
        "v_add_u32 v255, 114688, v254\n\t"
        "ds_read_b64_tr_b16 v[88:89], v255 offset:0\n\t"
        "ds_read_b64_tr_b16 v[90:91], v255 offset:1024\n\t"
        "ds_read_b64_tr_b16 v[92:93], v255 offset:8192\n\t"
        "ds_read_b64_tr_b16 v[94:95], v255 offset:9216\n\t"
        "s_waitcnt lgkmcnt(0)\n\t"
        "v_add_u32 v255, 114688, v251\n\t"
        "ds_read_b64_tr_b16 v[232:233], v255 offset:0\n\t"
        "ds_read_b64_tr_b16 v[234:235], v255 offset:1024\n\t"
        "ds_read_b64_tr_b16 v[236:237], v255 offset:8192\n\t"
        "ds_read_b64_tr_b16 v[238:239], v255 offset:9216\n\t"
        "s_waitcnt lgkmcnt(0)\n\t"
        "s_barrier\n\t"
        "s_add_i32 m0, s65, 114688\n\t"
        "s_mov_b32 s64, 7864320\n\t"
        "buffer_load_dwordx4 v253, s[44:47], s64 offen lds\n\t"
        "s_add_i32 m0, s65, 122880\n\t"
        "s_mov_b32 s64, 8126464\n\t"
        "buffer_load_dwordx4 v253, s[44:47], s64 offen lds\n\t"
        "s_add_i32 m0, s65, 131072\n\t"
        "s_mov_b32 s64, 7864320\n\t"
        "buffer_load_dwordx4 v253, s[48:51], s64 offen lds\n\t"
        "s_add_i32 m0, s65, 139264\n\t"
        "s_mov_b32 s64, 8126464\n\t"
        "buffer_load_dwordx4 v253, s[48:51], s64 offen lds\n\t"
        "s_waitcnt vmcnt(12)\n\t"
        "s_barrier\n\t"
        "v_add_u32 v255, 16384, v254\n\t"
        "ds_read_b64_tr_b16 v[96:97], v255 offset:0\n\t"
        "ds_read_b64_tr_b16 v[98:99], v255 offset:1024\n\t"
        "ds_read_b64_tr_b16 v[100:101], v255 offset:8192\n\t"
        "ds_read_b64_tr_b16 v[102:103], v255 offset:9216\n\t"
        "s_waitcnt lgkmcnt(0)\n\t"
        "v_add_u32 v255, 16384, v251\n\t"
        "ds_read_b64_tr_b16 v[176:177], v255 offset:0\n\t"
        "ds_read_b64_tr_b16 v[178:179], v255 offset:1024\n\t"
        "ds_read_b64_tr_b16 v[180:181], v255 offset:8192\n\t"
        "ds_read_b64_tr_b16 v[182:183], v255 offset:9216\n\t"
        "s_waitcnt lgkmcnt(0)\n\t"
        "s_barrier\n\t"
        "s_waitcnt vmcnt(8)\n\t"
        "s_barrier\n\t"
        "v_add_u32 v255, 49152, v254\n\t"
        "ds_read_b64_tr_b16 v[104:105], v255 offset:0\n\t"
        "ds_read_b64_tr_b16 v[106:107], v255 offset:1024\n\t"
        "ds_read_b64_tr_b16 v[108:109], v255 offset:8192\n\t"
        "ds_read_b64_tr_b16 v[110:111], v255 offset:9216\n\t"
        "s_waitcnt lgkmcnt(0)\n\t"
        "v_add_u32 v255, 49152, v251\n\t"
        "ds_read_b64_tr_b16 v[240:241], v255 offset:0\n\t"
        "ds_read_b64_tr_b16 v[242:243], v255 offset:1024\n\t"
        "ds_read_b64_tr_b16 v[244:245], v255 offset:8192\n\t"
        "ds_read_b64_tr_b16 v[246:247], v255 offset:9216\n\t"
        "s_waitcnt lgkmcnt(0)\n\t"
        "s_barrier\n\t"
        "s_waitcnt vmcnt(4)\n\t"
        "s_barrier\n\t"
        "v_add_u32 v255, 81920, v254\n\t"
        "ds_read_b64_tr_b16 v[112:113], v255 offset:0\n\t"
        "ds_read_b64_tr_b16 v[114:115], v255 offset:1024\n\t"
        "ds_read_b64_tr_b16 v[116:117], v255 offset:8192\n\t"
        "ds_read_b64_tr_b16 v[118:119], v255 offset:9216\n\t"
        "s_waitcnt lgkmcnt(0)\n\t"
        "v_add_u32 v255, 81920, v251\n\t"
        "ds_read_b64_tr_b16 v[184:185], v255 offset:0\n\t"
        "ds_read_b64_tr_b16 v[186:187], v255 offset:1024\n\t"
        "ds_read_b64_tr_b16 v[188:189], v255 offset:8192\n\t"
        "ds_read_b64_tr_b16 v[190:191], v255 offset:9216\n\t"
        "s_waitcnt lgkmcnt(0)\n\t"
        "s_barrier\n\t"
        "s_waitcnt vmcnt(0)\n\t"
        "s_barrier\n\t"
        "v_add_u32 v255, 114688, v254\n\t"
        "ds_read_b64_tr_b16 v[120:121], v255 offset:0\n\t"
        "ds_read_b64_tr_b16 v[122:123], v255 offset:1024\n\t"
        "ds_read_b64_tr_b16 v[124:125], v255 offset:8192\n\t"
        "ds_read_b64_tr_b16 v[126:127], v255 offset:9216\n\t"
        "s_waitcnt lgkmcnt(0)\n\t"
        "s_mul_i32 s58, s52, 14336\n\t"
        "s_add_i32 s72, s58, 45056\n\t"
        "s_lshl_b32 s58, s52, 11\n\t"
        "v_lshl_add_u32 v253, v252, 4, s58\n\t"
        "ds_write_b128 v253, v[192:195]\n\t"
        "ds_write_b128 v253, v[196:199] offset:1024\n\t"
        "s_waitcnt lgkmcnt(0)\n\t"
        "v_add_u32 v255, 114688, v251\n\t"
        "ds_read_b64_tr_b16 v[192:193], v255 offset:0\n\t"
        "ds_read_b64_tr_b16 v[194:195], v255 offset:1024\n\t"
        "ds_read_b64_tr_b16 v[196:197], v255 offset:8192\n\t"
        "ds_read_b64_tr_b16 v[198:199], v255 offset:9216\n\t"
        "s_waitcnt lgkmcnt(0)\n\t"
        "s_barrier\n\t"
        "v_lshl_add_u32 v255, v252, 4, s72\n\t"
        "ds_write_b128 v255, v[200:203] offset:2048\n\t"
        "ds_write_b128 v255, v[204:207] offset:3072\n\t"
        "ds_write_b128 v255, v[208:211] offset:4096\n\t"
        "ds_write_b128 v255, v[212:215] offset:5120\n\t"
        "ds_write_b128 v255, v[216:219] offset:6144\n\t"
        "ds_write_b128 v255, v[220:223] offset:7168\n\t"
        "ds_write_b128 v255, v[224:227] offset:8192\n\t"
        "ds_write_b128 v255, v[228:231] offset:9216\n\t"
        "ds_write_b128 v255, v[232:235] offset:10240\n\t"
        "ds_write_b128 v255, v[236:239] offset:11264\n\t"
        "ds_write_b128 v255, v[240:243] offset:12288\n\t"
        "ds_write_b128 v255, v[244:247] offset:13312\n\t"
        "s_waitcnt lgkmcnt(0)\n\t"
        "ds_read_b128 v[200:203], v253\n\t"
        "ds_read_b128 v[204:207], v253 offset:1024\n\t"
        "s_waitcnt lgkmcnt(0)\n\t"
        "ds_write_b128 v255, v[200:203]\n\t"
        "ds_write_b128 v255, v[204:207] offset:1024\n\t"
        "s_waitcnt lgkmcnt(0)\n\t"
        "s_barrier\n\t"
        "s_lshl_b32 s95, s53, 7\n\t"
        "s_add_i32 s95, s95, s54\n\t"
        "s_add_i32 s95, s95, 1703936\n\t"
        "s_mov_b32 s94, 70\n\t"
        "vt_%=:\n\t"
        "s_mov_b32 s58, %[vote]\n\t"
        "buffer_load_dword v200, off, s[40:43], s58 sc1\n\t"
        "s_waitcnt vmcnt(0)\n\t"
        "v_readfirstlane_b32 s58, v200\n\t"
        "s_nop 3\n\t"
        "s_and_b32 s59, s58, 0xffff\n\t"
        "s_cmp_ge_u32 s59, 96\n\t"
        "s_cbranch_scc1 vk_%=\n\t"
        "s_sleep 4\n\t"
        "s_sub_i32 s56, s56, 1\n\t"
        "s_cmp_gt_i32 s56, 0\n\t"
        "s_cbranch_scc1 vt_%=\n\t"
        "vk_%=:\n\t"
        "s_lshr_b32 s58, s58, 16\n\t"
        "s_cmp_gt_u32 s58, 48\n\t"
        "s_cselect_b32 s58, 8192, 0\n\t"
        "s_add_i32 s54, s54, s58\n\t"
        "s_add_i32 s67, s67, s58\n\t"
        "s_add_i32 s95, s95, s58\n\t"
        "s_cmp_ge_u32 s52, 4\n\t"
        "s_cbranch_scc1 PB_%=\n\t"
        "s_setprio 2\n\t"
        "s_and_b32 s58, s57, 3\n\t"
        "s_cmp_lg_u32 s58, 0\n\t"
        "s_cbranch_scc1 krS0_%=\n\t"
        "s_lshr_b32 s58, s57, 2\n\t"
        "s_mul_i32 s58, s58, 48\n\t"
        "s_lshr_b32 s59, s53, 1\n\t"
        "s_add_i32 s58, s58, s59\n\t"
        "s_add_i32 s58, s58, 32\n\t"
        "s_lshl_b32 s58, s58, 6\n\t"
        "s_add_i32 s58, s58, %[qf]\n\t"
        "kpS0_%=:\n\t"
        "buffer_load_dword v200, off, s[40:43], s58 sc1\n\t"
        "s_waitcnt vmcnt(0)\n\t"
        "v_cmp_eq_u32 vcc, 0, v200\n\t"
        "s_cbranch_vccz krS0_%=\n\t"
        "s_sleep 4\n\t"
        "s_sub_i32 s56, s56, 1\n\t"
        "s_cmp_gt_i32 s56, 0\n\t"
        "s_cbranch_scc1 kpS0_%=\n\t"
        "krS0_%=:\n\t"
        "v_mbcnt_lo_u32_b32 v200, -1, 0\n\t"
        "v_mbcnt_hi_u32_b32 v200, -1, v200\n\t"
        "v_lshlrev_b32 v200, 4, v200\n\t"
        "s_mul_i32 s58, s57, 786432\n\t"
        "s_add_i32 s58, s58, s66\n\t"
        "s_lshl_b32 s60, s52, 11\n\t"
        "s_add_i32 s58, s58, s60\n\t"
        "s_add_i32 s58, s58, 8192\n\t"
        "s_add_i32 m0, s60, 32768\n\t"
        "s_nop 0\n\t"
        "buffer_load_dwordx4 v200, s[40:43], s58 offen sc1 lds\n\t"
        "s_add_i32 m0, s60, 33792\n\t"
        "s_add_i32 s58, s58, 1024\n\t"
        "buffer_load_dwordx4 v200, s[40:43], s58 offen sc1 lds\n\t"
        "SL_%=:\n\t"
        "v_mbcnt_lo_u32_b32 v200, -1, 0\n\t"
        "v_mbcnt_hi_u32_b32 v200, -1, v200\n\t"
        "v_lshlrev_b32 v252, 4, v200\n\t"
        "v_add_u32 v248, s65, v252\n\t"
        "v_add_u32 v249, s72, v252\n\t"
        "ds_read_b128 v[212:215], v249 offset:0\n\t"
        "ds_read_b128 v[216:219], v249 offset:1024\n\t"
        "ds_read_b128 v[220:223], v249 offset:2048\n\t"
        "ds_read_b128 v[224:227], v249 offset:3072\n\t"
        "ds_read_b128 v[236:239], v249 offset:4096\n\t"
        "ds_read_b128 v[240:243], v249 offset:5120\n\t"
        "v_bfe_u32 v201, v200, 3, 1\n\t"
        "v_mul_u32_u24 v201, 0xaaaa, v201\n\t"
        "v_add_u32 v251, 0x4444, v201\n\t"
        "s_lshl_b32 s58, s52, 9\n\t"
        "s_add_i32 s58, s58, 40960\n\t"
        "v_bfe_u32 v201, v200, 5, 1\n\t"
        "v_lshl_add_u32 v250, v201, 8, s58\n\t"
        "v_bfe_u32 v201, v200, 0, 4\n\t"
        "v_lshl_add_u32 v250, v201, 4, v250\n\t"
        "v_bfe_u32 v201, v200, 4, 1\n\t"
        "v_lshl_add_u32 v250, v201, 3, v250\n\t"
        "s_mov_b32 s58, 40960\n\t"
        "v_bfe_u32 v201, v200, 4, 2\n\t"
        "v_lshl_add_u32 v253, v201, 7, s58\n\t"
        "v_bfe_u32 v201, v200, 2, 2\n\t"
        "v_lshl_add_u32 v253, v201, 4, v253\n\t"
        "v_bfe_u32 v201, v200, 0, 1\n\t"
        "v_lshl_add_u32 v253, v201, 3, v253\n\t"
        "v_bfe_u32 v201, v200, 5, 1\n\t"
        "v_lshl_add_u32 v254, v201, 9, s65\n\t"
        "v_bfe_u32 v201, v200, 3, 1\n\t"
        "v_lshl_add_u32 v254, v201, 8, v254\n\t"
        "v_bfe_u32 v201, v200, 2, 1\n\t"
        "v_lshl_add_u32 v254, v201, 7, v254\n\t"
        "v_bfe_u32 v201, v200, 4, 1\n\t"
        "v_lshl_add_u32 v254, v201, 6, v254\n\t"
        "v_bfe_u32 v201, v200, 0, 2\n\t"
        "v_lshl_add_u32 v254, v201, 4, v254\n\t"
        "s_and_b32 s58, s57, 1\n\t"
        "s_lshl_b32 s58, s58, 13\n\t"
        "v_add_u32 v255, s58, v252\n\t"
        "v_mov_b32 v228, 0\n\t"
        "v_mov_b32 v229, 0\n\t"
        "v_mov_b32 v230, 0\n\t"
        "v_mov_b32 v231, 0\n\t"
        "v_mov_b32 v232, 0\n\t"
        "v_mov_b32 v233, 0\n\t"
        "v_mov_b32 v234, 0\n\t"
        "v_mov_b32 v235, 0\n\t"
        "s_mov_b32 s73, 0\n\t"
        "s_cmp_eq_u32 s57, 0\n\t"
        "s_cbranch_scc1 Sns_%=\n\t"
        "s_sleep %[slp]\n\t"
        "Sns_%=:\n\t"
        "s_and_b32 s58, s57, 3\n\t"
        "s_lshl_b32 s58, s58, 15\n\t"
        "s_add_i32 s58, s58, s54\n\t"
        "s_add_i32 s59, s58, 16384\n\t"
        "Sg_%=:\n\t"
        "buffer_load_dwordx4 v[200:203], v248, s[40:43], s58 offen sc1\n\t"
        "buffer_load_dwordx4 v[204:207], v248, s[40:43], s59 offen sc1\n\t"
        "s_waitcnt vmcnt(1)\n\t"
        "v_max3_u32 v208, v200, v201, v202\n\t"
        "s_waitcnt vmcnt(0)\n\t"
        "v_max3_u32 v208, v208, v203, v204\n\t"
        "v_max3_u32 v208, v208, v205, v206\n\t"
        "v_max_u32 v208, v208, v207\n\t"
        "v_cmp_eq_u32 vcc, -1, v208\n\t"
        "s_cbranch_vccz Sgk_%=\n\t"
        "s_add_i32 s73, s73, 0x10000\n\t"
        "s_sub_i32 s56, s56, 1\n\t"
        "s_cmp_gt_i32 s56, 0\n\t"
        "s_cbranch_scc1 Sg_%=\n\t"
        "Sgk_%=:\n\t"
        "ds_write_b128 v254, v[200:203]\n\t"
        "ds_write_b128 v254, v[204:207] offset:8192\n\t"
        "s_cmp_eq_u32 s57, 63\n\t"
        "s_waitcnt lgkmcnt(0)\n\t"
        "s_barrier\n\t"
        "s_cbranch_scc1 LAST_%=\n\t"
        "ds_read_b128 v[200:203], v252 offset:0\n\t"
        "ds_read_b128 v[204:207], v252 offset:1024\n\t"
        "ds_read_b128 v[208:211], v252 offset:2048\n\t"
        "ds_read_b128 v[244:247], v252 offset:3072\n\t"
        "s_waitcnt lgkmcnt(3)\n\t"
        "v_smfmac_f32_16x16x64_bf16 v[228:231], v[200:203], v[0:7], v251\n\t"
        "v_smfmac_f32_16x16x64_bf16 v[232:235], v[200:203], v[128:135], v251\n\t"
        "ds_read_b128 v[200:203], v252 offset:4096\n\t"
        "s_waitcnt lgkmcnt(3)\n\t"
        "v_smfmac_f32_16x16x64_bf16 v[228:231], v[204:207], v[8:15], v251\n\t"
        "v_smfmac_f32_16x16x64_bf16 v[232:235], v[204:207], v[212:219], v251\n\t"
        "ds_read_b128 v[204:207], v252 offset:5120\n\t"
        "ds_read_b128 v[212:215], v249 offset:6144\n\t"
        "ds_read_b128 v[216:219], v249 offset:7168\n\t"
        "s_waitcnt lgkmcnt(5)\n\t"
        "v_smfmac_f32_16x16x64_bf16 v[228:231], v[208:211], v[16:23], v251\n\t"
        "v_smfmac_f32_16x16x64_bf16 v[232:235], v[208:211], v[136:143], v251\n\t"
        "ds_read_b128 v[208:211], v252 offset:6144\n\t"
        "s_lshl_b32 s60, s52, 11\n\t"
        "s_add_i32 s60, s60, 32768\n\t"
        "s_waitcnt lgkmcnt(5)\n\t"
        "v_smfmac_f32_16x16x64_bf16 v[228:231], v[244:247], v[24:31], v251\n\t"
        "v_smfmac_f32_16x16x64_bf16 v[232:235], v[244:247], v[220:227], v251\n\t"
        "ds_read_b128 v[244:247], v252 offset:7168\n\t"
        "ds_read_b128 v[220:223], v249 offset:8192\n\t"
        "ds_read_b128 v[224:227], v249 offset:9216\n\t"
        "s_waitcnt lgkmcnt(7)\n\t"
        "v_smfmac_f32_16x16x64_bf16 v[228:231], v[200:203], v[32:39], v251\n\t"
        "v_smfmac_f32_16x16x64_bf16 v[232:235], v[200:203], v[144:151], v251\n\t"
        "ds_read_b128 v[200:203], v252 offset:8192\n\t"
        "v_bfe_u32 v248, v252, 6, 4\n\t"
        "v_lshl_add_u32 v254, v248, 6, s60\n\t"
        "s_waitcnt lgkmcnt(7)\n\t"
        "v_smfmac_f32_16x16x64_bf16 v[228:231], v[204:207], v[40:47], v251\n\t"
        "v_smfmac_f32_16x16x64_bf16 v[232:235], v[204:207], v[236:243], v251\n\t"
        "ds_read_b128 v[204:207], v252 offset:9216\n\t"
        "ds_read_b128 v[236:239], v249 offset:10240\n\t"
        "ds_read_b128 v[240:243], v249 offset:11264\n\t"
        "s_waitcnt lgkmcnt(7)\n\t"
        "v_smfmac_f32_16x16x64_bf16 v[228:231], v[208:211], v[48:55], v251\n\t"
        "v_smfmac_f32_16x16x64_bf16 v[232:235], v[208:211], v[152:159], v251\n\t"
        "ds_read_b128 v[208:211], v252 offset:10240\n\t"
        "v_bfe_u32 v248, v252, 4, 2\n\t"
        "v_lshl_add_u32 v254, v248, 3, v254\n\t"
        "s_waitcnt lgkmcnt(7)\n\t"
        "v_smfmac_f32_16x16x64_bf16 v[228:231], v[244:247], v[56:63], v251\n\t"
        "v_smfmac_f32_16x16x64_bf16 v[232:235], v[244:247], v[212:219], v251\n\t"
        "ds_read_b128 v[244:247], v252 offset:11264\n\t"
        "ds_read_b128 v[212:215], v249 offset:12288\n\t"
        "ds_read_b128 v[216:219], v249 offset:13312\n\t"
        "s_waitcnt lgkmcnt(7)\n\t"
        "v_smfmac_f32_16x16x64_bf16 v[228:231], v[200:203], v[64:71], v251\n\t"
        "v_smfmac_f32_16x16x64_bf16 v[232:235], v[200:203], v[160:167], v251\n\t"
        "ds_read_b128 v[200:203], v252 offset:12288\n\t"
        "v_lshrrev_b32 v249, 1, v252\n\t"
        "s_lshl_b32 s60, s52, 8\n\t"
        "s_waitcnt lgkmcnt(7)\n\t"
        "v_smfmac_f32_16x16x64_bf16 v[228:231], v[204:207], v[72:79], v251\n\t"
        "v_smfmac_f32_16x16x64_bf16 v[232:235], v[204:207], v[220:227], v251\n\t"
        "ds_read_b128 v[204:207], v252 offset:13312\n\t"
        "s_waitcnt lgkmcnt(5)\n\t"
        "v_smfmac_f32_16x16x64_bf16 v[228:231], v[208:211], v[80:87], v251\n\t"
        "v_smfmac_f32_16x16x64_bf16 v[232:235], v[208:211], v[168:175], v251\n\t"
        "ds_read_b128 v[208:211], v252 offset:14336\n\t"
        "s_waitcnt lgkmcnt(5)\n\t"
        "v_smfmac_f32_16x16x64_bf16 v[228:231], v[244:247], v[88:95], v251\n\t"
        "v_smfmac_f32_16x16x64_bf16 v[232:235], v[244:247], v[236:243], v251\n\t"
        "ds_read_b128 v[244:247], v252 offset:15360\n\t"
        "s_waitcnt lgkmcnt(3)\n\t"
        "v_smfmac_f32_16x16x64_bf16 v[228:231], v[200:203], v[96:103], v251\n\t"
        "v_smfmac_f32_16x16x64_bf16 v[232:235], v[200:203], v[176:183], v251\n\t"
        "ds_read_b128 v[220:223], v255 offset:16384\n\t"
        "ds_read_b128 v[224:227], v255 offset:20480\n\t"
        "v_bfe_u32 v248, v249, 5, 1\n\t"
        "v_lshl_add_u32 v252, v248, 16, s60\n\t"
        "s_waitcnt lgkmcnt(4)\n\t"
        "v_smfmac_f32_16x16x64_bf16 v[228:231], v[204:207], v[104:111], v251\n\t"
        "v_smfmac_f32_16x16x64_bf16 v[232:235], v[204:207], v[212:219], v251\n\t"
        "ds_read_b128 v[236:239], v255 offset:17408\n\t"
        "ds_read_b128 v[240:243], v255 offset:21504\n\t"
        "v_bfe_u32 v248, v249, 7, 2\n\t"
        "v_lshl_add_u32 v252, v248, 6, v252\n\t"
        "s_waitcnt lgkmcnt(5)\n\t"
        "v_smfmac_f32_16x16x64_bf16 v[228:231], v[208:211], v[112:119], v251\n\t"
        "v_smfmac_f32_16x16x64_bf16 v[232:235], v[208:211], v[184:191], v251\n\t"
        "ds_read_b128 v[212:215], v255 offset:18432\n\t"
        "ds_read_b128 v[216:219], v255 offset:22528\n\t"
        "v_bfe_u32 v248, v249, 3, 2\n\t"
        "v_lshl_add_u32 v252, v248, 4, v252\n\t"
        "s_waitcnt lgkmcnt(6)\n\t"
        "v_smfmac_f32_16x16x64_bf16 v[228:231], v[244:247], v[120:127], v251\n\t"
        "v_smfmac_f32_16x16x64_bf16 v[232:235], v[244:247], v[192:199], v251\n\t"
        "ds_read_b128 v[200:203], v255 offset:19456\n\t"
        "ds_read_b128 v[204:207], v255 offset:23552\n\t"
        "v_bfe_u32 v248, v249, 6, 1\n\t"
        "v_lshl_add_u32 v252, v248, 30, v252\n\t"
        "s_and_b32 s58, s57, 3\n\t"
        "s_lshl_b32 s58, s58, 18\n\t"
        "s_add_i32 s58, s58, s67\n\t"
        "s_nop 0\n\t"
        "v_permlane32_swap_b32 v228, v232\n\t"
        "v_permlane32_swap_b32 v229, v233\n\t"
        "v_permlane32_swap_b32 v230, v234\n\t"
        "v_permlane32_swap_b32 v231, v235\n\t"
        "v_pk_add_f32 v[228:229], v[228:229], v[232:233]\n\t"
        "v_pk_add_f32 v[230:231], v[230:231], v[234:235]\n\t"
        "v_cvt_pk_bf16_f32 v232, v228, v229\n\t"
        "v_cvt_pk_bf16_f32 v233, v230, v231\n\t"
        "ds_write_b64 v250, v[232:233]\n\t"
        "s_memrealtime s[74:75]\n\t"
        "s_waitcnt lgkmcnt(0)\n\t"
        "s_barrier\n\t"
        "ds_read_b64_tr_b16 v[208:209], v253 offset:0\n\t"
        "ds_read_b64_tr_b16 v[210:211], v253 offset:64\n\t"
        "ds_read_b64_tr_b16 v[244:245], v253 offset:512\n\t"
        "ds_read_b64_tr_b16 v[246:247], v253 offset:576\n\t"
        "ds_read_b64_tr_b16 v[228:229], v253 offset:1024\n\t"
        "ds_read_b64_tr_b16 v[230:231], v253 offset:1088\n\t"
        "ds_read_b64_tr_b16 v[232:233], v253 offset:1536\n\t"
        "ds_read_b64_tr_b16 v[234:235], v253 offset:1600\n\t"
        "s_cmp_lg_u32 s52, 0\n\t"
        "s_cbranch_scc1 nf_%=\n\t"
        "s_add_i32 s62, s57, 1\n\t"
        "s_add_i32 s61, s74, s94\n\t"
        "v_mov_b32 v250, s62\n\t"
        "v_mov_b32 v251, s61\n\t"
        "buffer_store_dwordx2 v[250:251], off, s[40:43], s95 sc1\n\t"
        "nf_%=:\n\t"
        "s_waitcnt lgkmcnt(6)\n\t"
        "v_mfma_f32_16x16x32_bf16 v[220:223], v[220:223], v[208:211], 0\n\t"
        "v_mfma_f32_16x16x32_bf16 v[224:227], v[224:227], v[208:211], 0\n\t"
        "s_waitcnt lgkmcnt(4)\n\t"
        "v_mfma_f32_16x16x32_bf16 v[220:223], v[236:239], v[244:247], v[220:223]\n\t"
        "v_mfma_f32_16x16x32_bf16 v[224:227], v[240:243], v[244:247], v[224:227]\n\t"
        "ds_read_b64_tr_b16 v[236:237], v254\n\t"
        "ds_read_b64_tr_b16 v[238:239], v254 offset:1024\n\t"
        "ds_read_b64_tr_b16 v[240:241], v254 offset:32\n\t"
        "ds_read_b64_tr_b16 v[242:243], v254 offset:1056\n\t"
        "s_waitcnt lgkmcnt(6)\n\t"
        "v_mfma_f32_16x16x32_bf16 v[220:223], v[212:215], v[228:231], v[220:223]\n\t"
        "v_mfma_f32_16x16x32_bf16 v[224:227], v[216:219], v[228:231], v[224:227]\n\t"
        "s_waitcnt lgkmcnt(4)\n\t"
        "v_mfma_f32_16x16x32_bf16 v[220:223], v[200:203], v[232:235], v[220:223]\n\t"
        "v_mfma_f32_16x16x32_bf16 v[224:227], v[204:207], v[232:235], v[224:227]\n\t"
        "buffer_load_dwordx2 v[250:251], v249, s[40:43], s58 offen offset:512 sc1\n\t"
        "buffer_load_dwordx2 v[248:249], v249, s[40:43], s58 offen sc1\n\t"
        "s_and_b32 s60, s57, 3\n\t"
        "s_lshl_b32 s60, s60, 17\n\t"
        "s_add_i32 s60, s60, s69\n\t"
        "s_nop 0\n\t"
        "s_add_i32 s60, s60, s54\n\t"
        "v_max3_f32 v200, v220, v221, v222\n\t"
        "v_max3_f32 v200, v200, v223, v224\n\t"
        "v_max3_f32 v200, v200, v225, v226\n\t"
        "v_max_f32 v200, v200, v227\n\t"
        "v_mov_b32 v201, v200\n\t"
        "v_mov_b32 v212, 0x3f803f80\n\t"
        "v_mov_b32 v213, 0x3f803f80\n\t"
        "v_permlane16_swap_b32 v200, v201\n\t"
        "v_max_f32 v200, v200, v201\n\t"
        "v_mov_b32 v201, v200\n\t"
        "v_mov_b32 v214, 0x3f803f80\n\t"
        "v_mov_b32 v215, 0x3f803f80\n\t"
        "v_permlane32_swap_b32 v200, v201\n\t"
        "v_max_f32 v200, v200, v201\n\t"
        "v_mul_f32 v202, 0xbe0293ee, v200\n\t"
        "v_fma_f32 v220, v220, s70, v202\n\t"
        "v_fma_f32 v221, v221, s70, v202\n\t"
        "v_fma_f32 v222, v222, s70, v202\n\t"
        "v_fma_f32 v223, v223, s70, v202\n\t"
        "v_fma_f32 v224, v224, s70, v202\n\t"
        "v_fma_f32 v225, v225, s70, v202\n\t"
        "v_fma_f32 v226, v226, s70, v202\n\t"
        "v_fma_f32 v227, v227, s70, v202\n\t"
        "v_exp_f32 v220, v220\n\t"
        "v_exp_f32 v221, v221\n\t"
        "v_exp_f32 v222, v222\n\t"
        "v_exp_f32 v223, v223\n\t"
        "v_exp_f32 v224, v224\n\t"
        "v_exp_f32 v225, v225\n\t"
        "v_exp_f32 v226, v226\n\t"
        "v_exp_f32 v227, v227\n\t"
        "v_cvt_pk_bf16_f32 v204, v220, v221\n\t"
        "v_cvt_pk_bf16_f32 v205, v222, v223\n\t"
        "v_cvt_pk_bf16_f32 v206, v224, v225\n\t"
        "v_cvt_pk_bf16_f32 v207, v226, v227\n\t"
        "s_nop 0\n\t"
        "s_waitcnt lgkmcnt(0)\n\t"
        "v_mfma_f32_16x16x32_bf16 v[220:223], v[236:239], v[204:207], 0\n\t"
        "v_mfma_f32_16x16x32_bf16 v[224:227], v[240:243], v[204:207], 0\n\t"
        "v_mfma_f32_16x16x32_bf16 v[236:239], v[212:215], v[204:207], 0\n\t"
        "s_barrier\n\t"
        "ds_read_b64_tr_b16 v[240:241], v253 offset:2048\n\t"
        "ds_read_b64_tr_b16 v[242:243], v253 offset:2112\n\t"
        "ds_read_b64_tr_b16 v[204:205], v253 offset:2560\n\t"
        "ds_read_b64_tr_b16 v[206:207], v253 offset:2624\n\t"
        "ds_read_b64_tr_b16 v[216:217], v253 offset:3072\n\t"
        "ds_read_b64_tr_b16 v[218:219], v253 offset:3136\n\t"
        "ds_read_b64_tr_b16 v[212:213], v253 offset:3584\n\t"
        "ds_read_b64_tr_b16 v[214:215], v253 offset:3648\n\t"
        "s_waitcnt lgkmcnt(6)\n\t"
        "v_mfma_f32_16x16x32_bf16 v[240:243], v[240:243], v[208:211], 0\n\t"
        "s_waitcnt lgkmcnt(4)\n\t"
        "v_mfma_f32_16x16x32_bf16 v[240:243], v[204:207], v[244:247], v[240:243]\n\t"
        "v_mov_b32 v208, 0x3f003f00\n\t"
        "v_mov_b32 v209, 0x3f003f00\n\t"
        "s_waitcnt lgkmcnt(2)\n\t"
        "v_mfma_f32_16x16x32_bf16 v[240:243], v[216:219], v[228:231], v[240:243]\n\t"
        "s_waitcnt lgkmcnt(0)\n\t"
        "v_mfma_f32_16x16x32_bf16 v[240:243], v[212:215], v[232:235], v[240:243]\n\t"
        "s_waitcnt vmcnt(0)\n\t"
        "v_max3_u32 v201, v248, v249, v250\n\t"
        "v_max_u32 v201, v201, v251\n\t"
        "v_cmp_eq_u32 vcc, -1, v201\n\t"
        "v_mov_b32 v228, 0\n\t"
        "v_mov_b32 v229, 0\n\t"
        "v_mov_b32 v232, 0\n\t"
        "v_mov_b32 v233, 0\n\t"
        "v_max3_f32 v203, v240, v241, v242\n\t"
        "v_max3_f32 v203, v203, v243, v200\n\t"
        "v_mov_b32 v201, v203\n\t"
        "s_nop 1\n\t"
        "v_permlane16_swap_b32 v203, v201\n\t"
        "v_max_f32 v203, v203, v201\n\t"
        "v_mul_f32 v202, 0xbe0293ee, v203\n\t"
        "v_fma_f32 v200, v200, s70, v202\n\t"
        "v_fma_f32 v240, v240, s70, v202\n\t"
        "v_fma_f32 v241, v241, s70, v202\n\t"
        "v_fma_f32 v242, v242, s70, v202\n\t"
        "v_fma_f32 v243, v243, s70, v202\n\t"
        "v_exp_f32 v200, v200\n\t"
        "v_exp_f32 v240, v240\n\t"
        "v_exp_f32 v241, v241\n\t"
        "v_exp_f32 v242, v242\n\t"
        "v_exp_f32 v243, v243\n\t"
        "v_mul_f32 v236, v236, v200\n\t"
        "v_cvt_pk_bf16_f32 v230, v240, v241\n\t"
        "v_cvt_pk_bf16_f32 v231, v242, v243\n\t"
        "v_mul_f32 v220, v220, v200\n\t"
        "v_mul_f32 v221, v221, v200\n\t"
        "v_mul_f32 v222, v222, v200\n\t"
        "v_mul_f32 v223, v223, v200\n\t"
        "v_mfma_f32_16x16x32_bf16 v[236:239], v[208:211], v[230:233], v[236:239]\n\t"
        "s_cbranch_vccnz Vr_%=\n\t"
        "Vk_%=:\n\t"
        "v_mul_f32 v224, v224, v200\n\t"
        "v_mul_f32 v225, v225, v200\n\t"
        "v_mfma_f32_16x16x32_bf16 v[220:223], v[248:251], v[230:233], v[220:223]\n\t"
        "v_mul_f32 v226, v226, v200\n\t"
        "v_mul_f32 v227, v227, v200\n\t"
        "s_nop 1\n\t"
        "v_mfma_f32_16x16x32_bf16 v[224:227], v[248:251], v[228:231], v[224:227]\n\t"
        "s_nop 1\n\t"
        "v_rcp_f32 v200, v236\n\t"
        "s_nop 1\n\t"
        "v_mul_f32 v220, v220, v200\n\t"
        "v_mul_f32 v221, v221, v200\n\t"
        "v_mul_f32 v222, v222, v200\n\t"
        "v_mul_f32 v223, v223, v200\n\t"
        "v_cvt_pk_bf16_f32 v204, v220, v221\n\t"
        "v_cvt_pk_bf16_f32 v205, v222, v223\n\t"
        "v_mul_f32 v224, v224, v200\n\t"
        "v_mul_f32 v225, v225, v200\n\t"
        "v_mul_f32 v226, v226, v200\n\t"
        "v_mul_f32 v227, v227, v200\n\t"
        "v_min_u32 v204, -2, v204\n\t"
        "v_min_u32 v205, -2, v205\n\t"
        "v_cvt_pk_bf16_f32 v206, v224, v225\n\t"
        "v_cvt_pk_bf16_f32 v207, v226, v227\n\t"
        "v_min_u32 v206, -2, v206\n\t"
        "v_min_u32 v207, -2, v207\n\t"
        "buffer_store_dwordx4 v[204:207], v252, s[40:43], s60 offen sc1\n\t"
        "s_memrealtime s[60:61]\n\t"
        "s_waitcnt lgkmcnt(0)\n\t"
        "s_sub_i32 s60, s60, s74\n\t"
        "s_min_u32 s94, s60, 100\n\t"
        "s_add_i32 s57, s57, 1\n\t"
        "s_and_b32 s58, s57, 3\n\t"
        "s_cmp_lg_u32 s58, 0\n\t"
        "s_cbranch_scc1 krS1_%=\n\t"
        "s_lshr_b32 s58, s57, 2\n\t"
        "s_mul_i32 s58, s58, 48\n\t"
        "s_lshr_b32 s59, s53, 1\n\t"
        "s_add_i32 s58, s58, s59\n\t"
        "s_add_i32 s58, s58, 32\n\t"
        "s_lshl_b32 s58, s58, 6\n\t"
        "s_add_i32 s58, s58, %[qf]\n\t"
        "kpS1_%=:\n\t"
        "buffer_load_dword v200, off, s[40:43], s58 sc1\n\t"
        "s_waitcnt vmcnt(0)\n\t"
        "v_cmp_eq_u32 vcc, 0, v200\n\t"
        "s_cbranch_vccz krS1_%=\n\t"
        "s_sleep 4\n\t"
        "s_sub_i32 s56, s56, 1\n\t"
        "s_cmp_gt_i32 s56, 0\n\t"
        "s_cbranch_scc1 kpS1_%=\n\t"
        "krS1_%=:\n\t"
        "v_mbcnt_lo_u32_b32 v200, -1, 0\n\t"
        "v_mbcnt_hi_u32_b32 v200, -1, v200\n\t"
        "v_lshlrev_b32 v200, 4, v200\n\t"
        "s_mul_i32 s58, s57, 786432\n\t"
        "s_add_i32 s58, s58, s66\n\t"
        "s_lshl_b32 s60, s52, 11\n\t"
        "s_add_i32 s58, s58, s60\n\t"
        "s_add_i32 s58, s58, 8192\n\t"
        "s_add_i32 m0, s60, 32768\n\t"
        "s_nop 0\n\t"
        "buffer_load_dwordx4 v200, s[40:43], s58 offen sc1 lds\n\t"
        "s_add_i32 m0, s60, 33792\n\t"
        "s_add_i32 s58, s58, 1024\n\t"
        "buffer_load_dwordx4 v200, s[40:43], s58 offen sc1 lds\n\t"
        "s_branch SL_%=\n\t"
        "LAST_%=:\n\t"
        "s_barrier\n\t"
        "s_barrier\n\t"
        "s_branch END_%=\n\t"
        "Vr_%=:\n\t"
        "s_add_i32 s73, s73, 1\n\t"
        "s_sub_i32 s56, s56, 1\n\t"
        "s_cmp_gt_i32 s56, 0\n\t"
        "s_cbranch_scc0 Vk_%=\n\t"
        "v_mbcnt_lo_u32_b32 v255, -1, 0\n\t"
        "v_mbcnt_hi_u32_b32 v255, -1, v255\n\t"
        "v_lshlrev_b32 v255, 3, v255\n\t"
        "buffer_load_dwordx2 v[248:249], v255, s[40:43], s58 offen sc1\n\t"
        "buffer_load_dwordx2 v[250:251], v255, s[40:43], s58 offen offset:512 sc1\n\t"
        "s_waitcnt vmcnt(0)\n\t"
        "v_max3_u32 v201, v248, v249, v250\n\t"
        "v_max_u32 v201, v201, v251\n\t"
        "v_cmp_eq_u32 vcc, -1, v201\n\t"
        "s_cbranch_vccnz Vr_%=\n\t"
        "s_branch Vk_%=\n\t"
        "PB_%=:\n\t"
        "s_and_b32 s58, s57, 3\n\t"
        "s_cmp_lg_u32 s58, 0\n\t"
        "s_cbranch_scc1 krH0_%=\n\t"
        "s_lshr_b32 s58, s57, 2\n\t"
        "s_mul_i32 s58, s58, 48\n\t"
        "s_lshr_b32 s59, s53, 1\n\t"
        "s_add_i32 s58, s58, s59\n\t"
        "s_add_i32 s58, s58, 16\n\t"
        "s_lshl_b32 s58, s58, 6\n\t"
        "s_add_i32 s58, s58, %[qf]\n\t"
        "kpH0_%=:\n\t"
        "buffer_load_dword v200, off, s[40:43], s58 sc1\n\t"
        "s_waitcnt vmcnt(0)\n\t"
        "v_cmp_eq_u32 vcc, 0, v200\n\t"
        "s_cbranch_vccz krH0_%=\n\t"
        "s_sleep 4\n\t"
        "s_sub_i32 s56, s56, 1\n\t"
        "s_cmp_gt_i32 s56, 0\n\t"
        "s_cbranch_scc1 kpH0_%=\n\t"
        "krH0_%=:\n\t"
        "v_mbcnt_lo_u32_b32 v200, -1, 0\n\t"
        "v_mbcnt_hi_u32_b32 v200, -1, v200\n\t"
        "v_lshlrev_b32 v200, 4, v200\n\t"
        "s_mul_i32 s58, s57, 786432\n\t"
        "s_add_i32 s58, s58, s66\n\t"
        "s_and_b32 s60, s52, 3\n\t"
        "s_lshl_b32 s60, s60, 10\n\t"
        "s_add_i32 s58, s58, s60\n\t"
        "s_and_b32 s59, s57, 1\n\t"
        "s_lshl_b32 s59, s59, 13\n\t"
        "s_add_i32 s60, s60, s59\n\t"
        "s_add_i32 m0, s60, 16384\n\t"
        "s_nop 0\n\t"
        "buffer_load_dwordx4 v200, s[40:43], s58 offen sc1 lds\n\t"
        "s_add_i32 m0, s60, 20480\n\t"
        "s_add_i32 s58, s58, 4096\n\t"
        "buffer_load_dwordx4 v200, s[40:43], s58 offen sc1 lds\n\t"
        "HL_%=:\n\t"
        "v_mbcnt_lo_u32_b32 v200, -1, 0\n\t"
        "v_mbcnt_hi_u32_b32 v200, -1, v200\n\t"
        "v_lshlrev_b32 v252, 4, v200\n\t"
        "v_add_u32 v248, s65, v252\n\t"
        "v_add_u32 v249, s72, v252\n\t"
        "ds_read_b128 v[212:215], v249 offset:0\n\t"
        "ds_read_b128 v[216:219], v249 offset:1024\n\t"
        "ds_read_b128 v[220:223], v249 offset:2048\n\t"
        "ds_read_b128 v[224:227], v249 offset:3072\n\t"
        "ds_read_b128 v[236:239], v249 offset:4096\n\t"
        "ds_read_b128 v[240:243], v249 offset:5120\n\t"
        "v_bfe_u32 v201, v200, 3, 1\n\t"
        "v_mul_u32_u24 v201, 0xaaaa, v201\n\t"
        "v_add_u32 v251, 0x4444, v201\n\t"
        "s_lshl_b32 s58, s52, 9\n\t"
        "s_add_i32 s58, s58, 40960\n\t"
        "v_bfe_u32 v201, v200, 5, 1\n\t"
        "v_lshl_add_u32 v250, v201, 8, s58\n\t"
        "v_bfe_u32 v201, v200, 0, 4\n\t"
        "v_lshl_add_u32 v250, v201, 4, v250\n\t"
        "v_bfe_u32 v201, v200, 4, 1\n\t"
        "v_lshl_add_u32 v250, v201, 3, v250\n\t"
        "s_mov_b32 s58, 40960\n\t"
        "v_bfe_u32 v201, v200, 4, 2\n\t"
        "v_lshl_add_u32 v253, v201, 7, s58\n\t"
        "v_bfe_u32 v201, v200, 2, 2\n\t"
        "v_lshl_add_u32 v253, v201, 4, v253\n\t"
        "v_bfe_u32 v201, v200, 0, 1\n\t"
        "v_lshl_add_u32 v253, v201, 3, v253\n\t"
        "v_bfe_u32 v201, v200, 5, 1\n\t"
        "v_lshl_add_u32 v254, v201, 9, s65\n\t"
        "v_bfe_u32 v201, v200, 3, 1\n\t"
        "v_lshl_add_u32 v254, v201, 8, v254\n\t"
        "v_bfe_u32 v201, v200, 2, 1\n\t"
        "v_lshl_add_u32 v254, v201, 7, v254\n\t"
        "v_bfe_u32 v201, v200, 4, 1\n\t"
        "v_lshl_add_u32 v254, v201, 6, v254\n\t"
        "v_bfe_u32 v201, v200, 0, 2\n\t"
        "v_lshl_add_u32 v254, v201, 4, v254\n\t"
        "v_mov_b32 v228, 0\n\t"
        "v_mov_b32 v229, 0\n\t"
        "v_mov_b32 v230, 0\n\t"
        "v_mov_b32 v231, 0\n\t"
        "v_mov_b32 v232, 0\n\t"
        "v_mov_b32 v233, 0\n\t"
        "v_mov_b32 v234, 0\n\t"
        "v_mov_b32 v235, 0\n\t"
        "s_mov_b32 s73, 0\n\t"
        "s_cmp_eq_u32 s57, 0\n\t"
        "s_cbranch_scc1 Hns_%=\n\t"
        "s_sleep %[slh]\n\t"
        "Hns_%=:\n\t"
        "s_and_b32 s58, s57, 3\n\t"
        "s_lshl_b32 s58, s58, 15\n\t"
        "s_add_i32 s58, s58, s54\n\t"
        "s_add_i32 s59, s58, 16384\n\t"
        "Hg_%=:\n\t"
        "buffer_load_dwordx4 v[200:203], v248, s[40:43], s58 offen sc1\n\t"
        "buffer_load_dwordx4 v[204:207], v248, s[40:43], s59 offen sc1\n\t"
        "s_waitcnt vmcnt(1)\n\t"
        "v_max3_u32 v208, v200, v201, v202\n\t"
        "s_waitcnt vmcnt(0)\n\t"
        "v_max3_u32 v208, v208, v203, v204\n\t"
        "v_max3_u32 v208, v208, v205, v206\n\t"
        "v_max_u32 v208, v208, v207\n\t"
        "v_cmp_eq_u32 vcc, -1, v208\n\t"
        "s_cbranch_vccz Hgk_%=\n\t"
        "s_add_i32 s73, s73, 0x10000\n\t"
        "s_sub_i32 s56, s56, 1\n\t"
        "s_cmp_gt_i32 s56, 0\n\t"
        "s_cbranch_scc1 Hg_%=\n\t"
        "Hgk_%=:\n\t"
        "ds_write_b128 v254, v[200:203]\n\t"
        "ds_write_b128 v254, v[204:207] offset:8192\n\t"
        "s_cmp_eq_u32 s57, 63\n\t"
        "s_waitcnt lgkmcnt(0)\n\t"
        "s_barrier\n\t"
        "ds_read_b128 v[200:203], v252 offset:0\n\t"
        "ds_read_b128 v[204:207], v252 offset:1024\n\t"
        "ds_read_b128 v[208:211], v252 offset:2048\n\t"
        "ds_read_b128 v[244:247], v252 offset:3072\n\t"
        "s_cmp_lg_u32 s52, 6\n\t"
        "s_cbranch_scc1 np_%=\n\t"
        "s_lshl_b32 s61, s53, 7\n\t"
        "s_add_i32 s61, s61, %[pg]\n\t"
        "v_mov_b32 v255, s57\n\t"
        "buffer_store_dword v255, off, s[40:43], s61 sc1\n\t"
        "np_%=:\n\t"
        "s_waitcnt lgkmcnt(3)\n\t"
        "v_smfmac_f32_16x16x64_bf16 v[228:231], v[200:203], v[0:7], v251\n\t"
        "v_smfmac_f32_16x16x64_bf16 v[232:235], v[200:203], v[128:135], v251\n\t"
        "ds_read_b128 v[200:203], v252 offset:4096\n\t"
        "s_waitcnt lgkmcnt(3)\n\t"
        "v_smfmac_f32_16x16x64_bf16 v[228:231], v[204:207], v[8:15], v251\n\t"
        "v_smfmac_f32_16x16x64_bf16 v[232:235], v[204:207], v[212:219], v251\n\t"
        "ds_read_b128 v[204:207], v252 offset:5120\n\t"
        "ds_read_b128 v[212:215], v249 offset:6144\n\t"
        "ds_read_b128 v[216:219], v249 offset:7168\n\t"
        "s_waitcnt lgkmcnt(5)\n\t"
        "v_smfmac_f32_16x16x64_bf16 v[228:231], v[208:211], v[16:23], v251\n\t"
        "v_smfmac_f32_16x16x64_bf16 v[232:235], v[208:211], v[136:143], v251\n\t"
        "ds_read_b128 v[208:211], v252 offset:6144\n\t"
        "s_waitcnt lgkmcnt(5)\n\t"
        "v_smfmac_f32_16x16x64_bf16 v[228:231], v[244:247], v[24:31], v251\n\t"
        "v_smfmac_f32_16x16x64_bf16 v[232:235], v[244:247], v[220:227], v251\n\t"
        "ds_read_b128 v[244:247], v252 offset:7168\n\t"
        "ds_read_b128 v[220:223], v249 offset:8192\n\t"
        "ds_read_b128 v[224:227], v249 offset:9216\n\t"
        "s_waitcnt lgkmcnt(7)\n\t"
        "v_smfmac_f32_16x16x64_bf16 v[228:231], v[200:203], v[32:39], v251\n\t"
        "v_smfmac_f32_16x16x64_bf16 v[232:235], v[200:203], v[144:151], v251\n\t"
        "ds_read_b128 v[200:203], v252 offset:8192\n\t"
        "s_waitcnt lgkmcnt(7)\n\t"
        "v_smfmac_f32_16x16x64_bf16 v[228:231], v[204:207], v[40:47], v251\n\t"
        "v_smfmac_f32_16x16x64_bf16 v[232:235], v[204:207], v[236:243], v251\n\t"
        "ds_read_b128 v[204:207], v252 offset:9216\n\t"
        "ds_read_b128 v[236:239], v249 offset:10240\n\t"
        "ds_read_b128 v[240:243], v249 offset:11264\n\t"
        "s_waitcnt lgkmcnt(7)\n\t"
        "v_smfmac_f32_16x16x64_bf16 v[228:231], v[208:211], v[48:55], v251\n\t"
        "v_smfmac_f32_16x16x64_bf16 v[232:235], v[208:211], v[152:159], v251\n\t"
        "ds_read_b128 v[208:211], v252 offset:10240\n\t"
        "s_waitcnt lgkmcnt(7)\n\t"
        "v_smfmac_f32_16x16x64_bf16 v[228:231], v[244:247], v[56:63], v251\n\t"
        "v_smfmac_f32_16x16x64_bf16 v[232:235], v[244:247], v[212:219], v251\n\t"
        "ds_read_b128 v[244:247], v252 offset:11264\n\t"
        "ds_read_b128 v[212:215], v249 offset:12288\n\t"
        "ds_read_b128 v[216:219], v249 offset:13312\n\t"
        "s_barrier\n\t"
        "s_setprio 3\n\t"
        "s_waitcnt lgkmcnt(7)\n\t"
        "v_smfmac_f32_16x16x64_bf16 v[228:231], v[200:203], v[64:71], v251\n\t"
        "v_smfmac_f32_16x16x64_bf16 v[232:235], v[200:203], v[160:167], v251\n\t"
        "ds_read_b128 v[200:203], v252 offset:12288\n\t"
        "s_waitcnt lgkmcnt(7)\n\t"
        "v_smfmac_f32_16x16x64_bf16 v[228:231], v[204:207], v[72:79], v251\n\t"
        "v_smfmac_f32_16x16x64_bf16 v[232:235], v[204:207], v[220:227], v251\n\t"
        "ds_read_b128 v[204:207], v252 offset:13312\n\t"
        "s_waitcnt lgkmcnt(5)\n\t"
        "v_smfmac_f32_16x16x64_bf16 v[228:231], v[208:211], v[80:87], v251\n\t"
        "v_smfmac_f32_16x16x64_bf16 v[232:235], v[208:211], v[168:175], v251\n\t"
        "ds_read_b128 v[208:211], v252 offset:14336\n\t"
        "s_waitcnt lgkmcnt(5)\n\t"
        "v_smfmac_f32_16x16x64_bf16 v[228:231], v[244:247], v[88:95], v251\n\t"
        "v_smfmac_f32_16x16x64_bf16 v[232:235], v[244:247], v[236:243], v251\n\t"
        "ds_read_b128 v[244:247], v252 offset:15360\n\t"
        "s_waitcnt lgkmcnt(3)\n\t"
        "v_smfmac_f32_16x16x64_bf16 v[228:231], v[200:203], v[96:103], v251\n\t"
        "v_smfmac_f32_16x16x64_bf16 v[232:235], v[200:203], v[176:183], v251\n\t"
        "s_waitcnt lgkmcnt(2)\n\t"
        "v_smfmac_f32_16x16x64_bf16 v[228:231], v[204:207], v[104:111], v251\n\t"
        "v_smfmac_f32_16x16x64_bf16 v[232:235], v[204:207], v[212:219], v251\n\t"
        "s_waitcnt lgkmcnt(1)\n\t"
        "v_smfmac_f32_16x16x64_bf16 v[228:231], v[208:211], v[112:119], v251\n\t"
        "v_smfmac_f32_16x16x64_bf16 v[232:235], v[208:211], v[184:191], v251\n\t"
        "s_waitcnt lgkmcnt(0)\n\t"
        "v_smfmac_f32_16x16x64_bf16 v[228:231], v[244:247], v[120:127], v251\n\t"
        "v_smfmac_f32_16x16x64_bf16 v[232:235], v[244:247], v[192:199], v251\n\t"
        "s_and_b32 s58, s57, 3\n\t"
        "s_lshl_b32 s58, s58, 18\n\t"
        "s_add_i32 s58, s58, s67\n\t"
        "s_nop 4\n\t"
        "v_permlane32_swap_b32 v228, v232\n\t"
        "v_permlane32_swap_b32 v229, v233\n\t"
        "v_permlane32_swap_b32 v230, v234\n\t"
        "v_permlane32_swap_b32 v231, v235\n\t"
        "v_pk_add_f32 v[228:229], v[228:229], v[232:233]\n\t"
        "v_pk_add_f32 v[230:231], v[230:231], v[234:235]\n\t"
        "v_cvt_pk_bf16_f32 v232, v228, v229\n\t"
        "v_cvt_pk_bf16_f32 v233, v230, v231\n\t"
        "ds_write_b64 v250, v[232:233]\n\t"
        "s_waitcnt lgkmcnt(0)\n\t"
        "s_barrier\n\t"
        "s_setprio 0\n\t"
        "s_and_b32 s60, s52, 3\n\t"
        "s_lshl_b32 s61, s60, 9\n\t"
        "v_add_u32 v253, s61, v253\n\t"
        "s_cmp_eq_u32 s57, 63\n\t"
        "s_cbranch_scc1 nsl_%=\n\t"
        "s_sleep %[slk]\n\t"
        "nsl_%=:\n\t"
        "ds_read_b64_tr_b16 v[204:205], v253 offset:2048\n\t"
        "ds_read_b64_tr_b16 v[206:207], v253 offset:2112\n\t"
        "s_lshl_b32 s61, s57, 17\n\t"
        "s_add_i32 s61, s61, s68\n\t"
        "s_lshl_b32 s62, s60, 10\n\t"
        "s_add_i32 s61, s61, s62\n\t"
        "v_mov_b32 v208, -1\n\t"
        "v_mov_b32 v209, -1\n\t"
        "v_mov_b32 v210, -1\n\t"
        "v_mov_b32 v211, -1\n\t"
        "s_waitcnt lgkmcnt(0)\n\t"
        "buffer_store_dwordx4 v[204:207], v252, s[40:43], s61 offen sc1\n\t"
        "s_cmp_gt_u32 s60, 1\n\t"
        "s_cbranch_scc1 nra_%=\n\t"
        "s_add_i32 s61, s57, 2\n\t"
        "s_and_b32 s61, s61, 3\n\t"
        "s_lshl_b32 s61, s61, 17\n\t"
        "s_add_i32 s61, s61, s69\n\t"
        "s_add_i32 s61, s61, s54\n\t"
        "s_lshl_b32 s60, s60, 16\n\t"
        "s_add_i32 s61, s61, s60\n\t"
        "buffer_store_dwordx4 v[208:211], v252, s[40:43], s61 offen sc1\n\t"
        "nra_%=:\n\t"
        "s_cmp_eq_u32 s57, 63\n\t"
        "s_cbranch_scc1 END_%=\n\t"
        "s_add_i32 s57, s57, 1\n\t"
        "s_and_b32 s58, s57, 3\n\t"
        "s_cmp_lg_u32 s58, 0\n\t"
        "s_cbranch_scc1 krH1_%=\n\t"
        "s_lshr_b32 s58, s57, 2\n\t"
        "s_mul_i32 s58, s58, 48\n\t"
        "s_lshr_b32 s59, s53, 1\n\t"
        "s_add_i32 s58, s58, s59\n\t"
        "s_add_i32 s58, s58, 16\n\t"
        "s_lshl_b32 s58, s58, 6\n\t"
        "s_add_i32 s58, s58, %[qf]\n\t"
        "kpH1_%=:\n\t"
        "buffer_load_dword v200, off, s[40:43], s58 sc1\n\t"
        "s_waitcnt vmcnt(0)\n\t"
        "v_cmp_eq_u32 vcc, 0, v200\n\t"
        "s_cbranch_vccz krH1_%=\n\t"
        "s_sleep 4\n\t"
        "s_sub_i32 s56, s56, 1\n\t"
        "s_cmp_gt_i32 s56, 0\n\t"
        "s_cbranch_scc1 kpH1_%=\n\t"
        "krH1_%=:\n\t"
        "v_mbcnt_lo_u32_b32 v200, -1, 0\n\t"
        "v_mbcnt_hi_u32_b32 v200, -1, v200\n\t"
        "v_lshlrev_b32 v200, 4, v200\n\t"
        "s_mul_i32 s58, s57, 786432\n\t"
        "s_add_i32 s58, s58, s66\n\t"
        "s_and_b32 s60, s52, 3\n\t"
        "s_lshl_b32 s60, s60, 10\n\t"
        "s_add_i32 s58, s58, s60\n\t"
        "s_and_b32 s59, s57, 1\n\t"
        "s_lshl_b32 s59, s59, 13\n\t"
        "s_add_i32 s60, s60, s59\n\t"
        "s_add_i32 m0, s60, 16384\n\t"
        "s_nop 0\n\t"
        "buffer_load_dwordx4 v200, s[40:43], s58 offen sc1 lds\n\t"
        "s_add_i32 m0, s60, 20480\n\t"
        "s_add_i32 s58, s58, 4096\n\t"
        "buffer_load_dwordx4 v200, s[40:43], s58 offen sc1 lds\n\t"
        "s_branch HL_%=\n\t"
        "END_%=:\n\t"
        "s_setprio 0\n\t"
        "s_waitcnt vmcnt(0) lgkmcnt(0)\n\t"
        "s_barrier\n\t"
        "s_cmp_lg_u32 s52, 6\n\t"
        "s_cbranch_scc1 nq_%=\n\t"
        "s_lshl_b32 s61, s53, 7\n\t"
        "s_add_i32 s61, s61, %[pg]\n\t"
        "v_mov_b32 v212, 64\n\t"
        "buffer_store_dword v212, off, s[40:43], s61 sc1\n\t"
        "nq_%=:\n\t"
        "v_mbcnt_lo_u32_b32 v250, -1, 0\n\t"
        "v_mbcnt_hi_u32_b32 v250, -1, v250\n\t"
        "s_lshl_b32 s58, s52, 8\n\t"
        "s_add_i32 s58, s58, 159744\n\t"
        "v_lshl_add_u32 v250, v250, 2, s58\n\t"
        "ds_read_b32 v199, v250\n\t"
        "s_waitcnt lgkmcnt(0)\n\t"
      :
      : [ws] "s"(ws), [wa] "s"(wa), [wb] "s"(wb), [wave] "s"(wave), [idx] "s"(idx), [rp] "s"(rp), [half] "s"(half),
        [wsn] "i"((int)WS_END), [bud] "i"(SPIN_CAP), [vb] "i"((int)WS_VB), [dbg] "i"((int)WS_DBG), [pg] "i"((int)C_PROG), [vote] "i"((int)C_VOTE), [kx] "i"((int)WS_QKV + 8192), [km] "i"((int)WS_KM), [qf] "i"((int)C_QKVF), [slp] "i"(SLEEP_P), [slh] "i"(SLEEP_H), [slk] "i"(SLEEP_K), [stw] "i"(STW_P)
      : "memory", "vcc", "scc", CHAIN_CLOBBERS);
}
DEVI void chain_VM_asm(const unsigned char* ws, const u16* wa, const u16* wb, int wave, int idx, int rp, int half) {
  asm volatile(
        "s_mov_b64 s[40:41], %[ws]\n\t"
        "s_mov_b32 s42, %[wsn]\n\t"
        "s_mov_b32 s43, 0x20000\n\t"
        "s_mov_b64 s[44:45], %[wa]\n\t"
        "s_mov_b32 s46, 8388608\n\t"
        "s_mov_b32 s47, 0x20000\n\t"
        "s_mov_b64 s[48:49], %[wb]\n\t"
        "s_mov_b32 s50, 8388608\n\t"
        "s_mov_b32 s51, 0x20000\n\t"
        "s_mov_b32 s52, %[wave]\n\t"
        "s_mov_b32 s53, %[idx]\n\t"
        "s_mov_b32 s54, %[rp]\n\t"
        "s_mov_b32 s56, %[bud]\n\t"
        "s_mov_b32 s57, 0\n\t"
        "s_mov_b32 s70, 0x3e0293ee\n\t"
        "s_mov_b32 s71, 0x7ff00000\n\t"
        "v_mbcnt_lo_u32_b32 v250, -1, 0\n\t"
        "v_mbcnt_hi_u32_b32 v250, -1, v250\n\t"
        "s_lshl_b32 s58, s52, 8\n\t"
        "s_add_i32 s58, s58, 159744\n\t"
        "v_lshl_add_u32 v250, v250, 2, s58\n\t"
        "ds_write_b32 v250, v199\n\t"
        "s_waitcnt lgkmcnt(0)\n\t"
        "s_mov_b32 s92, %[half]\n\t"
        "s_add_i32 s58, s92, 1\n\t"
        "s_lshl_b32 s58, s58, 5\n\t"
        "s_add_i32 s58, s58, s53\n\t"
        "s_lshl_b32 s58, s58, 7\n\t"
        "s_add_i32 s93, s58, %[pg]\n\t"
        "s_lshl_b32 s65, s52, 10\n\t"
        "s_and_b32 s58, s52, 3\n\t"
        "s_lshl_b32 s59, s92, 2\n\t"
        "s_add_i32 s62, s58, s59\n\t"
        "s_lshl_b32 s58, s62, 9\n\t"
        "s_lshr_b32 s59, s52, 2\n\t"
        "s_lshl_b32 s59, s59, 8\n\t"
        "s_add_i32 s60, s58, s59\n\t"
        "s_lshl_b32 s58, s53, 12\n\t"
        "s_add_i32 s67, s58, s60\n\t"
        "s_add_i32 s67, s67, %[vb]\n\t"
        "s_lshr_b32 s58, s53, 1\n\t"
        "s_lshl_b32 s58, s58, 14\n\t"
        "s_and_b32 s59, s53, 1\n\t"
        "s_lshl_b32 s59, s59, 12\n\t"
        "s_add_i32 s66, s58, s59\n\t"
        "s_add_i32 s66, s66, s60\n\t"
        "s_add_i32 s66, s66, 655360\n\t"
        "s_lshr_b32 s58, s53, 4\n\t"
        "s_lshl_b32 s58, s58, 14\n\t"
        "s_and_b32 s59, s53, 15\n\t"
        "s_lshl_b32 s59, s59, 9\n\t"
        "s_add_i32 s72, s58, s59\n\t"
        "s_lshl_b32 s59, s92, 8\n\t"
        "s_add_i32 s72, s72, s59\n\t"
        "v_mbcnt_lo_u32_b32 v252, -1, 0\n\t"
        "v_mbcnt_hi_u32_b32 v252, -1, v252\n\t"
        "s_lshr_b32 s58, s52, 1\n\t"
        "s_and_b32 s58, s58, 1\n\t"
        "s_lshl_b32 s58, s58, 2\n\t"
        "v_lshrrev_b32 v255, 4, v252\n\t"
        "v_or_b32 v255, s58, v255\n\t"
        "v_bfe_u32 v253, v252, 1, 3\n\t"
        "v_xor_b32 v253, v253, v255\n\t"
        "v_lshlrev_b32 v253, 5, v253\n\t"
        "v_and_b32 v255, 1, v252\n\t"
        "v_lshl_add_u32 v253, v255, 4, v253\n\t"
        "v_lshrrev_b32 v255, 4, v252\n\t"
        "v_lshl_add_u32 v253, v255, 13, v253\n\t"
        "s_lshl_b32 s58, s52, 15\n\t"
        "s_lshl_b32 s59, s53, 8\n\t"
        "s_add_i32 s58, s58, s59\n\t"
        "v_add_u32 v253, s58, v253\n\t"
        "v_bfe_u32 v254, v252, 2, 3\n\t"
        "v_xor_b32 v254, s62, v254\n\t"
        "v_lshlrev_b32 v254, 5, v254\n\t"
        "v_bfe_u32 v255, v252, 4, 2\n\t"
        "v_lshl_add_u32 v254, v255, 11, v254\n\t"
        "v_bfe_u32 v255, v252, 2, 2\n\t"
        "v_lshl_add_u32 v254, v255, 8, v254\n\t"
        "v_bfe_u32 v255, v252, 0, 2\n\t"
        "v_lshl_add_u32 v254, v255, 3, v254\n\t"
        "s_lshr_b32 s58, s52, 2\n\t"
        "s_lshl_b32 s58, s58, 14\n\t"
        "v_add_u32 v254, s58, v254\n\t"
        "s_add_i32 m0, s65, 24576\n\t"
        "s_mov_b32 s64, 0\n\t"
        "buffer_load_dwordx4 v253, s[44:47], s64 offen lds\n\t"
        "s_add_i32 m0, s65, 32768\n\t"
        "s_mov_b32 s64, 262144\n\t"
        "buffer_load_dwordx4 v253, s[44:47], s64 offen lds\n\t"
        "s_add_i32 m0, s65, 40960\n\t"
        "s_mov_b32 s64, 4194304\n\t"
        "buffer_load_dwordx4 v253, s[44:47], s64 offen lds\n\t"
        "s_add_i32 m0, s65, 49152\n\t"
        "s_mov_b32 s64, 4456448\n\t"
        "buffer_load_dwordx4 v253, s[44:47], s64 offen lds\n\t"
        "s_add_i32 m0, s65, 57344\n\t"
        "s_mov_b32 s64, 524288\n\t"
        "buffer_load_dwordx4 v253, s[44:47], s64 offen lds\n\t"
        "s_add_i32 m0, s65, 65536\n\t"
        "s_mov_b32 s64, 786432\n\t"
        "buffer_load_dwordx4 v253, s[44:47], s64 offen lds\n\t"
        "s_add_i32 m0, s65, 73728\n\t"
        "s_mov_b32 s64, 4718592\n\t"
        "buffer_load_dwordx4 v253, s[44:47], s64 offen lds\n\t"
        "s_add_i32 m0, s65, 81920\n\t"
        "s_mov_b32 s64, 4980736\n\t"
        "buffer_load_dwordx4 v253, s[44:47], s64 offen lds\n\t"
        "s_add_i32 m0, s65, 90112\n\t"
        "s_mov_b32 s64, 1048576\n\t"
        "buffer_load_dwordx4 v253, s[44:47], s64 offen lds\n\t"
        "s_add_i32 m0, s65, 98304\n\t"
        "s_mov_b32 s64, 1310720\n\t"
        "buffer_load_dwordx4 v253, s[44:47], s64 offen lds\n\t"
        "s_add_i32 m0, s65, 106496\n\t"
        "s_mov_b32 s64, 5242880\n\t"
        "buffer_load_dwordx4 v253, s[44:47], s64 offen lds\n\t"
        "s_add_i32 m0, s65, 114688\n\t"
        "s_mov_b32 s64, 5505024\n\t"
        "buffer_load_dwordx4 v253, s[44:47], s64 offen lds\n\t"
        "s_add_i32 m0, s65, 122880\n\t"
        "s_mov_b32 s64, 1572864\n\t"
        "buffer_load_dwordx4 v253, s[44:47], s64 offen lds\n\t"
        "s_add_i32 m0, s65, 131072\n\t"
        "s_mov_b32 s64, 1835008\n\t"
        "buffer_load_dwordx4 v253, s[44:47], s64 offen lds\n\t"
        "s_add_i32 m0, s65, 139264\n\t"
        "s_mov_b32 s64, 5767168\n\t"
        "buffer_load_dwordx4 v253, s[44:47], s64 offen lds\n\t"
        "s_add_i32 m0, s65, 147456\n\t"
        "s_mov_b32 s64, 6029312\n\t"
        "buffer_load_dwordx4 v253, s[44:47], s64 offen lds\n\t"
        "s_waitcnt vmcnt(12)\n\t"
        "s_barrier\n\t"
        "v_add_u32 v255, 24576, v254\n\t"
        "ds_read_b64_tr_b16 v[128:129], v255 offset:0\n\t"
        "ds_read_b64_tr_b16 v[130:131], v255 offset:1024\n\t"
        "ds_read_b64_tr_b16 v[132:133], v255 offset:8192\n\t"
        "ds_read_b64_tr_b16 v[134:135], v255 offset:9216\n\t"
        "s_waitcnt lgkmcnt(0)\n\t"
        "s_barrier\n\t"
        "s_add_i32 m0, s65, 24576\n\t"
        "s_mov_b32 s64, 2097152\n\t"
        "buffer_load_dwordx4 v253, s[44:47], s64 offen lds\n\t"
        "s_add_i32 m0, s65, 32768\n\t"
        "s_mov_b32 s64, 2359296\n\t"
        "buffer_load_dwordx4 v253, s[44:47], s64 offen lds\n\t"
        "s_add_i32 m0, s65, 40960\n\t"
        "s_mov_b32 s64, 6291456\n\t"
        "buffer_load_dwordx4 v253, s[44:47], s64 offen lds\n\t"
        "s_add_i32 m0, s65, 49152\n\t"
        "s_mov_b32 s64, 6553600\n\t"
        "buffer_load_dwordx4 v253, s[44:47], s64 offen lds\n\t"
        "s_waitcnt vmcnt(12)\n\t"
        "s_barrier\n\t"
        "v_add_u32 v255, 57344, v254\n\t"
        "ds_read_b64_tr_b16 v[136:137], v255 offset:0\n\t"
        "ds_read_b64_tr_b16 v[138:139], v255 offset:1024\n\t"
        "ds_read_b64_tr_b16 v[140:141], v255 offset:8192\n\t"
        "ds_read_b64_tr_b16 v[142:143], v255 offset:9216\n\t"
        "s_waitcnt lgkmcnt(0)\n\t"
        "s_barrier\n\t"
        "s_add_i32 m0, s65, 57344\n\t"
        "s_mov_b32 s64, 2621440\n\t"
        "buffer_load_dwordx4 v253, s[44:47], s64 offen lds\n\t"
        "s_add_i32 m0, s65, 65536\n\t"
        "s_mov_b32 s64, 2883584\n\t"
        "buffer_load_dwordx4 v253, s[44:47], s64 offen lds\n\t"
        "s_add_i32 m0, s65, 73728\n\t"
        "s_mov_b32 s64, 6815744\n\t"
        "buffer_load_dwordx4 v253, s[44:47], s64 offen lds\n\t"
        "s_add_i32 m0, s65, 81920\n\t"
        "s_mov_b32 s64, 7077888\n\t"
        "buffer_load_dwordx4 v253, s[44:47], s64 offen lds\n\t"
        "s_waitcnt vmcnt(12)\n\t"
        "s_barrier\n\t"
        "v_add_u32 v255, 90112, v254\n\t"
        "ds_read_b64_tr_b16 v[144:145], v255 offset:0\n\t"
        "ds_read_b64_tr_b16 v[146:147], v255 offset:1024\n\t"
        "ds_read_b64_tr_b16 v[148:149], v255 offset:8192\n\t"
        "ds_read_b64_tr_b16 v[150:151], v255 offset:9216\n\t"
        "s_waitcnt lgkmcnt(0)\n\t"
        "s_barrier\n\t"
        "s_add_i32 m0, s65, 90112\n\t"
        "s_mov_b32 s64, 3145728\n\t"
        "buffer_load_dwordx4 v253, s[44:47], s64 offen lds\n\t"
        "s_add_i32 m0, s65, 98304\n\t"
        "s_mov_b32 s64, 3407872\n\t"
        "buffer_load_dwordx4 v253, s[44:47], s64 offen lds\n\t"
        "s_add_i32 m0, s65, 106496\n\t"
        "s_mov_b32 s64, 7340032\n\t"
        "buffer_load_dwordx4 v253, s[44:47], s64 offen lds\n\t"
        "s_add_i32 m0, s65, 114688\n\t"
        "s_mov_b32 s64, 7602176\n\t"
        "buffer_load_dwordx4 v253, s[44:47], s64 offen lds\n\t"
        "s_waitcnt vmcnt(12)\n\t"
        "s_barrier\n\t"
        "v_add_u32 v255, 122880, v254\n\t"
        "ds_read_b64_tr_b16 v[152:153], v255 offset:0\n\t"
        "ds_read_b64_tr_b16 v[154:155], v255 offset:1024\n\t"
        "ds_read_b64_tr_b16 v[156:157], v255 offset:8192\n\t"
        "ds_read_b64_tr_b16 v[158:159], v255 offset:9216\n\t"
        "s_waitcnt lgkmcnt(0)\n\t"
        "s_barrier\n\t"
        "s_add_i32 m0, s65, 122880\n\t"
        "s_mov_b32 s64, 3670016\n\t"
        "buffer_load_dwordx4 v253, s[44:47], s64 offen lds\n\t"
        "s_add_i32 m0, s65, 131072\n\t"
        "s_mov_b32 s64, 3932160\n\t"
        "buffer_load_dwordx4 v253, s[44:47], s64 offen lds\n\t"
        "s_add_i32 m0, s65, 139264\n\t"
        "s_mov_b32 s64, 7864320\n\t"
        "buffer_load_dwordx4 v253, s[44:47], s64 offen lds\n\t"
        "s_add_i32 m0, s65, 147456\n\t"
        "s_mov_b32 s64, 8126464\n\t"
        "buffer_load_dwordx4 v253, s[44:47], s64 offen lds\n\t"
        "s_waitcnt vmcnt(12)\n\t"
        "s_barrier\n\t"
        "v_add_u32 v255, 24576, v254\n\t"
        "ds_read_b64_tr_b16 v[160:161], v255 offset:0\n\t"
        "ds_read_b64_tr_b16 v[162:163], v255 offset:1024\n\t"
        "ds_read_b64_tr_b16 v[164:165], v255 offset:8192\n\t"
        "ds_read_b64_tr_b16 v[166:167], v255 offset:9216\n\t"
        "s_waitcnt lgkmcnt(0)\n\t"
        "s_barrier\n\t"
        "s_waitcnt vmcnt(8)\n\t"
        "s_barrier\n\t"
        "v_add_u32 v255, 57344, v254\n\t"
        "ds_read_b64_tr_b16 v[168:169], v255 offset:0\n\t"
        "ds_read_b64_tr_b16 v[170:171], v255 offset:1024\n\t"
        "ds_read_b64_tr_b16 v[172:173], v255 offset:8192\n\t"
        "ds_read_b64_tr_b16 v[174:175], v255 offset:9216\n\t"
        "s_waitcnt lgkmcnt(0)\n\t"
        "s_barrier\n\t"
        "s_waitcnt vmcnt(4)\n\t"
        "s_barrier\n\t"
        "v_add_u32 v255, 90112, v254\n\t"
        "ds_read_b64_tr_b16 v[176:177], v255 offset:0\n\t"
        "ds_read_b64_tr_b16 v[178:179], v255 offset:1024\n\t"
        "ds_read_b64_tr_b16 v[180:181], v255 offset:8192\n\t"
        "ds_read_b64_tr_b16 v[182:183], v255 offset:9216\n\t"
        "s_waitcnt lgkmcnt(0)\n\t"
        "s_barrier\n\t"
        "s_waitcnt vmcnt(0)\n\t"
        "s_barrier\n\t"
        "v_add_u32 v255, 122880, v254\n\t"
        "ds_read_b64_tr_b16 v[184:185], v255 offset:0\n\t"
        "ds_read_b64_tr_b16 v[186:187], v255 offset:1024\n\t"
        "ds_read_b64_tr_b16 v[188:189], v255 offset:8192\n\t"
        "ds_read_b64_tr_b16 v[190:191], v255 offset:9216\n\t"
        "s_waitcnt lgkmcnt(0)\n\t"
        "s_barrier\n\t"
        "s_lshl_b32 s58, s52, 20\n\t"
        "s_lshl_b32 s59, s53, 6\n\t"
        "s_add_i32 s58, s58, s59\n\t"
        "v_bfe_u32 v255, v252, 3, 1\n\t"
        "v_lshl_add_u32 v253, v255, 16, s58\n\t"
        "v_bfe_u32 v255, v252, 5, 1\n\t"
        "v_lshl_add_u32 v253, v255, 15, v253\n\t"
        "v_bfe_u32 v255, v252, 4, 1\n\t"
        "v_lshl_add_u32 v253, v255, 12, v253\n\t"
        "v_bfe_u32 v255, v252, 2, 1\n\t"
        "v_lshl_add_u32 v253, v255, 11, v253\n\t"
        "v_bfe_u32 v255, v252, 0, 2\n\t"
        "v_lshl_add_u32 v253, v255, 4, v253\n\t"
        "s_lshl_b32 s63, s52, 14\n\t"
        "s_add_i32 s63, s63, 24576\n\t"
        "v_bfe_u32 v255, v252, 4, 2\n\t"
        "v_lshl_add_u32 v254, v255, 9, s63\n\t"
        "v_bfe_u32 v255, v252, 2, 2\n\t"
        "v_lshl_add_u32 v254, v255, 6, v254\n\t"
        "v_bfe_u32 v255, v252, 0, 2\n\t"
        "v_lshl_add_u32 v254, v255, 3, v254\n\t"
        "s_add_i32 m0, s63, 0\n\t"
        "s_mov_b32 s64, 0\n\t"
        "buffer_load_dwordx4 v253, s[48:51], s64 offen lds\n\t"
        "s_add_i32 m0, s63, 1024\n\t"
        "s_mov_b32 s64, 8192\n\t"
        "buffer_load_dwordx4 v253, s[48:51], s64 offen lds\n\t"
        "s_add_i32 m0, s63, 2048\n\t"
        "s_mov_b32 s64, 16384\n\t"
        "buffer_load_dwordx4 v253, s[48:51], s64 offen lds\n\t"
        "s_add_i32 m0, s63, 3072\n\t"
        "s_mov_b32 s64, 24576\n\t"
        "buffer_load_dwordx4 v253, s[48:51], s64 offen lds\n\t"
        "s_add_i32 m0, s63, 4096\n\t"
        "s_mov_b32 s64, 131072\n\t"
        "buffer_load_dwordx4 v253, s[48:51], s64 offen lds\n\t"
        "s_add_i32 m0, s63, 5120\n\t"
        "s_mov_b32 s64, 139264\n\t"
        "buffer_load_dwordx4 v253, s[48:51], s64 offen lds\n\t"
        "s_add_i32 m0, s63, 6144\n\t"
        "s_mov_b32 s64, 147456\n\t"
        "buffer_load_dwordx4 v253, s[48:51], s64 offen lds\n\t"
        "s_add_i32 m0, s63, 7168\n\t"
        "s_mov_b32 s64, 155648\n\t"
        "buffer_load_dwordx4 v253, s[48:51], s64 offen lds\n\t"
        "s_add_i32 m0, s63, 8192\n\t"
        "s_mov_b32 s64, 262144\n\t"
        "buffer_load_dwordx4 v253, s[48:51], s64 offen lds\n\t"
        "s_add_i32 m0, s63, 9216\n\t"
        "s_mov_b32 s64, 270336\n\t"
        "buffer_load_dwordx4 v253, s[48:51], s64 offen lds\n\t"
        "s_add_i32 m0, s63, 10240\n\t"
        "s_mov_b32 s64, 278528\n\t"
        "buffer_load_dwordx4 v253, s[48:51], s64 offen lds\n\t"
        "s_add_i32 m0, s63, 11264\n\t"
        "s_mov_b32 s64, 286720\n\t"
        "buffer_load_dwordx4 v253, s[48:51], s64 offen lds\n\t"
        "s_add_i32 m0, s63, 12288\n\t"
        "s_mov_b32 s64, 393216\n\t"
        "buffer_load_dwordx4 v253, s[48:51], s64 offen lds\n\t"
        "s_add_i32 m0, s63, 13312\n\t"
        "s_mov_b32 s64, 401408\n\t"
        "buffer_load_dwordx4 v253, s[48:51], s64 offen lds\n\t"
        "s_add_i32 m0, s63, 14336\n\t"
        "s_mov_b32 s64, 409600\n\t"
        "buffer_load_dwordx4 v253, s[48:51], s64 offen lds\n\t"
        "s_add_i32 m0, s63, 15360\n\t"
        "s_mov_b32 s64, 417792\n\t"
        "buffer_load_dwordx4 v253, s[48:51], s64 offen lds\n\t"
        "s_waitcnt vmcnt(12)\n\t"
        "v_add_u32 v255, 0, v254\n\t"
        "ds_read_b64_tr_b16 v[0:1], v255 offset:0\n\t"
        "ds_read_b64_tr_b16 v[2:3], v255 offset:256\n\t"
        "ds_read_b64_tr_b16 v[4:5], v255 offset:2048\n\t"
        "ds_read_b64_tr_b16 v[6:7], v255 offset:2304\n\t"
        "s_waitcnt lgkmcnt(0)\n\t"
        "v_add_u32 v255, 32, v254\n\t"
        "ds_read_b64_tr_b16 v[8:9], v255 offset:0\n\t"
        "ds_read_b64_tr_b16 v[10:11], v255 offset:256\n\t"
        "ds_read_b64_tr_b16 v[12:13], v255 offset:2048\n\t"
        "ds_read_b64_tr_b16 v[14:15], v255 offset:2304\n\t"
        "s_waitcnt lgkmcnt(0)\n\t"
        "s_add_i32 m0, s63, 0\n\t"
        "s_mov_b32 s64, 524288\n\t"
        "buffer_load_dwordx4 v253, s[48:51], s64 offen lds\n\t"
        "s_add_i32 m0, s63, 1024\n\t"
        "s_mov_b32 s64, 532480\n\t"
        "buffer_load_dwordx4 v253, s[48:51], s64 offen lds\n\t"
        "s_add_i32 m0, s63, 2048\n\t"
        "s_mov_b32 s64, 540672\n\t"
        "buffer_load_dwordx4 v253, s[48:51], s64 offen lds\n\t"
        "s_add_i32 m0, s63, 3072\n\t"
        "s_mov_b32 s64, 548864\n\t"
        "buffer_load_dwordx4 v253, s[48:51], s64 offen lds\n\t"
        "s_waitcnt vmcnt(12)\n\t"
        "v_add_u32 v255, 4096, v254\n\t"
        "ds_read_b64_tr_b16 v[16:17], v255 offset:0\n\t"
        "ds_read_b64_tr_b16 v[18:19], v255 offset:256\n\t"
        "ds_read_b64_tr_b16 v[20:21], v255 offset:2048\n\t"
        "ds_read_b64_tr_b16 v[22:23], v255 offset:2304\n\t"
        "s_waitcnt lgkmcnt(0)\n\t"
        "v_add_u32 v255, 4128, v254\n\t"
        "ds_read_b64_tr_b16 v[24:25], v255 offset:0\n\t"
        "ds_read_b64_tr_b16 v[26:27], v255 offset:256\n\t"
        "ds_read_b64_tr_b16 v[28:29], v255 offset:2048\n\t"
        "ds_read_b64_tr_b16 v[30:31], v255 offset:2304\n\t"
        "s_waitcnt lgkmcnt(0)\n\t"
        "s_add_i32 m0, s63, 4096\n\t"
        "s_mov_b32 s64, 655360\n\t"
        "buffer_load_dwordx4 v253, s[48:51], s64 offen lds\n\t"
        "s_add_i32 m0, s63, 5120\n\t"
        "s_mov_b32 s64, 663552\n\t"
        "buffer_load_dwordx4 v253, s[48:51], s64 offen lds\n\t"
        "s_add_i32 m0, s63, 6144\n\t"
        "s_mov_b32 s64, 671744\n\t"
        "buffer_load_dwordx4 v253, s[48:51], s64 offen lds\n\t"
        "s_add_i32 m0, s63, 7168\n\t"
        "s_mov_b32 s64, 679936\n\t"
        "buffer_load_dwordx4 v253, s[48:51], s64 offen lds\n\t"
        "s_waitcnt vmcnt(12)\n\t"
        "v_add_u32 v255, 8192, v254\n\t"
        "ds_read_b64_tr_b16 v[32:33], v255 offset:0\n\t"
        "ds_read_b64_tr_b16 v[34:35], v255 offset:256\n\t"
        "ds_read_b64_tr_b16 v[36:37], v255 offset:2048\n\t"
        "ds_read_b64_tr_b16 v[38:39], v255 offset:2304\n\t"
        "s_waitcnt lgkmcnt(0)\n\t"
        "v_add_u32 v255, 8224, v254\n\t"
        "ds_read_b64_tr_b16 v[40:41], v255 offset:0\n\t"
        "ds_read_b64_tr_b16 v[42:43], v255 offset:256\n\t"
        "ds_read_b64_tr_b16 v[44:45], v255 offset:2048\n\t"
        "ds_read_b64_tr_b16 v[46:47], v255 offset:2304\n\t"
        "s_waitcnt lgkmcnt(0)\n\t"
        "s_add_i32 m0, s63, 8192\n\t"
        "s_mov_b32 s64, 786432\n\t"
        "buffer_load_dwordx4 v253, s[48:51], s64 offen lds\n\t"
        "s_add_i32 m0, s63, 9216\n\t"
        "s_mov_b32 s64, 794624\n\t"
        "buffer_load_dwordx4 v253, s[48:51], s64 offen lds\n\t"
        "s_add_i32 m0, s63, 10240\n\t"
        "s_mov_b32 s64, 802816\n\t"
        "buffer_load_dwordx4 v253, s[48:51], s64 offen lds\n\t"
        "s_add_i32 m0, s63, 11264\n\t"
        "s_mov_b32 s64, 811008\n\t"
        "buffer_load_dwordx4 v253, s[48:51], s64 offen lds\n\t"
        "s_waitcnt vmcnt(12)\n\t"
        "v_add_u32 v255, 12288, v254\n\t"
        "ds_read_b64_tr_b16 v[48:49], v255 offset:0\n\t"
        "ds_read_b64_tr_b16 v[50:51], v255 offset:256\n\t"
        "ds_read_b64_tr_b16 v[52:53], v255 offset:2048\n\t"
        "ds_read_b64_tr_b16 v[54:55], v255 offset:2304\n\t"
        "s_waitcnt lgkmcnt(0)\n\t"
        "v_add_u32 v255, 12320, v254\n\t"
        "ds_read_b64_tr_b16 v[56:57], v255 offset:0\n\t"
        "ds_read_b64_tr_b16 v[58:59], v255 offset:256\n\t"
        "ds_read_b64_tr_b16 v[60:61], v255 offset:2048\n\t"
        "ds_read_b64_tr_b16 v[62:63], v255 offset:2304\n\t"
        "s_waitcnt lgkmcnt(0)\n\t"
        "s_add_i32 m0, s63, 12288\n\t"
        "s_mov_b32 s64, 917504\n\t"
        "buffer_load_dwordx4 v253, s[48:51], s64 offen lds\n\t"
        "s_add_i32 m0, s63, 13312\n\t"
        "s_mov_b32 s64, 925696\n\t"
        "buffer_load_dwordx4 v253, s[48:51], s64 offen lds\n\t"
        "s_add_i32 m0, s63, 14336\n\t"
        "s_mov_b32 s64, 933888\n\t"
        "buffer_load_dwordx4 v253, s[48:51], s64 offen lds\n\t"
        "s_add_i32 m0, s63, 15360\n\t"
        "s_mov_b32 s64, 942080\n\t"
        "buffer_load_dwordx4 v253, s[48:51], s64 offen lds\n\t"
        "s_waitcnt vmcnt(12)\n\t"
        "v_add_u32 v255, 0, v254\n\t"
        "ds_read_b64_tr_b16 v[64:65], v255 offset:0\n\t"
        "ds_read_b64_tr_b16 v[66:67], v255 offset:256\n\t"
        "ds_read_b64_tr_b16 v[68:69], v255 offset:2048\n\t"
        "ds_read_b64_tr_b16 v[70:71], v255 offset:2304\n\t"
        "s_waitcnt lgkmcnt(0)\n\t"
        "v_add_u32 v255, 32, v254\n\t"
        "ds_read_b64_tr_b16 v[72:73], v255 offset:0\n\t"
        "ds_read_b64_tr_b16 v[74:75], v255 offset:256\n\t"
        "ds_read_b64_tr_b16 v[76:77], v255 offset:2048\n\t"
        "ds_read_b64_tr_b16 v[78:79], v255 offset:2304\n\t"
        "s_waitcnt lgkmcnt(0)\n\t"
        "s_waitcnt vmcnt(8)\n\t"
        "v_add_u32 v255, 4096, v254\n\t"
        "ds_read_b64_tr_b16 v[80:81], v255 offset:0\n\t"
        "ds_read_b64_tr_b16 v[82:83], v255 offset:256\n\t"
        "ds_read_b64_tr_b16 v[84:85], v255 offset:2048\n\t"
        "ds_read_b64_tr_b16 v[86:87], v255 offset:2304\n\t"
        "s_waitcnt lgkmcnt(0)\n\t"
        "v_add_u32 v255, 4128, v254\n\t"
        "ds_read_b64_tr_b16 v[88:89], v255 offset:0\n\t"
        "ds_read_b64_tr_b16 v[90:91], v255 offset:256\n\t"
        "ds_read_b64_tr_b16 v[92:93], v255 offset:2048\n\t"
        "ds_read_b64_tr_b16 v[94:95], v255 offset:2304\n\t"
        "s_waitcnt lgkmcnt(0)\n\t"
        "s_waitcnt vmcnt(4)\n\t"
        "v_add_u32 v255, 8192, v254\n\t"
        "ds_read_b64_tr_b16 v[96:97], v255 offset:0\n\t"
        "ds_read_b64_tr_b16 v[98:99], v255 offset:256\n\t"
        "ds_read_b64_tr_b16 v[100:101], v255 offset:2048\n\t"
        "ds_read_b64_tr_b16 v[102:103], v255 offset:2304\n\t"
        "s_waitcnt lgkmcnt(0)\n\t"
        "v_add_u32 v255, 8224, v254\n\t"
        "ds_read_b64_tr_b16 v[104:105], v255 offset:0\n\t"
        "ds_read_b64_tr_b16 v[106:107], v255 offset:256\n\t"
        "ds_read_b64_tr_b16 v[108:109], v255 offset:2048\n\t"
        "ds_read_b64_tr_b16 v[110:111], v255 offset:2304\n\t"
        "s_waitcnt lgkmcnt(0)\n\t"
        "s_waitcnt vmcnt(0)\n\t"
        "v_add_u32 v255, 12288, v254\n\t"
        "ds_read_b64_tr_b16 v[112:113], v255 offset:0\n\t"
        "ds_read_b64_tr_b16 v[114:115], v255 offset:256\n\t"
        "ds_read_b64_tr_b16 v[116:117], v255 offset:2048\n\t"
        "ds_read_b64_tr_b16 v[118:119], v255 offset:2304\n\t"
        "s_waitcnt lgkmcnt(0)\n\t"
        "v_add_u32 v255, 12320, v254\n\t"
        "ds_read_b64_tr_b16 v[120:121], v255 offset:0\n\t"
        "ds_read_b64_tr_b16 v[122:123], v255 offset:256\n\t"
        "ds_read_b64_tr_b16 v[124:125], v255 offset:2048\n\t"
        "ds_read_b64_tr_b16 v[126:127], v255 offset:2304\n\t"
        "s_waitcnt lgkmcnt(0)\n\t"
        "v_lshlrev_b32 v248, 4, v252\n\t"
        "v_add_u32 v249, s65, v248\n\t"
        "v_bfe_u32 v255, v252, 5, 1\n\t"
        "v_lshl_add_u32 v246, v255, 9, s65\n\t"
        "v_bfe_u32 v255, v252, 3, 1\n\t"
        "v_lshl_add_u32 v246, v255, 8, v246\n\t"
        "v_bfe_u32 v255, v252, 2, 1\n\t"
        "v_lshl_add_u32 v246, v255, 7, v246\n\t"
        "v_bfe_u32 v255, v252, 4, 1\n\t"
        "v_lshl_add_u32 v246, v255, 6, v246\n\t"
        "v_bfe_u32 v255, v252, 0, 2\n\t"
        "v_lshl_add_u32 v246, v255, 4, v246\n\t"
        "s_lshr_b32 s58, s52, 2\n\t"
        "s_lshl_b32 s58, s58, 13\n\t"
        "v_add_u32 v245, s58, v248\n\t"
        "v_bfe_u32 v240, v252, 3, 1\n\t"
        "v_mul_u32_u24 v240, 0xaaaa, v240\n\t"
        "v_add_u32 v240, 0x4444, v240\n\t"
        "v_bfe_u32 v255, v252, 0, 5\n\t"
        "v_lshl_add_u32 v251, v255, 3, 0\n\t"
        "v_bfe_u32 v255, v252, 5, 1\n\t"
        "v_lshl_add_u32 v251, v255, 2, v251\n\t"
        "v_bfe_u32 v255, v252, 2, 1\n\t"
        "v_lshl_add_u32 v244, v255, 9, 0\n\t"
        "v_bfe_u32 v255, v252, 3, 1\n\t"
        "v_lshl_add_u32 v244, v255, 8, v244\n\t"
        "v_bfe_u32 v255, v252, 4, 2\n\t"
        "v_lshl_add_u32 v244, v255, 6, v244\n\t"
        "v_bfe_u32 v255, v252, 0, 2\n\t"
        "v_lshl_add_u32 v244, v255, 4, v244\n\t"
        "v_bfe_u32 v255, v252, 0, 2\n\t"
        "v_lshl_add_u32 v242, v255, 7, 0\n\t"
        "s_lshl_b32 s58, s52, 9\n\t"
        "s_add_i32 s58, s58, 16384\n\t"
        "v_bfe_u32 v255, v252, 5, 1\n\t"
        "v_lshl_add_u32 v253, v255, 8, s58\n\t"
        "v_bfe_u32 v255, v252, 0, 4\n\t"
        "v_lshl_add_u32 v253, v255, 4, v253\n\t"
        "v_bfe_u32 v255, v252, 4, 1\n\t"
        "v_lshl_add_u32 v253, v255, 3, v253\n\t"
        "s_and_b32 s58, s52, 1\n\t"
        "s_lshl_b32 s59, s58, 8\n\t"
        "s_add_i32 s59, s59, 16384\n\t"
        "v_bfe_u32 v255, v252, 1, 2\n\t"
        "v_lshl_add_u32 v254, v255, 6, s59\n\t"
        "v_bfe_u32 v255, v252, 5, 1\n\t"
        "v_lshl_add_u32 v254, v255, 5, v254\n\t"
        "v_bfe_u32 v255, v252, 0, 1\n\t"
        "v_lshl_add_u32 v254, v255, 4, v254\n\t"
        "v_bfe_u32 v255, v252, 3, 2\n\t"
        "v_lshl_add_u32 v254, v255, 2, v254\n\t"
        "s_lshl_b32 s58, s58, 7\n\t"
        "v_bfe_u32 v255, v252, 1, 5\n\t"
        "v_lshl_add_u32 v250, v255, 2, s58\n\t"
        "v_bfe_u32 v255, v252, 0, 1\n\t"
        "v_lshl_add_u32 v250, v255, 30, v250\n\t"
        "s_lshl_b32 s58, s92, 2\n\t"
        "s_lshr_b32 s59, s52, 1\n\t"
        "s_add_i32 s58, s58, s59\n\t"
        "s_lshl_b32 s69, s58, 14\n\t"
        "s_and_b32 s59, s52, 1\n\t"
        "s_lshl_b32 s59, s59, 12\n\t"
        "s_add_i32 s69, s69, s59\n\t"
        "s_add_i32 s69, s69, 131072\n\t"
        "s_add_i32 s69, s69, s54\n\t"
        "s_lshl_b32 s68, s52, 9\n\t"
        "s_add_i32 s68, s68, 1703936\n\t"
        "s_add_i32 s68, s68, s54\n\t"
        "s_mov_b32 s96, 0\n\t"
        "s_mov_b32 s73, 0\n\t"
        "vt_%=:\n\t"
        "s_mov_b32 s58, %[vote]\n\t"
        "buffer_load_dword v192, off, s[40:43], s58 sc1\n\t"
        "s_waitcnt vmcnt(0)\n\t"
        "v_readfirstlane_b32 s58, v192\n\t"
        "s_nop 3\n\t"
        "s_and_b32 s59, s58, 0xffff\n\t"
        "s_cmp_ge_u32 s59, 96\n\t"
        "s_cbranch_scc1 vk_%=\n\t"
        "s_sleep 4\n\t"
        "s_sub_i32 s56, s56, 1\n\t"
        "s_cmp_gt_i32 s56, 0\n\t"
        "s_cbranch_scc1 vt_%=\n\t"
        "vk_%=:\n\t"
        "s_lshr_b32 s58, s58, 16\n\t"
        "s_cmp_gt_u32 s58, 48\n\t"
        "s_cselect_b32 s58, 8192, 0\n\t"
        "s_add_i32 s54, s54, s58\n\t"
        "s_add_i32 s69, s69, s58\n\t"
        "s_add_i32 s68, s68, s58\n\t"
        "L_%=:\n\t"
        "v_mov_b32 v224, 0\n\t"
        "v_mov_b32 v225, 0\n\t"
        "v_mov_b32 v226, 0\n\t"
        "v_mov_b32 v227, 0\n\t"
        "s_mov_b32 s73, 0\n\t"
        "s_sleep %[slv]\n\t"
        "s_and_b32 s58, s57, 3\n\t"
        "s_lshl_b32 s58, s58, 15\n\t"
        "s_add_i32 s58, s58, s54\n\t"
        "s_add_i32 s59, s58, 16384\n\t"
        "g_%=:\n\t"
        "buffer_load_dwordx4 v[192:195], v249, s[40:43], s58 offen sc1\n\t"
        "buffer_load_dwordx4 v[196:199], v249, s[40:43], s59 offen sc1\n\t"
        "s_waitcnt vmcnt(1)\n\t"
        "v_max3_u32 v200, v192, v193, v194\n\t"
        "s_waitcnt vmcnt(0)\n\t"
        "v_max3_u32 v200, v200, v195, v196\n\t"
        "v_max3_u32 v200, v200, v197, v198\n\t"
        "v_max_u32 v200, v200, v199\n\t"
        "v_cmp_eq_u32 vcc, -1, v200\n\t"
        "s_cbranch_vccz gk_%=\n\t"
        "s_add_i32 s73, s73, 0x10000\n\t"
        "s_sub_i32 s56, s56, 1\n\t"
        "s_cmp_gt_i32 s56, 0\n\t"
        "s_cbranch_scc1 g_%=\n\t"
        "gk_%=:\n\t"
        "ds_write_b128 v246, v[192:195]\n\t"
        "ds_write_b128 v246, v[196:199] offset:8192\n\t"
        "s_cmp_eq_u32 s57, 63\n\t"
        "s_waitcnt lgkmcnt(0)\n\t"
        "s_barrier\n\t"
        "ds_read_b128 v[192:195], v245 offset:0\n\t"
        "ds_read_b128 v[196:199], v245 offset:1024\n\t"
        "ds_read_b128 v[200:203], v245 offset:2048\n\t"
        "ds_read_b128 v[204:207], v245 offset:3072\n\t"
        "ds_read_b128 v[208:211], v245 offset:4096\n\t"
        "ds_read_b128 v[212:215], v245 offset:5120\n\t"
        "ds_read_b128 v[216:219], v245 offset:6144\n\t"
        "ds_read_b128 v[220:223], v245 offset:7168\n\t"
        "s_lshl_b32 s58, s57, 17\n\t"
        "s_add_i32 s58, s58, s67\n\t"
        "s_and_b32 s59, s57, 3\n\t"
        "s_lshl_b32 s59, s59, 18\n\t"
        "s_add_i32 s59, s59, s66\n\t"
        "s_add_i32 s59, s59, s54\n\t"
        "s_waitcnt lgkmcnt(7)\n\t"
        "v_smfmac_f32_16x16x64_bf16 v[224:227], v[192:195], v[128:135], v240\n\t"
        "s_waitcnt lgkmcnt(6)\n\t"
        "v_smfmac_f32_16x16x64_bf16 v[224:227], v[196:199], v[136:143], v240\n\t"
        "s_waitcnt lgkmcnt(5)\n\t"
        "v_smfmac_f32_16x16x64_bf16 v[224:227], v[200:203], v[144:151], v240\n\t"
        "s_waitcnt lgkmcnt(4)\n\t"
        "v_smfmac_f32_16x16x64_bf16 v[224:227], v[204:207], v[152:159], v240\n\t"
        "s_waitcnt lgkmcnt(3)\n\t"
        "v_smfmac_f32_16x16x64_bf16 v[224:227], v[208:211], v[160:167], v240\n\t"
        "s_waitcnt lgkmcnt(2)\n\t"
        "v_smfmac_f32_16x16x64_bf16 v[224:227], v[212:215], v[168:175], v240\n\t"
        "s_waitcnt lgkmcnt(1)\n\t"
        "v_smfmac_f32_16x16x64_bf16 v[224:227], v[216:219], v[176:183], v240\n\t"
        "s_waitcnt lgkmcnt(0)\n\t"
        "v_smfmac_f32_16x16x64_bf16 v[224:227], v[220:223], v[184:191], v240\n\t"
        "s_nop 7\n\t"
        "v_permlane32_swap_b32 v224, v226\n\t"
        "v_permlane32_swap_b32 v225, v227\n\t"
        "v_pk_add_f32 v[224:225], v[224:225], v[226:227]\n\t"
        "v_cvt_pk_bf16_f32 v255, v224, v225\n\t"
        "v_min_u32 v255, -2, v255\n\t"
        "buffer_store_dword v255, v251, s[40:43], s59 offen sc1\n\t"
        "buffer_store_dword v255, v251, s[40:43], s58 offen sc1\n\t"
        "s_cmp_eq_u32 s57, 63\n\t"
        "s_cbranch_scc1 END_%=\n\t"
        "v_mov_b32 v247, -1\n\t"
        "s_add_i32 s59, s57, 2\n\t"
        "s_and_b32 s61, s59, 3\n\t"
        "s_lshl_b32 s59, s61, 18\n\t"
        "s_add_i32 s59, s59, s66\n\t"
        "s_add_i32 s59, s59, s54\n\t"
        "buffer_store_dword v247, v251, s[40:43], s59 offen sc1\n\t"
        "s_cmp_lg_u32 s52, 7\n\t"
        "s_cbranch_scc1 na_%=\n\t"
        "s_lshl_b32 s61, s61, 15\n\t"
        "s_add_i32 s61, s61, s72\n\t"
        "s_add_i32 s61, s61, s54\n\t"
        "buffer_store_dword v247, v251, s[40:43], s61 offen sc1\n\t"
        "na_%=:\n\t"
        "s_cmp_lg_u32 s52, 2\n\t"
        "s_cbranch_scc1 npg_%=\n\t"
        "v_mov_b32 v196, s57\n\t"
        "buffer_store_dword v196, off, s[40:43], s93 sc1\n\t"
        "npg_%=:\n\t"
        "v_mov_b32 v224, 0\n\t"
        "v_mov_b32 v225, 0\n\t"
        "v_mov_b32 v226, 0\n\t"
        "v_mov_b32 v227, 0\n\t"
        "v_mov_b32 v228, 0\n\t"
        "v_mov_b32 v229, 0\n\t"
        "v_mov_b32 v230, 0\n\t"
        "v_mov_b32 v231, 0\n\t"
        "v_mov_b32 v232, 0\n\t"
        "v_mov_b32 v233, 0\n\t"
        "v_mov_b32 v234, 0\n\t"
        "v_mov_b32 v235, 0\n\t"
        "v_mov_b32 v236, 0\n\t"
        "v_mov_b32 v237, 0\n\t"
        "v_mov_b32 v238, 0\n\t"
        "v_mov_b32 v239, 0\n\t"
        "v_mov_b32 v241, 0\n\t"
        "s_and_b32 s58, s57, 3\n\t"
        "s_lshl_b32 s58, s58, 17\n\t"
        "s_add_i32 s58, s58, s69\n\t"
        "s_add_i32 s60, s57, 1\n\t"
        "s_and_b32 s60, s60, 3\n\t"
        "s_lshl_b32 s60, s60, 15\n\t"
        "s_add_i32 s60, s60, s72\n\t"
        "s_add_i32 s60, s60, s54\n\t"
        "F_%=:\n\t"
        "buffer_load_dwordx2 v[192:193], v242, s[40:43], s68 offen sc1\n\t"
        "s_waitcnt vmcnt(0)\n\t"
        "v_cmp_ge_i32 vcc, s57, v192\n\t"
        "s_cbranch_vccz Fk_%=\n\t"
        "s_sub_i32 s56, s56, 1\n\t"
        "s_cmp_gt_i32 s56, 0\n\t"
        "s_cbranch_scc1 F_%=\n\t"
        "Fk_%=:\n\t"
        "v_readlane_b32 s61, v193, 0\n\t"
        "v_readlane_b32 s62, v193, 1\n\t"
        "v_readlane_b32 s63, v193, 2\n\t"
        "v_readlane_b32 s64, v193, 3\n\t"
        "s_memrealtime s[74:75]\n\t"
        "s_waitcnt lgkmcnt(0)\n\t"
        "s_sub_i32 s61, s61, s74\n\t"
        "s_sub_i32 s62, s62, s74\n\t"
        "s_sub_i32 s63, s63, s74\n\t"
        "s_sub_i32 s64, s64, s74\n\t"
        "s_max_i32 s61, s61, s62\n\t"
        "s_max_i32 s63, s63, s64\n\t"
        "s_max_i32 s61, s61, s63\n\t"
        "s_add_i32 s61, s61, %[slp]\n\t"
        "s_min_i32 s61, s61, 150\n\t"
        "s_add_i32 s61, s61, s74\n\t"
        "Tw_%=:\n\t"
        "s_memrealtime s[74:75]\n\t"
        "s_waitcnt lgkmcnt(0)\n\t"
        "s_sub_i32 s62, s61, s74\n\t"
        "s_cmp_gt_i32 s62, 0\n\t"
        "s_cbranch_scc1 Tw_%=\n\t"
        "buffer_load_dwordx4 v[192:195], v244, s[40:43], s58 offen offset:0 sc1\n\t"
        "buffer_load_dwordx4 v[196:199], v244, s[40:43], s58 offen offset:1024 sc1\n\t"
        "buffer_load_dwordx4 v[200:203], v244, s[40:43], s58 offen offset:2048 sc1\n\t"
        "buffer_load_dwordx4 v[204:207], v244, s[40:43], s58 offen offset:3072 sc1\n\t"
        "s_waitcnt vmcnt(3)\n\t"
        "v_smfmac_f32_16x16x64_bf16 v[224:227], v[192:195], v[0:7], v240\n\t"
        "v_smfmac_f32_16x16x64_bf16 v[228:231], v[192:195], v[8:15], v240\n\t"
        "v_smfmac_f32_16x16x64_bf16 v[232:235], v[192:195], v[16:23], v240\n\t"
        "v_smfmac_f32_16x16x64_bf16 v[236:239], v[192:195], v[24:31], v240\n\t"
        "v_max3_u32 v241, v241, v192, v193\n\t"
        "v_max3_u32 v241, v241, v194, v195\n\t"
        "s_waitcnt vmcnt(2)\n\t"
        "v_smfmac_f32_16x16x64_bf16 v[224:227], v[196:199], v[32:39], v240\n\t"
        "v_smfmac_f32_16x16x64_bf16 v[228:231], v[196:199], v[40:47], v240\n\t"
        "v_smfmac_f32_16x16x64_bf16 v[232:235], v[196:199], v[48:55], v240\n\t"
        "v_smfmac_f32_16x16x64_bf16 v[236:239], v[196:199], v[56:63], v240\n\t"
        "v_max3_u32 v241, v241, v196, v197\n\t"
        "v_max3_u32 v241, v241, v198, v199\n\t"
        "s_waitcnt vmcnt(1)\n\t"
        "v_smfmac_f32_16x16x64_bf16 v[224:227], v[200:203], v[64:71], v240\n\t"
        "v_smfmac_f32_16x16x64_bf16 v[228:231], v[200:203], v[72:79], v240\n\t"
        "v_smfmac_f32_16x16x64_bf16 v[232:235], v[200:203], v[80:87], v240\n\t"
        "v_smfmac_f32_16x16x64_bf16 v[236:239], v[200:203], v[88:95], v240\n\t"
        "v_max3_u32 v241, v241, v200, v201\n\t"
        "v_max3_u32 v241, v241, v202, v203\n\t"
        "s_waitcnt vmcnt(0)\n\t"
        "v_smfmac_f32_16x16x64_bf16 v[224:227], v[204:207], v[96:103], v240\n\t"
        "v_smfmac_f32_16x16x64_bf16 v[228:231], v[204:207], v[104:111], v240\n\t"
        "v_smfmac_f32_16x16x64_bf16 v[232:235], v[204:207], v[112:119], v240\n\t"
        "v_smfmac_f32_16x16x64_bf16 v[236:239], v[204:207], v[120:127], v240\n\t"
        "v_max3_u32 v241, v241, v204, v205\n\t"
        "v_max3_u32 v241, v241, v206, v207\n\t"
        "v_cmp_eq_u32 vcc, -1, v241\n\t"
        "s_cbranch_vccnz R_%=\n\t"
        "s_nop 3\n\t"
        "G_%=:\n\t"
        "v_mov_b32_dpp v224, v232 quad_perm:[0,1,2,3] row_mask:0xa bank_mask:0xf\n\t"
        "v_mov_b32_dpp v225, v233 quad_perm:[0,1,2,3] row_mask:0xa bank_mask:0xf\n\t"
        "v_mov_b32_dpp v226, v234 quad_perm:[0,1,2,3] row_mask:0xa bank_mask:0xf\n\t"
        "v_mov_b32_dpp v227, v235 quad_perm:[0,1,2,3] row_mask:0xa bank_mask:0xf\n\t"
        "v_mov_b32_dpp v228, v236 quad_perm:[0,1,2,3] row_mask:0xa bank_mask:0xf\n\t"
        "v_mov_b32_dpp v229, v237 quad_perm:[0,1,2,3] row_mask:0xa bank_mask:0xf\n\t"
        "v_mov_b32_dpp v230, v238 quad_perm:[0,1,2,3] row_mask:0xa bank_mask:0xf\n\t"
        "v_mov_b32_dpp v231, v239 quad_perm:[0,1,2,3] row_mask:0xa bank_mask:0xf\n\t"
        "v_permlane32_swap_b32 v224, v228\n\t"
        "v_permlane32_swap_b32 v225, v229\n\t"
        "v_permlane32_swap_b32 v226, v230\n\t"
        "v_permlane32_swap_b32 v227, v231\n\t"
        "v_pk_add_f32 v[224:225], v[224:225], v[228:229]\n\t"
        "v_pk_add_f32 v[226:227], v[226:227], v[230:231]\n\t"
        "s_cmp_ge_u32 s52, 2\n\t"
        "s_nop 0\n\t"
        "v_permlane16_swap_b32 v224, v226\n\t"
        "v_permlane16_swap_b32 v225, v227\n\t"
        "v_pk_add_f32 v[224:225], v[224:225], v[226:227]\n\t"
        "ds_write_b64 v253, v[224:225]\n\t"
        "s_waitcnt lgkmcnt(0)\n\t"
        "s_barrier\n\t"
        "s_cbranch_scc1 nr_%=\n\t"
        "ds_read_b32 v192, v254 offset:0\n\t"
        "ds_read_b32 v193, v254 offset:512\n\t"
        "ds_read_b32 v194, v254 offset:1024\n\t"
        "ds_read_b32 v195, v254 offset:1536\n\t"
        "ds_read_b32 v196, v254 offset:2048\n\t"
        "ds_read_b32 v197, v254 offset:2560\n\t"
        "ds_read_b32 v198, v254 offset:3072\n\t"
        "ds_read_b32 v199, v254 offset:3584\n\t"
        "s_waitcnt lgkmcnt(4)\n\t"
        "v_add_f32 v192, v192, v193\n\t"
        "v_add_f32 v194, v194, v195\n\t"
        "v_add_f32 v192, v192, v194\n\t"
        "s_waitcnt lgkmcnt(0)\n\t"
        "v_add_f32 v196, v196, v197\n\t"
        "v_add_f32 v198, v198, v199\n\t"
        "v_add_f32 v192, v192, v196\n\t"
        "v_add_f32 v192, v192, v198\n\t"
        "s_nop 1\n\t"
        "v_mov_b32_dpp v193, v192 quad_perm:[1,0,3,2] row_mask:0xf bank_mask:0xf\n\t"
        "v_cvt_pk_bf16_f32 v192, v192, v193\n\t"
        "v_min_u32 v192, -2, v192\n\t"
        "buffer_store_dword v192, v250, s[40:43], s60 offen sc1\n\t"
        "s_add_i32 s57, s57, 1\n\t"
        "s_branch L_%=\n\t"
        "nr_%=:\n\t"
        "s_sleep 3\n\t"
        "s_add_i32 s57, s57, 1\n\t"
        "s_branch L_%=\n\t"
        "R_%=:\n\t"
        "s_add_i32 s73, s73, 1\n\t"
        "s_mov_b32 s62, 0\n\t"
        "v_max3_u32 v241, v192, v193, v194\n\t"
        "v_max_u32 v241, v241, v195\n\t"
        "v_cmp_eq_u32 vcc, -1, v241\n\t"
        "s_cbranch_vccz Q0_%=\n\t"
        "buffer_load_dwordx4 v[192:195], v244, s[40:43], s58 offen offset:0 sc1\n\t"
        "s_mov_b32 s62, 1\n\t"
        "Q0_%=:\n\t"
        "v_max3_u32 v241, v196, v197, v198\n\t"
        "v_max_u32 v241, v241, v199\n\t"
        "v_cmp_eq_u32 vcc, -1, v241\n\t"
        "s_cbranch_vccz Q1_%=\n\t"
        "buffer_load_dwordx4 v[196:199], v244, s[40:43], s58 offen offset:1024 sc1\n\t"
        "s_mov_b32 s62, 1\n\t"
        "Q1_%=:\n\t"
        "v_max3_u32 v241, v200, v201, v202\n\t"
        "v_max_u32 v241, v241, v203\n\t"
        "v_cmp_eq_u32 vcc, -1, v241\n\t"
        "s_cbranch_vccz Q2_%=\n\t"
        "buffer_load_dwordx4 v[200:203], v244, s[40:43], s58 offen offset:2048 sc1\n\t"
        "s_mov_b32 s62, 1\n\t"
        "Q2_%=:\n\t"
        "v_max3_u32 v241, v204, v205, v206\n\t"
        "v_max_u32 v241, v241, v207\n\t"
        "v_cmp_eq_u32 vcc, -1, v241\n\t"
        "s_cbranch_vccz Q3_%=\n\t"
        "buffer_load_dwordx4 v[204:207], v244, s[40:43], s58 offen offset:3072 sc1\n\t"
        "s_mov_b32 s62, 1\n\t"
        "Q3_%=:\n\t"
        "s_waitcnt vmcnt(0)\n\t"
        "s_sub_i32 s56, s56, 1\n\t"
        "s_cmp_gt_i32 s56, 0\n\t"
        "s_cselect_b32 s62, s62, 0\n\t"
        "s_cmp_lg_u32 s62, 0\n\t"
        "s_cbranch_scc1 R_%=\n\t"
        "v_mov_b32 v224, 0\n\t"
        "v_mov_b32 v225, 0\n\t"
        "v_mov_b32 v226, 0\n\t"
        "v_mov_b32 v227, 0\n\t"
        "v_mov_b32 v228, 0\n\t"
        "v_mov_b32 v229, 0\n\t"
        "v_mov_b32 v230, 0\n\t"
        "v_mov_b32 v231, 0\n\t"
        "v_mov_b32 v232, 0\n\t"
        "v_mov_b32 v233, 0\n\t"
        "v_mov_b32 v234, 0\n\t"
        "v_mov_b32 v235, 0\n\t"
        "v_mov_b32 v236, 0\n\t"
        "v_mov_b32 v237, 0\n\t"
        "v_mov_b32 v238, 0\n\t"
        "v_mov_b32 v239, 0\n\t"
        "s_nop 1\n\t"
        "v_smfmac_f32_16x16x64_bf16 v[224:227], v[192:195], v[0:7], v240\n\t"
        "v_smfmac_f32_16x16x64_bf16 v[228:231], v[192:195], v[8:15], v240\n\t"
        "v_smfmac_f32_16x16x64_bf16 v[232:235], v[192:195], v[16:23], v240\n\t"
        "v_smfmac_f32_16x16x64_bf16 v[236:239], v[192:195], v[24:31], v240\n\t"
        "v_smfmac_f32_16x16x64_bf16 v[224:227], v[196:199], v[32:39], v240\n\t"
        "v_smfmac_f32_16x16x64_bf16 v[228:231], v[196:199], v[40:47], v240\n\t"
        "v_smfmac_f32_16x16x64_bf16 v[232:235], v[196:199], v[48:55], v240\n\t"
        "v_smfmac_f32_16x16x64_bf16 v[236:239], v[196:199], v[56:63], v240\n\t"
        "v_smfmac_f32_16x16x64_bf16 v[224:227], v[200:203], v[64:71], v240\n\t"
        "v_smfmac_f32_16x16x64_bf16 v[228:231], v[200:203], v[72:79], v240\n\t"
        "v_smfmac_f32_16x16x64_bf16 v[232:235], v[200:203], v[80:87], v240\n\t"
        "v_smfmac_f32_16x16x64_bf16 v[236:239], v[200:203], v[88:95], v240\n\t"
        "v_smfmac_f32_16x16x64_bf16 v[224:227], v[204:207], v[96:103], v240\n\t"
        "v_smfmac_f32_16x16x64_bf16 v[228:231], v[204:207], v[104:111], v240\n\t"
        "v_smfmac_f32_16x16x64_bf16 v[232:235], v[204:207], v[112:119], v240\n\t"
        "v_smfmac_f32_16x16x64_bf16 v[236:239], v[204:207], v[120:127], v240\n\t"
        "s_nop 7\n\t"
        "s_branch G_%=\n\t"
        "END_%=:\n\t"
        "s_waitcnt vmcnt(0) lgkmcnt(0)\n\t"
        "s_barrier\n\t"
        "s_cmp_lg_u32 s52, 2\n\t"
        "s_cbranch_scc1 nq_%=\n\t"
        "v_mov_b32 v192, 64\n\t"
        "buffer_store_dword v192, off, s[40:43], s93 sc1\n\t"
        "nq_%=:\n\t"
        "v_mbcnt_lo_u32_b32 v250, -1, 0\n\t"
        "v_mbcnt_hi_u32_b32 v250, -1, v250\n\t"
        "s_lshl_b32 s58, s52, 8\n\t"
        "s_add_i32 s58, s58, 159744\n\t"
        "v_lshl_add_u32 v250, v250, 2, s58\n\t"
        "ds_read_b32 v199, v250\n\t"
        "s_waitcnt lgkmcnt(0)\n\t"
      :
      : [ws] "s"(ws), [wa] "s"(wa), [wb] "s"(wb), [wave] "s"(wave), [idx] "s"(idx), [rp] "s"(rp), [half] "s"(half),
        [wsn] "i"((int)WS_END), [bud] "i"(SPIN_CAP), [vb] "i"((int)WS_VB), [dbg] "i"((int)WS_DBG), [pg] "i"((int)C_PROG), [vote] "i"((int)C_VOTE), [slp] "i"(SLEEP_M), [slv] "i"(SLEEP_V), [stw] "i"(STW_M)
      : "memory", "vcc", "scc", CHAIN_CLOBBERS);
}

constexpr int NGRP = 7, TL_LEN = 1519, CHAIN_SPLIT = 1;
__device__ const __attribute__((aligned(8))) unsigned g_tasks[7][3038] = {
  {0x20010,0x1,0x20024,0x1,0x20015,0x1,0x20029,0x1,0x2001a,0x1,0x2002e,0x1,0x2001f,0x1,0x20020,0x1,0x20011,0x1,0x20021,0x1,0x20012,0x1,0x20022,0x1,0x20013,0x1,0x20023,0x1,0x20014,0x1,0x20025,0x1,0x20016,0x1,0x20026,0x1,0x20017,0x1,0x20027,0x1,0x20018,0x1,0x20028,0x1,0x20019,0x1,0x2002a,0x1,0x2001b,0x1,0x2002b,0x1,0x2001c,0x1,0x2002c,0x1,0x2001d,0x1,0x2002d,0x1,0x2001e,0x1,0x2002f,0x1,0x220010,0x1,0x220024,0x1,0x220015,0x1,0x220029,0x1,0x22001a,0x1,0x22002e,0x1,0x22001f,0x1,0x220020,0x1,0x220011,0x1,0x220025,0x1,0x220016,0x1,0x22002a,0x1,0x22001b,0x1,0x22002f,0x1,0x320010,0x1,0x320024,0x1,0x320015,0x1,0x320029,0x1,0x32001a,0x1,0x32002e,0x1,0x32001f,0x1,0x320020,0x1,0x320011,0x1,0x320025,0x1,0x320016,0x1,0x32002a,0x1,0x32001b,0x1,0x32002f,0x1,0x20000,0x1,0x20008,0x1,0x420010,0x1,0x420024,0x1,0x420015,0x1,0x420029,0x1,0x42001a,0x1,0x42002e,0x1,0x42001f,0x1,0x120000,0x1,0x120008,0x1,0x520010,0x1,0x520024,0x1,0x520015,0x1,0x520029,0x1,0x52001a,0x1,0x52002e,0x1,0x52001f,0x1,0x220000,0x1,0x220008,0x1,0x620010,0x1,0x620024,0x1,0x620015,0x1,0x620029,0x1,0x62001a,0x1,0x62002e,0x1,0x62001f,0x1,0x10000000,0x10108,0x10000008,0x10108,0x10000010,0x10108,0x10000018,0x10108,0x10000020,0x10208,0x10000028,0x10208,0x10000030,0x10208,0x10000038,0x10208,0x10000040,0x10308,0x20020201,0x2200501,0x20020603,0x4200501,0x320000,0x1,0x320008,0x1,0x720010,0x1,0x720024,0x1,0x20020000,0x1200501,0x20020402,0x3200501,0x720015,0x1,0x720029,0x1,0x72001a,0x1,0x72002e,0x1,0x72001f,0x1,0x10100000,0x10908,0x10100008,0x10908,0x10100010,0x10908,0x10100018,0x10908,0x10100020,0x10a08,0x10100028,0x10a08,0x10100030,0x10a08,0x10100038,0x10a08,0x10100040,0x10b08,0x10100048,0x10b08,0x10100050,0x10b08,0x10100058,0x10b08,0x10100060,0x10c08,0x10100068,0x10c08,0x10100070,0x10c08,0x10100078,0x10c08,0x420000,0x1,0x420008,0x1,0x820010,0x1,0x820024,0x1,0x820015,0x1,0x820029,0x1,0x82001a,0x1,0x20120201,0xa200d01,0x20120603,0xc200d01,0x82002e,0x1,0x82001f,0x1,0x10200010,0x11108,0x10200018,0x11108,0x10200020,0x11208,0x10200028,0x11208,0x10200030,0x11208,0x10200038,0x11208,0x10200040,0x11308,0x10200048,0x11308,0x10200050,0x11308,0x10200058,0x11308,0x10200060,0x11408,0x10200068,0x11408,0x10200070,0x11408,0x10200078,0x11408,0x20120000,0x9200d01,0x20120402,0xb200d01,0x520000,0x1,0x20220000,0x11201501,0x20220201,0x12201501,0x20220402,0x13201501,0x20220603,0x14201501,0x520008,0x1,0x920010,0x1,0x920024,0x1,0x920015,0x1,0x920029,0x1,0x92001a,0x1,0x92002e,0x1,0x92001f,0x1,0x620000,0x1,0x620008,0x1,0x30008008,0x5100608,0x30008010,0x5100608,0x30008018,0x5100608,0x30008020,0x5100608,0x30008028,0x5100608,0x30008030,0x5100608,0x30008038,0x5100608,0x40020004,0x6800701,0x40020009,0x6800701,0x10300008,0x11908,0x10300010,0x11908,0x10300018,0x11908,0x10300020,0x11a08,0x10300028,0x11a08,0x10300058,0x11b08,0x10300060,0x11c08,0x10300068,0x11c08,0x10300070,0x11c08,0xb20010,0x1,0xb20024,0x1,0xb20015,0x1,0xb20029,0x1,0xb2001a,0x1,0xb2002e,0x1,0xb2001f,0x1,0x20320000,0x19201d01,0x20320201,0x1a201d01,0x820000,0x1,0x820008,0x1,0xc20010,0x1,0x40020002,0x6800701,0x40020003,0x6800701,0x40020006,0x6800701,0x40020007,0x6800701,0x40020008,0x6800701,0x4002000b,0x6800701,0x4002000c,0x6800701,0x4002000d,0x6800701,0x30108048,0xd100e08,0x30108050,0xd100e08,0x30108058,0xd100e08,0x30108060,0xd100e08,0x30108068,0xd100e08,0x30108070,0xd100e08,0x30108078,0xd100e08,0x20320402,0x1b201d01,0x20320603,0x1c201d01,0x10400000,0x12108,0x40220004,0x16801701,0x40220009,0x16801701,0x40220001,0x16801701,0x40220002,0x16801701,0x40220005,0x16801701,0x40220006,0x16801701,0x40220007,0x16801701,0x20420000,0x21202501,0x20420201,0x22202501,0x10500058,0x12b08,0x10500060,0x12c08,0x10500068,0x12c08,0x10500070,0x12c08,0x10500078,0x12c08,0x5001c003,0x70e0801,0x5001c400,0x70e0801,0x30308028,0x1d101e08,0x30308030,0x1d101e08,0x30308038,0x1d101e08,0x30308060,0x1d101e08,0x30308068,0x1d101e08,0x30308070,0x1d101e08,0x20520000,0x29202d01,0x20520201,0x2a202d01,0x20520402,0x2b202d01,0x20520603,0x2c202d01,0x20520002,0x29202d01,0x20520003,0x29202d01,0x20520200,0x2a202d01,0x20520203,0x2a202d01,0x20520401,0x2b202d01,0x20520600,0x2c202d01,0x5011c003,0xf0e1001,0x5011c400,0xf0e1001,0x5011c601,0xf0e1001,0x5011c002,0xf0e1001,0x5011c200,0xf0e1001,0x70000005,0x0,0x70000005,0x0,0x70000005,0x0,0x70000005,0x0,0x70000005,0x0,0x70000005,0x0,0x70000005,0x0,0x70000005,0x0,0x70000005,0x0,0x70000005,0x0,0x70000005,0x0,0x70000005,0x0,0x70000005,0x0,0x70000005,0x0,0x70000005,0x0,0x70000005,0x0,0x70000005,0x0,0x70000005,0x0,0x70000005,0x0,0x70000005,0x0,0x70000005,0x0,0x70000005,0x0,0x70000005,0x0,0x70000005,0x0,0x70000005,0x0,0x70000005,0x0,0x70000005,0x0,0x70000005,0x0,0x70000005,0x0,0x70000005,0x0,0x70000005,0x0,0x70000005,0x0,0x70000005,0x0,0x70000005,0x0,0x70000005,0x0,0x70000005,0x0,0x70000005,0x0,0x70000005,0x0,0x70000005,0x0,0x70000005,0x0,0x70000005,0x0,0x70000005,0x0,0x70000005,0x0,0x70000005,0x0,0x70000005,0x0,0x70000005,0x0,0x70000005,0x0,0x70000005,0x0,0x70000005,0x0,0x70000005,0x0,0x70000005,0x0,0x70000005,0x0,0x70000005,0x0,0x70000005,0x0,0x70000005,0x0,0x70000005,0x0,0x70000005,0x0,0x70000005,0x0,0x70000005,0x0,0x70000005,0x0,0x70000005,0x0,0x70000005,0x0,0x70000005,0x0,0x70000005,0x0,0x70000005,0x0,0x70000005,0x0,0x70000005,0x0,0x70000005,0x0,0x70000005,0x0,0x70000005,0x0,0x70000005,0x0,0x70000005,0x0,0x70000005,0x0,0x70000005,0x0,0x70000005,0x0,0x70000005,0x0,0x70000005,0x0,0x70000005,0x0,0x70000005,0x0,0x70000005,0x0,0x70000005,0x0,0x70000005,0x0,0x70000005,0x0,0x70000005,0x0,0x70000005,0x0,0x70000005,0x0,0x70000005,0x0,0x70000005,0x0,0x70000005,0x0,0x70000005,0x0,0x70000005,0x0,0x70000005,0x0,0x70000005,0x0,0x70000005,0x0,0x70000005,0x0,0x70000005,0x0,0x70000005,0x0,0x70000005,0x0,0x70000005,0x0,0x70000005,0x0,0x70000005,0x0,0x70000005,0x0,0x70000005,0x0,0x70000005,0x0,0x70000005,0x0,0x70000005,0x0,0x70000005,0x0,0x70000005,0x0,0x70000005,0x0,0x70000005,0x0,0x70000005,0x0,0x70000005,0x0,0x70000005,0x0,0x70000005,0x0,0x70000005,0x0,0x70000005,0x0,0x70000005,0x0,0x70000005,0x0,0x70000005,0x0,0x70000005,0x0,0x70000005,0x0,0x70000005,0x0,0x70000005,0x0,0x70000005,0x0,0x70000005,0x0,0x70000005,0x0,0x70000005,0x0,0x70000005,0x0,0x70000005,0x0,0x70000005,0x0,0x70000005,0x0,0x70000005,0x0,0x70000005,0x0,0x70000005,0x0,0x70000005,0x0,0x70000005,0x0,0x70000005,0x0,0x70000005,0x0,0x70000005,0x0,0x70000005,0x0,0x70000005,0x0,0x70000005,0x0,0x70000005,0x0,0x70000005,0x0,0x70000005,0x0,0x70000005,0x0,0x70000005,0x0,0x70000005,0x0,0x70000005,0x0,0x70000005,0x0,0x70000005,0x0,0x70000005,0x0,0x70000005,0x0,0x70000005,0x0,0x70000005,0x0,0x70000005,0x0,0x70000005,0x0,0x70000005,0x0,0x70000005,0x0,0x70000005,0x0,0x70000005,0x0,0x70000005,0x0,0x70000005,0x0,0x70000005,0x0,0x70000005,0x0,0x70000005,0x0,0x70000005,0x0,0x70000005,0x0,0x70000005,0x0,0x70000005,0x0,0x70000005,0x0,0x70000005,0x0,0x70000005,0x0,0x70000005,0x0,0x70000005,0x0,0x70000005,0x0,0x70000005,0x0,0x70000005,0x0,0x70000005,0x0,0x70000005,0x0,0x70000005,0x0,0x70000005,0x0,0x70000005,0x0,0x70000005,0x0,0x70000005,0x0,0x70000005,0x0,0x70000005,0x0,0x70000005,0x0,0x70000005,0x0,0x70000005,0x0,0x70000005,0x0,0x70000005,0x0,0x70000005,0x0,0x70000005,0x0,0x70000005,0x0,0x70000005,0x0,0x70000005,0x0,0x70000005,0x0,0x70000005,0x0,0x70000005,0x0,0x70000005,0x0,0x70000005,0x0,0x70000005,0x0,0x70000005,0x0,0x70000005,0x0,0x70000005,0x0,0x70000005,0x0,0x70000005,0x0,0x70000005,0x0,0x70000005,0x0,0x70000005,0x0,0x70000005,0x0,0x70000005,0x0,0x70000005,0x0,0x70000005,0x0,0x70000005,0x0,0x70000005,0x0,0x70000005,0x0,0x70000005,0x0,0x70000005,0x0,0x70000005,0x0,0x70000005,0x0,0x70000005,0x0,0x70000005,0x0,0x70000005,0x0,0x70000005,0x0,0x70000005,0x0,0x70000005,0x0,0x70000005,0x0,0x70000005,0x0,0x70000005,0x0,0x70000005,0x0,0x70000005,0x0,0x70000005,0x0,0x70000005,0x0,0x70000005,0x0,0x70000005,0x0,0x70000005,0x0,0x70000005,0x0,0x70000005,0x0,0x70000005,0x0,0x70000005,0x0,0x70000005,0x0,0x70000005,0x0,0x70000005,0x0,0x70000005,0x0,0x70000005,0x0,0x70000005,0x0,0x70000005,0x0,0x70000005,0x0,0x70000005,0x0,0x70000005,0x0,0x70000005,0x0,0x70000005,0x0,0x70000005,0x0,0x70000005,0x0,0x70000005,0x0,0x70000005,0x0,0x70000005,0x0,0x70000005,0x0,0x70000005,0x0,0x70000005,0x0,0x70000005,0x0,0x70000005,0x0,0x70000005,0x0,0x70000005,0x0,0x70000005,0x0,0x70000005,0x0,0x70000005,0x0,0x70000005,0x0,0x70000005,0x0,0x70000005,0x0,0x70000005,0x0,0x70000005,0x0,0x70000005,0x0,0x70000005,0x0,0x70000005,0x0,0x70000005,0x0,0x70000005,0x0,0x70000005,0x0,0x70000005,0x0,0x70000005,0x0,0x70000005,0x0,0x70000005,0x0,0x70000005,0x0,0x70000005,0x0,0x70000005,0x0,0x70000005,0x0,0x70000005,0x0,0x70000005,0x0,0x70000005,0x0,0x70000005,0x0,0x70000005,0x0,0x70000005,0x0,0x70000005,0x0,0x70000005,0x0,0x70000005,0x0,0x70000005,0x0,0x70000005,0x0,0x70000005,0x0,0x70000005,0x0,0x70000005,0x0,0x70000005,0x0,0x70000005,0x0,0x70000005,0x0,0x70000005,0x0,0x70000005,0x0,0x70000005,0x0,0x70000005,0x0,0x70000005,0x0,0x70000005,0x0,0x70000005,0x0,0x70000005,0x0,0x70000005,0x0,0x70000005,0x0,0x70000005,0x0,0x70000005,0x0,0x70000005,0x0,0x70000005,0x0,0x70000005,0x0,0x70000005,0x0,0x70000005,0x0,0x70000005,0x0,0x70000005,0x0,0x70000005,0x0,0x70000005,0x0,0x70000005,0x0,0x70000005,0x0,0x70000005,0x0,0x70000005,0x0,0x70000005,0x0,0x70000005,0x0,0x70000005,0x0,0x70000005,0x0,0x70000005,0x0,0x70000005,0x0,0x70000005,0x0,0x70000005,0x0,0x70000005,0x0,0x70000005,0x0,0x70000005,0x0,0x70000005,0x0,0x70000005,0x0,0x70000005,0x0,0x70000005,0x0,0x70000005,0x0,0x70000005,0x0,0x70000005,0x0,0x70000005,0x0,0x70000005,0x0,0x70000005,0x0,0x70000005,0x0,0x70000005,0x0,0x70000005,0x0,0x70000005,0x0,0x70000005,0x0,0x70000005,0x0,0x70000005,0x0,0x70000005,0x0,0x70000005,0x0,0x70000005,0x0,0x70000005,0x0,0x70000005,0x0,0x70000005,0x0,0x70000005,0x0,0x70000005,0x0,0x70000005,0x0,0x70000005,0x0,0x70000005,0x0,0x70000005,0x0,0x70000005,0x0,0x70000005,0x0,0x70000005,0x0,0x70000005,0x0,0x70000005,0x0,0x70000005,0x0,0x70000005,0x0,0x70000005,0x0,0x70000005,0x0,0x70000005,0x0,0x70000005,0x0,0x70000005,0x0,0x70000005,0x0,0x70000005,0x0,0x70000005,0x0,0x70000005,0x0,0x70000005,0x0,0x70000005,0x0,0x70000005,0x0,0x70000005,0x0,0x70000005,0x0,0x70000005,0x0,0x70000005,0x0,0x70000005,0x0,0x70000005,0x0,0x70000005,0x0,0x70000005,0x0,0x70000005,0x0,0x70000005,0x0,0x70000005,0x0,0x70000005,0x0,0x70000005,0x0,0x70000005,0x0,0x70000005,0x0,0x70000005,0x0,0x70000005,0x0,0x70000005,0x0,0x70000005,0x0,0x70000005,0x0,0x70000005,0x0,0x70000005,0x0,0x70000005,0x0,0x70000005,0x0,0x70000005,0x0,0x70000005,0x0,0x70000005,0x0,0x70000005,0x0,0x70000005,0x0,0x70000005,0x0,0x70000005,0x0,0x70000005,0x0,0x70000005,0x0,0x70000005,0x0,0x70000005,0x0,0x70000005,0x0,0x70000005,0x0,0x70000005,0x0,0x70000005,0x0,0x70000005,0x0,0x70000005,0x0,0x70000005,0x0,0x70000005,0x0,0x70000005,0x0,0x70000005,0x0,0x70000005,0x0,0x70000005,0x0,0x70000005,0x0,0x70000005,0x0,0x70000005,0x0,0x70000005,0x0,0x70000005,0x0,0x70000005,0x0,0x70000005,0x0,0x70000005,0x0,0x70000005,0x0,0x70000005,0x0,0x70000005,0x0,0x70000005,0x0,0x70000005,0x0,0x70000005,0x0,0x70000005,0x0,0x70000005,0x0,0x70000005,0x0,0x70000005,0x0,0x70000005,0x0,0x70000005,0x0,0x70000005,0x0,0x70000005,0x0,0x70000005,0x0,0x70000005,0x0,0x70000005,0x0,0x70000005,0x0,0x70000005,0x0,0x70000005,0x0,0x70000005,0x0,0x70000005,0x0,0x70000005,0x0,0x70000005,0x0,0x70000005,0x0,0x70000005,0x0,0x70000005,0x0,0x70000005,0x0,0x70000005,0x0,0x70000005,0x0,0x70000005,0x0,0x70000005,0x0,0x70000005,0x0,0x70000005,0x0,0x70000005,0x0,0x70000005,0x0,0x70000005,0x0,0x70000005,0x0,0x70000005,0x0,0x70000005,0x0,0x70000005,0x0,0x70000005,0x0,0x70000005,0x0,0x70000005,0x0,0x70000005,0x0,0x70000005,0x0,0x70000005,0x0,0x70000005,0x0,0x70000005,0x0,0x70000005,0x0,0x70000005,0x0,0x70000005,0x0,0x70000005,0x0,0x70000005,0x0,0x70000005,0x0,0x70000005,0x0,0x70000005,0x0,0x70000005,0x0,0x70000005,0x0,0x70000005,0x0,0x70000005,0x0,0x70000005,0x0,0x70000005,0x0,0x70000005,0x0,0x70000005,0x0,0x70000005,0x0,0x70000005,0x0,0x70000005,0x0,0x70000005,0x0,0x70000005,0x0,0x70000005,0x0,0x70000005,0x0,0x70000005,0x0,0x70000005,0x0,0x70000005,0x0,0x70000005,0x0,0x70000005,0x0,0x70000005,0x0,0x70000005,0x0,0x70000005,0x0,0x70000005,0x0,0x70000005,0x0,0x70000005,0x0,0x70000005,0x0,0x70000005,0x0,0x70000005,0x0,0x70000005,0x0,0x70000005,0x0,0x70000005,0x0,0x70000005,0x0,0x70000005,0x0,0x70000005,0x0,0x70000005,0x0,0x70000005,0x0,0x70000005,0x0,0x70000005,0x0,0x70000005,0x0,0x70000005,0x0,0x70000005,0x0,0x70000005,0x0,0x70000005,0x0,0x70000005,0x0,0x70000005,0x0,0x70000005,0x0,0x70000005,0x0,0x70000005,0x0,0x70000005,0x0,0x70000005,0x0,0x70000005,0x0,0x70000005,0x0,0x70000005,0x0,0x70000005,0x0,0x70000005,0x0,0x70000005,0x0,0x70000005,0x0,0x70000005,0x0,0x70000005,0x0,0x70000005,0x0,0x70000005,0x0,0x70000005,0x0,0x70000005,0x0,0x70000005,0x0,0x70000005,0x0,0x70000005,0x0,0x70000005,0x0,0x70000005,0x0,0x70000005,0x0,0x70000005,0x0,0x70000005,0x0,0x70000005,0x0,0x70000005,0x0,0x70000005,0x0,0x70000005,0x0,0x70000005,0x0,0x70000005,0x0,0x70000005,0x0,0x70000005,0x0,0x70000005,0x0,0x70000005,0x0,0x70000005,0x0,0x70000005,0x0,0x70000005,0x0,0x70000005,0x0,0x70000005,0x0,0x70000005,0x0,0x70000005,0x0,0x70000005,0x0,0x70000005,0x0,0x70000005,0x0,0x70000005,0x0,0x70000005,0x0,0x70000005,0x0,0x70000005,0x0,0x70000005,0x0,0x70000005,0x0,0x70000005,0x0,0x70000005,0x0,0x70000005,0x0,0x70000005,0x0,0x70000005,0x0,0x70000005,0x0,0x70000005,0x0,0x70000005,0x0,0x70000005,0x0,0x70000005,0x0,0x70000005,0x0,0x70000005,0x0,0x70000005,0x0,0x70000005,0x0,0x70000005,0x0,0x70000005,0x0,0x70000005,0x0,0x70000005,0x0,0x70000005,0x0,0x70000005,0x0,0x70000005,0x0,0x70000005,0x0,0x70000005,0x0,0x70000005,0x0,0x70000005,0x0,0x70000005,0x0,0x70000005,0x0,0x70000005,0x0,0x70000005,0x0,0x70000005,0x0,0x70000005,0x0,0x70000005,0x0,0x70000005,0x0,0x70000005,0x0,0x70000005,0x0,0x70000005,0x0,0x70000005,0x0,0x70000005,0x0,0x70000005,0x0,0x70000005,0x0,0x70000005,0x0,0x70000005,0x0,0x70000005,0x0,0x70000005,0x0,0x70000005,0x0,0x70000005,0x0,0x70000005,0x0,0x70000005,0x0,0x70000005,0x0,0x70000005,0x0,0x70000005,0x0,0x70000005,0x0,0x70000005,0x0,0x70000005,0x0,0x70000005,0x0,0x70000005,0x0,0x70000005,0x0,0x70000005,0x0,0x70000005,0x0,0x70000005,0x0,0x70000005,0x0,0x70000005,0x0,0x70000005,0x0,0x70000005,0x0,0x70000005,0x0,0x70000005,0x0,0x70000005,0x0,0x70000005,0x0,0x70000005,0x0,0x70000005,0x0,0x70000005,0x0,0x70000005,0x0,0x70000005,0x0,0x70000005,0x0,0x70000005,0x0,0x70000005,0x0,0x70000005,0x0,0x70000005,0x0,0x70000005,0x0,0x70000005,0x0,0x70000005,0x0,0x70000005,0x0,0x70000005,0x0,0x70000005,0x0,0x70000005,0x0,0x70000005,0x0,0x70000005,0x0,0x70000005,0x0,0x70000005,0x0,0x70000005,0x0,0x70000005,0x0,0x70000005,0x0,0x70000005,0x0,0x70000005,0x0,0x70000005,0x0,0x70000005,0x0,0x70000005,0x0,0x70000005,0x0,0x70000005,0x0,0x70000005,0x0,0x70000005,0x0,0x70000005,0x0,0x70000005,0x0,0x70000005,0x0,0x70000005,0x0,0x70000005,0x0,0x70000005,0x0,0x70000005,0x0,0x70000005,0x0,0x70000005,0x0,0x70000005,0x0,0x70000005,0x0,0x70000005,0x0,0x70000005,0x0,0x70000005,0x0,0x70000005,0x0,0x70000005,0x0,0x70000005,0x0,0x70000005,0x0,0x70000005,0x0,0x70000005,0x0,0x70000005,0x0,0x70000005,0x0,0x70000005,0x0,0x70000005,0x0,0x70000005,0x0,0x70000005,0x0,0x70000005,0x0,0x70000005,0x0,0x70000005,0x0,0x70000005,0x0,0x70000005,0x0,0x70000005,0x0,0x70000005,0x0,0x70000005,0x0,0x70000005,0x0,0x70000005,0x0,0x70000005,0x0,0x70000005,0x0,0x70000005,0x0,0x70000005,0x0,0x70000005,0x0,0x70000005,0x0,0x70000005,0x0,0x70000005,0x0,0x70000005,0x0,0x70000005,0x0,0x70000005,0x0,0x70000005,0x0,0x70000005,0x0,0x70000005,0x0,0x70000005,0x0,0x70000005,0x0,0x70000005,0x0,0x70000005,0x0,0x70000005,0x0,0x70000005,0x0,0x70000005,0x0,0x70000005,0x0,0x70000005,0x0,0x70000005,0x0,0x70000005,0x0,0x70000005,0x0,0x70000005,0x0,0x70000005,0x0,0x70000005,0x0,0x70000005,0x0,0x70000005,0x0,0x70000005,0x0,0x70000005,0x0,0x70000005,0x0,0x70000005,0x0,0x70000005,0x0,0x70000005,0x0,0x70000005,0x0,0x70000005,0x0,0x70000005,0x0,0x70000005,0x0,0x70000005,0x0,0x70000005,0x0,0x70000005,0x0,0x70000005,0x0,0x70000005,0x0,0x70000005,0x0,0x70000005,0x0,0x70000005,0x0,0x70000005,0x0,0x70000005,0x0,0x70000005,0x0,0x70000005,0x0,0x70000005,0x0,0x70000005,0x0,0x70000005,0x0,0x70000005,0x0,0x70000005,0x0,0x70000005,0x0,0x70000005,0x0,0x70000005,0x0,0x70000005,0x0,0x70000005,0x0,0x70000005,0x0,0x70000005,0x0,0x70000005,0x0,0x70000005,0x0,0x70000005,0x0,0x70000005,0x0,0x70000005,0x0,0x70000005,0x0,0x70000005,0x0,0x70000005,0x0,0x70000005,0x0,0x70000005,0x0,0x70000005,0x0,0x70000005,0x0,0x70000005,0x0,0x70000005,0x0,0x70000005,0x0,0x70000005,0x0,0x70000005,0x0,0x70000005,0x0,0x70000005,0x0,0x70000005,0x0,0x70000005,0x0,0x70000005,0x0,0x70000005,0x0,0x70000005,0x0,0x70000005,0x0,0x70000005,0x0,0x70000005,0x0,0x70000005,0x0,0x70000005,0x0,0x70000005,0x0,0x70000005,0x0,0x70000005,0x0,0x70000005,0x0,0x70000005,0x0,0x70000005,0x0,0x70000005,0x0,0x70000005,0x0,0x70000005,0x0,0x70000005,0x0,0x70000005,0x0,0x70000005,0x0,0x70000005,0x0,0x70000005,0x0,0x70000005,0x0,0x70000005,0x0,0x70000005,0x0,0x70000005,0x0,0x70000005,0x0,0x70000005,0x0,0x70000005,0x0,0x70000005,0x0,0x70000005,0x0,0x70000005,0x0,0x70000005,0x0,0x70000005,0x0,0x70000005,0x0,0x70000005,0x0,0x70000005,0x0,0x70000005,0x0,0x70000005,0x0,0x70000005,0x0,0x70000005,0x0,0x70000005,0x0,0x70000005,0x0,0x70000005,0x0,0x70000005,0x0,0x70000005,0x0,0x70000005,0x0,0x70000005,0x0,0x70000005,0x0,0x70000005,0x0,0x70000005,0x0,0x70000005,0x0,0x70000005,0x0,0x70000005,0x0,0x70000005,0x0,0x70000005,0x0,0x70000005,0x0,0x70000005,0x0,0x70000005,0x0,0x70000005,0x0,0x70000005,0x0,0x70000005,0x0,0x70000005,0x0,0x70000005,0x0,0x70000005,0x0,0x70000005,0x0,0x70000005,0x0,0x70000005,0x0,0x70000005,0x0,0x70000005,0x0,0x70000005,0x0,0x70000005,0x0,0x70000005,0x0,0x70000005,0x0,0x70000005,0x0,0x70000005,0x0,0x70000005,0x0,0x70000005,0x0,0x70000005,0x0,0x70000005,0x0,0x70000005,0x0,0x70000005,0x0,0x70000005,0x0,0x70000005,0x0,0x70000005,0x0,0x70000005,0x0,0x70000005,0x0,0x70000005,0x0,0x70000005,0x0,0x70000005,0x0,0x70000005,0x0,0x70000005,0x0,0x70000005,0x0,0x70000005,0x0,0x70000005,0x0,0x70000005,0x0,0x70000005,0x0,0x70000005,0x0,0x70000005,0x0,0x70000005,0x0,0x70000005,0x0,0x70000005,0x0,0x70000005,0x0,0x70000005,0x0,0x70000005,0x0,0x70000005,0x0,0x70000005,0x0,0x70000005,0x0,0x70000005,0x0,0x70000005,0x0,0x70000005,0x0,0x70000005,0x0,0x70000005,0x0,0x70000005,0x0,0x70000005,0x0,0x70000005,0x0,0x70000005,0x0,0x70000005,0x0,0x70000005,0x0,0x70000005,0x0,0x70000005,0x0,0x70000005,0x0,0x70000005,0x0,0x70000005,0x0,0x70000005,0x0,0x70000005,0x0,0x70000005,0x0,0x70000005,0x0,0x70000005,0x0,0x70000005,0x0,0x70000005,0x0,0x70000005,0x0,0x70000005,0x0,0x70000005,0x0,0x70000005,0x0,0x70000005,0x0,0x70000005,0x0,0x70000005,0x0,0x70000005,0x0,0x70000005,0x0,0x70000005,0x0,0x70000005,0x0,0x70000005,0x0,0x70000005,0x0,0x70000005,0x0,0x70000005,0x0,0x70000005,0x0,0x70000005,0x0,0x70000005,0x0,0x70000005,0x0,0x70000005,0x0,0x70000005,0x0,0x70000005,0x0,0x70000005,0x0,0x70000005,0x0,0x70000005,0x0,0x70000005,0x0,0x70000005,0x0,0x70000005,0x0,0x70000005,0x0,0x70000005,0x0,0x70000005,0x0,0x70000005,0x0,0x70000005,0x0,0x70000005,0x0,0x70000005,0x0,0x70000005,0x0,0x70000005,0x0,0x70000005,0x0,0x70000005,0x0,0x70000005,0x0,0x70000005,0x0,0x70000005,0x0,0x70000005,0x0,0x70000005,0x0,0x70000005,0x0,0x70000005,0x0,0x70000005,0x0,0x70000005,0x0,0x70000005,0x0,0x70000005,0x0,0x70000005,0x0,0x70000005,0x0,0x70000005,0x0,0x70000005,0x0,0x70000005,0x0,0x70000005,0x0,0x70000005,0x0,0x70000005,0x0,0x70000005,0x0,0x70000005,0x0,0x70000005,0x0,0x70000005,0x0,0x70000005,0x0,0x70000005,0x0,0x70000005,0x0,0x70000005,0x0,0x70000005,0x0,0x70000005,0x0,0x70000005,0x0,0x70000005,0x0,0x70000005,0x0,0x70000005,0x0,0x70000005,0x0,0x70000005,0x0,0x70000005,0x0,0x70000005,0x0,0x70000005,0x0,0x70000005,0x0,0x70000005,0x0,0x70000005,0x0,0x70000005,0x0,0x70000005,0x0,0x70000005,0x0,0x70000005,0x0,0x70000005,0x0,0x70000005,0x0,0x70000005,0x0,0x70000005,0x0,0x70000005,0x0,0x70000005,0x0,0x70000005,0x0,0x70000005,0x0,0x70000005,0x0,0x70000005,0x0,0x70000005,0x0,0x70000005,0x0,0x70000005,0x0,0x70000005,0x0,0x70000005,0x0,0x70000005,0x0,0x70000005,0x0,0x70000005,0x0,0x70000005,0x0,0x70000005,0x0,0x70000005,0x0,0x70000005,0x0,0x70000005,0x0,0x70000005,0x0,0x70000005,0x0,0x70000005,0x0,0x70000005,0x0,0x70000005,0x0,0x70000005,0x0,0x70000005,0x0,0x70000005,0x0,0x70000005,0x0,0x70000005,0x0,0x70000005,0x0,0x70000005,0x0,0x70000005,0x0,0x70000005,0x0,0x70000005,0x0,0x70000005,0x0,0x70000005,0x0,0x70000005,0x0,0x70000005,0x0,0x70000005,0x0,0x70000005,0x0,0x70000005,0x0,0x70000005,0x0,0x70000005,0x0,0x70000005,0x0,0x70000005,0x0,0x70000005,0x0,0x70000005,0x0,0x70000005,0x0,0x70000005,0x0,0x70000005,0x0,0x70000005,0x0,0x70000005,0x0,0x70000005,0x0,0x70000005,0x0,0x70000005,0x0,0x70000005,0x0,0x70000005,0x0,0x70000005,0x0,0x70000005,0x0,0x70000005,0x0,0x70000005,0x0,0x70000005,0x0,0x70000005,0x0,0x70000005,0x0,0x70000005,0x0,0x70000005,0x0,0x70000005,0x0,0x70000005,0x0,0x70000005,0x0,0x70000005,0x0,0x70000005,0x0,0x70000005,0x0,0x70000005,0x0,0x70000005,0x0,0x70000005,0x0,0x70000005,0x0,0x70000005,0x0,0x70000005,0x0,0x70000005,0x0,0x70000005,0x0,0x70000005,0x0,0x70000005,0x0,0x70000005,0x0,0x70000005,0x0,0x70000005,0x0,0x70000005,0x0,0x70000005,0x0,0x70000005,0x0,0x70000005,0x0,0x70000005,0x0,0x70000005,0x0,0x70000005,0x0,0x70000005,0x0,0x70000005,0x0,0x70000005,0x0,0x70000005,0x0,0x70000005,0x0,0x70000005,0x0,0x70000005,0x0,0x70000005,0x0,0x70000005,0x0,0x70000005,0x0,0x70000005,0x0,0x70000005,0x0,0x70000005,0x0,0x70000005,0x0,0x70000005,0x0,0x70000005,0x0,0x70000005,0x0,0x70000005,0x0,0x70000005,0x0,0x70000005,0x0,0x70000005,0x0,0x70000005,0x0,0x70000005,0x0,0x70000005,0x0,0x70000005,0x0,0x70000005,0x0,0x70000005,0x0,0x70000005,0x0,0x70000005,0x0,0x70000005,0x0,0x70000005,0x0,0x70000005,0x0,0x70000005,0x0,0x70000005,0x0,0x70000005,0x0,0x70000005,0x0,0x70000005,0x0,0x70000005,0x0,0x70000005,0x0,0x70000005,0x0,0x70000005,0x0,0x70000005,0x0,0x70000005,0x0,0x70000005,0x0,0x70000005,0x0,0x70000005,0x0,0x70000005,0x0,0x70000005,0x0,0x70000005,0x0,0x70000005,0x0,0x70000005,0x0,0x70000005,0x0,0x70000005,0x0,0x70000005,0x0,0x70000005,0x0,0x70000005,0x0,0x70000005,0x0,0x70000005,0x0,0x70000005,0x0,0x70000005,0x0,0x70000005,0x0,0x70000005,0x0,0x70000005,0x0,0x70000005,0x0,0x70000005,0x0,0x70000005,0x0,0x70000005,0x0,0x70000005,0x0,0x70000005,0x0,0x70000005,0x0,0x70000005,0x0,0x70000005,0x0,0x70000005,0x0,0x70000005,0x0,0x70000005,0x0,0x70000005,0x0,0x70000005,0x0,0x70000005,0x0,0x70000005,0x0,0x70000005,0x0,0x70000005,0x0,0x70000005,0x0,0x70000005,0x0,0x70000005,0x0,0x70000005,0x0,0x70000005,0x0,0x70000005,0x0,0x70000005,0x0,0x70000005,0x0,0x70000005,0x0,0x70000005,0x0,0x70000005,0x0,0x70000005,0x0,0x70000005,0x0,0x70000005,0x0,0x70000005,0x0,0x70000005,0x0,0x70000005,0x0,0x70000005,0x0,0x70000005,0x0,0x70000005,0x0,0x70000005,0x0,0x70000005,0x0,0x70000005,0x0,0x70000005,0x0,0x70000005,0x0,0x70000005,0x0,0x70000005,0x0,0x70000005,0x0,0x70000005,0x0,0x70000005,0x0,0x70000005,0x0,0x70000005,0x0,0x70000005,0x0,0x70000005,0x0,0x70000005,0x0,0x70000005,0x0,0x70000005,0x0,0x70000005,0x0,0x70000005,0x0,0x70000005,0x0,0x70000005,0x0,0x70000005,0x0,0x70000005,0x0,0x70000005,0x0,0x70000005,0x0,0x70000005,0x0,0x70000005,0x0,0x70000005,0x0,0x70000005,0x0,0x70000005,0x0,0x70000005,0x0,0x70000005,0x0,0x70000005,0x0},
  {0x120020,0x1,0x120011,0x1,0x120025,0x1,0x120016,0x1,0x12002a,0x1,0x12001b,0x1,0x12002f,0x1,0x120010,0x1,0x120021,0x1,0x120012,0x1,0x120022,0x1,0x120013,0x1,0x120023,0x1,0x120014,0x1,0x120024,0x1,0x120015,0x1,0x120026,0x1,0x120017,0x1,0x120027,0x1,0x120018,0x1,0x120028,0x1,0x120019,0x1,0x120029,0x1,0x12001a,0x1,0x12002b,0x1,0x12001c,0x1,0x12002c,0x1,0x12001d,0x1,0x12002d,0x1,0x12001e,0x1,0x12002e,0x1,0x12001f,0x1,0x20001,0x1,0x20009,0x1,0x420020,0x1,0x420011,0x1,0x420025,0x1,0x420016,0x1,0x42002a,0x1,0x42001b,0x1,0x42002f,0x1,0x120001,0x1,0x120009,0x1,0x520020,0x1,0x520011,0x1,0x520025,0x1,0x520016,0x1,0x52002a,0x1,0x52001b,0x1,0x52002f,0x1,0x220001,0x1,0x220009,0x1,0x620020,0x1,0x620011,0x1,0x620025,0x1,0x620016,0x1,0x62002a,0x1,0x62001b,0x1,0x62002f,0x1,0x320001,0x1,0x320009,0x1,0x720020,0x1,0x720011,0x1,0x720025,0x1,0x10000048,0x10308,0x10000050,0x10308,0x10000058,0x10308,0x10000060,0x10408,0x10000068,0x10408,0x10000070,0x10408,0x10000078,0x10408,0x720016,0x1,0x72002a,0x1,0x72001b,0x1,0x72002f,0x1,0x420001,0x1,0x420009,0x1,0x820020,0x1,0x820011,0x1,0x820025,0x1,0x820016,0x1,0x82002a,0x1,0x82001b,0x1,0x82002f,0x1,0x520001,0x1,0x520009,0x1,0x920020,0x1,0x920011,0x1,0x920025,0x1,0x920016,0x1,0x92002a,0x1,0x92001b,0x1,0x92002f,0x1,0x620001,0x1,0x620009,0x1,0xa20020,0x1,0x20020202,0x2200501,0xa20011,0x1,0xa20025,0x1,0xa20016,0x1,0xa2002a,0x1,0xa2001b,0x1,0x20020001,0x1200501,0x20020403,0x3200501,0xa2002f,0x1,0x720001,0x1,0x720009,0x1,0xb20020,0x1,0xb20011,0x1,0xb20025,0x1,0xb20016,0x1,0xb2002a,0x1,0xb2001b,0x1,0xb2002f,0x1,0x820001,0x1,0x820009,0x1,0xc20020,0x1,0xc20011,0x1,0xc20025,0x1,0xc20016,0x1,0xc2002a,0x1,0xc2001b,0x1,0xc2002f,0x1,0x920001,0x1,0x920009,0x1,0xd20020,0x1,0xd20011,0x1,0xd20025,0x1,0x10200000,0x11108,0x10200008,0x11108,0x20120202,0xa200d01,0xd20016,0x1,0xd2002a,0x1,0xd2001b,0x1,0xd2002f,0x1,0x20120001,0x9200d01,0x20120403,0xb200d01,0x30008000,0x5100608,0x30008040,0x5100608,0x30008048,0x5100608,0x30008050,0x5100608,0x30008058,0x5100608,0x30008060,0x5100608,0x30008068,0x5100608,0x30008070,0x5100608,0x30008078,0x5100608,0x20220001,0x11201501,0x20220202,0x12201501,0x20220403,0x13201501,0xa20001,0x1,0xa20009,0x1,0xe20020,0x1,0xe20011,0x1,0xe20025,0x1,0xe20016,0x1,0xe2002a,0x1,0xe2001b,0x1,0xe2002f,0x1,0xb20001,0x1,0xb20009,0x1,0xc20001,0x1,0xc20009,0x1,0x10300000,0x11908,0x40020000,0x6800701,0x40020005,0x6800701,0x4002000a,0x6800701,0x10300030,0x11a08,0x10300038,0x11a08,0x10300040,0x11b08,0x10300048,0x11b08,0x10300050,0x11b08,0x10300078,0x11c08,0xf20020,0x1,0xf20011,0x1,0xf20025,0x1,0xf20016,0x1,0xf2002a,0x1,0x20320001,0x19201d01,0x20320202,0x1a201d01,0xf2001b,0x1,0xf2002f,0x1,0xd20001,0x1,0xd20009,0x1,0x40020001,0x6800701,0x20320403,0x1b201d01,0x30208000,0x15101608,0x30208008,0x15101608,0x30208010,0x15101608,0x30208030,0x15101608,0x30208038,0x15101608,0x30208040,0x15101608,0x30208048,0x15101608,0x30208050,0x15101608,0x30208058,0x15101608,0x30208060,0x15101608,0x30208068,0x15101608,0x30208070,0x15101608,0x30208078,0x15101608,0x10400008,0x12108,0x10400010,0x12108,0x10400018,0x12108,0x20420001,0x21202501,0x20420202,0x22202501,0x20420403,0x23202501,0x20420002,0x21202501,0x20420203,0x22202501,0x20420402,0x23202501,0x10500008,0x12908,0x10500010,0x12908,0x10500018,0x12908,0x10500020,0x12a08,0x10500028,0x12a08,0x10500030,0x12a08,0x10500038,0x12a08,0x10500040,0x12b08,0x10500048,0x12b08,0x10500050,0x12b08,0x5001c200,0x70e0801,0x5001c401,0x70e0801,0x5001c602,0x70e0801,0x5001c000,0x70e0801,0x5001c001,0x70e0801,0x5001c002,0x70e0801,0x5001c201,0x70e0801,0x30308040,0x1d101e08,0x30308048,0x1d101e08,0x30308050,0x1d101e08,0x30308058,0x1d101e08,0x30308078,0x1d101e08,0x20520001,0x29202d01,0x20520202,0x2a202d01,0x20520403,0x2b202d01,0x20520400,0x2b202d01,0x5011c401,0xf0e1001,0x5011c602,0xf0e1001,0x5011c203,0xf0e1001,0x5011c600,0xf0e1001,0x40320000,0x1e801f01,0x40320005,0x1e801f01,0x4032000a,0x1e801f01,0x5021c200,0x170e1801,0x5021c401,0x170e1801,0x5021c602,0x170e1801,0x70000005,0x0,0x70000005,0x0,0x70000005,0x0,0x70000005,0x0,0x70000005,0x0,0x70000005,0x0,0x70000005,0x0,0x70000005,0x0,0x70000005,0x0,0x70000005,0x0,0x70000005,0x0,0x70000005,0x0,0x70000005,0x0,0x70000005,0x0,0x70000005,0x0,0x70000005,0x0,0x70000005,0x0,0x70000005,0x0,0x70000005,0x0,0x70000005,0x0,0x70000005,0x0,0x70000005,0x0,0x70000005,0x0,0x70000005,0x0,0x70000005,0x0,0x70000005,0x0,0x70000005,0x0,0x70000005,0x0,0x70000005,0x0,0x70000005,0x0,0x70000005,0x0,0x70000005,0x0,0x70000005,0x0,0x70000005,0x0,0x70000005,0x0,0x70000005,0x0,0x70000005,0x0,0x70000005,0x0,0x70000005,0x0,0x70000005,0x0,0x70000005,0x0,0x70000005,0x0,0x70000005,0x0,0x70000005,0x0,0x70000005,0x0,0x70000005,0x0,0x70000005,0x0,0x70000005,0x0,0x70000005,0x0,0x70000005,0x0,0x70000005,0x0,0x70000005,0x0,0x70000005,0x0,0x70000005,0x0,0x70000005,0x0,0x70000005,0x0,0x70000005,0x0,0x70000005,0x0,0x70000005,0x0,0x70000005,0x0,0x70000005,0x0,0x70000005,0x0,0x70000005,0x0,0x70000005,0x0,0x70000005,0x0,0x70000005,0x0,0x70000005,0x0,0x70000005,0x0,0x70000005,0x0,0x70000005,0x0,0x70000005,0x0,0x70000005,0x0,0x70000005,0x0,0x70000005,0x0,0x70000005,0x0,0x70000005,0x0,0x70000005,0x0,0x70000005,0x0,0x70000005,0x0,0x70000005,0x0,0x70000005,0x0,0x70000005,0x0,0x70000005,0x0,0x70000005,0x0,0x70000005,0x0,0x70000005,0x0,0x70000005,0x0,0x70000005,0x0,0x70000005,0x0,0x70000005,0x0,0x70000005,0x0,0x70000005,0x0,0x70000005,0x0,0x70000005,0x0,0x70000005,0x0,0x70000005,0x0,0x70000005,0x0,0x70000005,0x0,0x70000005,0x0,0x70000005,0x0,0x70000005,0x0,0x70000005,0x0,0x70000005,0x0,0x70000005,0x0,0x70000005,0x0,0x70000005,0x0,0x70000005,0x0,0x70000005,0x0,0x70000005,0x0,0x70000005,0x0,0x70000005,0x0,0x70000005,0x0,0x70000005,0x0,0x70000005,0x0,0x70000005,0x0,0x70000005,0x0,0x70000005,0x0,0x70000005,0x0,0x70000005,0x0,0x70000005,0x0,0x70000005,0x0,0x70000005,0x0,0x70000005,0x0,0x70000005,0x0,0x70000005,0x0,0x70000005,0x0,0x70000005,0x0,0x70000005,0x0,0x70000005,0x0,0x70000005,0x0,0x70000005,0x0,0x70000005,0x0,0x70000005,0x0,0x70000005,0x0,0x70000005,0x0,0x70000005,0x0,0x70000005,0x0,0x70000005,0x0,0x70000005,0x0,0x70000005,0x0,0x70000005,0x0,0x70000005,0x0,0x70000005,0x0,0x70000005,0x0,0x70000005,0x0,0x70000005,0x0,0x70000005,0x0,0x70000005,0x0,0x70000005,0x0,0x70000005,0x0,0x70000005,0x0,0x70000005,0x0,0x70000005,0x0,0x70000005,0x0,0x70000005,0x0,0x70000005,0x0,0x70000005,0x0,0x70000005,0x0,0x70000005,0x0,0x70000005,0x0,0x70000005,0x0,0x70000005,0x0,0x70000005,0x0,0x70000005,0x0,0x70000005,0x0,0x70000005,0x0,0x70000005,0x0,0x70000005,0x0,0x70000005,0x0,0x70000005,0x0,0x70000005,0x0,0x70000005,0x0,0x70000005,0x0,0x70000005,0x0,0x70000005,0x0,0x70000005,0x0,0x70000005,0x0,0x70000005,0x0,0x70000005,0x0,0x70000005,0x0,0x70000005,0x0,0x70000005,0x0,0x70000005,0x0,0x70000005,0x0,0x70000005,0x0,0x70000005,0x0,0x70000005,0x0,0x70000005,0x0,0x70000005,0x0,0x70000005,0x0,0x70000005,0x0,0x70000005,0x0,0x70000005,0x0,0x70000005,0x0,0x70000005,0x0,0x70000005,0x0,0x70000005,0x0,0x70000005,0x0,0x70000005,0x0,0x70000005,0x0,0x70000005,0x0,0x70000005,0x0,0x70000005,0x0,0x70000005,0x0,0x70000005,0x0,0x70000005,0x0,0x70000005,0x0,0x70000005,0x0,0x70000005,0x0,0x70000005,0x0,0x70000005,0x0,0x70000005,0x0,0x70000005,0x0,0x70000005,0x0,0x70000005,0x0,0x70000005,0x0,0x70000005,0x0,0x70000005,0x0,0x70000005,0x0,0x70000005,0x0,0x70000005,0x0,0x70000005,0x0,0x70000005,0x0,0x70000005,0x0,0x70000005,0x0,0x70000005,0x0,0x70000005,0x0,0x70000005,0x0,0x70000005,0x0,0x70000005,0x0,0x70000005,0x0,0x70000005,0x0,0x70000005,0x0,0x70000005,0x0,0x70000005,0x0,0x70000005,0x0,0x70000005,0x0,0x70000005,0x0,0x70000005,0x0,0x70000005,0x0,0x70000005,0x0,0x70000005,0x0,0x70000005,0x0,0x70000005,0x0,0x70000005,0x0,0x70000005,0x0,0x70000005,0x0,0x70000005,0x0,0x70000005,0x0,0x70000005,0x0,0x70000005,0x0,0x70000005,0x0,0x70000005,0x0,0x70000005,0x0,0x70000005,0x0,0x70000005,0x0,0x70000005,0x0,0x70000005,0x0,0x70000005,0x0,0x70000005,0x0,0x70000005,0x0,0x70000005,0x0,0x70000005,0x0,0x70000005,0x0,0x70000005,0x0,0x70000005,0x0,0x70000005,0x0,0x70000005,0x0,0x70000005,0x0,0x70000005,0x0,0x70000005,0x0,0x70000005,0x0,0x70000005,0x0,0x70000005,0x0,0x70000005,0x0,0x70000005,0x0,0x70000005,0x0,0x70000005,0x0,0x70000005,0x0,0x70000005,0x0,0x70000005,0x0,0x70000005,0x0,0x70000005,0x0,0x70000005,0x0,0x70000005,0x0,0x70000005,0x0,0x70000005,0x0,0x70000005,0x0,0x70000005,0x0,0x70000005,0x0,0x70000005,0x0,0x70000005,0x0,0x70000005,0x0,0x70000005,0x0,0x70000005,0x0,0x70000005,0x0,0x70000005,0x0,0x70000005,0x0,0x70000005,0x0,0x70000005,0x0,0x70000005,0x0,0x70000005,0x0,0x70000005,0x0,0x70000005,0x0,0x70000005,0x0,0x70000005,0x0,0x70000005,0x0,0x70000005,0x0,0x70000005,0x0,0x70000005,0x0,0x70000005,0x0,0x70000005,0x0,0x70000005,0x0,0x70000005,0x0,0x70000005,0x0,0x70000005,0x0,0x70000005,0x0,0x70000005,0x0,0x70000005,0x0,0x70000005,0x0,0x70000005,0x0,0x70000005,0x0,0x70000005,0x0,0x70000005,0x0,0x70000005,0x0,0x70000005,0x0,0x70000005,0x0,0x70000005,0x0,0x70000005,0x0,0x70000005,0x0,0x70000005,0x0,0x70000005,0x0,0x70000005,0x0,0x70000005,0x0,0x70000005,0x0,0x70000005,0x0,0x70000005,0x0,0x70000005,0x0,0x70000005,0x0,0x70000005,0x0,0x70000005,0x0,0x70000005,0x0,0x70000005,0x0,0x70000005,0x0,0x70000005,0x0,0x70000005,0x0,0x70000005,0x0,0x70000005,0x0,0x70000005,0x0,0x70000005,0x0,0x70000005,0x0,0x70000005,0x0,0x70000005,0x0,0x70000005,0x0,0x70000005,0x0,0x70000005,0x0,0x70000005,0x0,0x70000005,0x0,0x70000005,0x0,0x70000005,0x0,0x70000005,0x0,0x70000005,0x0,0x70000005,0x0,0x70000005,0x0,0x70000005,0x0,0x70000005,0x0,0x70000005,0x0,0x70000005,0x0,0x70000005,0x0,0x70000005,0x0,0x70000005,0x0,0x70000005,0x0,0x70000005,0x0,0x70000005,0x0,0x70000005,0x0,0x70000005,0x0,0x70000005,0x0,0x70000005,0x0,0x70000005,0x0,0x70000005,0x0,0x70000005,0x0,0x70000005,0x0,0x70000005,0x0,0x70000005,0x0,0x70000005,0x0,0x70000005,0x0,0x70000005,0x0,0x70000005,0x0,0x70000005,0x0,0x70000005,0x0,0x70000005,0x0,0x70000005,0x0,0x70000005,0x0,0x70000005,0x0,0x70000005,0x0,0x70000005,0x0,0x70000005,0x0,0x70000005,0x0,0x70000005,0x0,0x70000005,0x0,0x70000005,0x0,0x70000005,0x0,0x70000005,0x0,0x70000005,0x0,0x70000005,0x0,0x70000005,0x0,0x70000005,0x0,0x70000005,0x0,0x70000005,0x0,0x70000005,0x0,0x70000005,0x0,0x70000005,0x0,0x70000005,0x0,0x70000005,0x0,0x70000005,0x0,0x70000005,0x0,0x70000005,0x0,0x70000005,0x0,0x70000005,0x0,0x70000005,0x0,0x70000005,0x0,0x70000005,0x0,0x70000005,0x0,0x70000005,0x0,0x70000005,0x0,0x70000005,0x0,0x70000005,0x0,0x70000005,0x0,0x70000005,0x0,0x70000005,0x0,0x70000005,0x0,0x70000005,0x0,0x70000005,0x0,0x70000005,0x0,0x70000005,0x0,0x70000005,0x0,0x70000005,0x0,0x70000005,0x0,0x70000005,0x0,0x70000005,0x0,0x70000005,0x0,0x70000005,0x0,0x70000005,0x0,0x70000005,0x0,0x70000005,0x0,0x70000005,0x0,0x70000005,0x0,0x70000005,0x0,0x70000005,0x0,0x70000005,0x0,0x70000005,0x0,0x70000005,0x0,0x70000005,0x0,0x70000005,0x0,0x70000005,0x0,0x70000005,0x0,0x70000005,0x0,0x70000005,0x0,0x70000005,0x0,0x70000005,0x0,0x70000005,0x0,0x70000005,0x0,0x70000005,0x0,0x70000005,0x0,0x70000005,0x0,0x70000005,0x0,0x70000005,0x0,0x70000005,0x0,0x70000005,0x0,0x70000005,0x0,0x70000005,0x0,0x70000005,0x0,0x70000005,0x0,0x70000005,0x0,0x70000005,0x0,0x70000005,0x0,0x70000005,0x0,0x70000005,0x0,0x70000005,0x0,0x70000005,0x0,0x70000005,0x0,0x70000005,0x0,0x70000005,0x0,0x70000005,0x0,0x70000005,0x0,0x70000005,0x0,0x70000005,0x0,0x70000005,0x0,0x70000005,0x0,0x70000005,0x0,0x70000005,0x0,0x70000005,0x0,0x70000005,0x0,0x70000005,0x0,0x70000005,0x0,0x70000005,0x0,0x70000005,0x0,0x70000005,0x0,0x70000005,0x0,0x70000005,0x0,0x70000005,0x0,0x70000005,0x0,0x70000005,0x0,0x70000005,0x0,0x70000005,0x0,0x70000005,0x0,0x70000005,0x0,0x70000005,0x0,0x70000005,0x0,0x70000005,0x0,0x70000005,0x0,0x70000005,0x0,0x70000005,0x0,0x70000005,0x0,0x70000005,0x0,0x70000005,0x0,0x70000005,0x0,0x70000005,0x0,0x70000005,0x0,0x70000005,0x0,0x70000005,0x0,0x70000005,0x0,0x70000005,0x0,0x70000005,0x0,0x70000005,0x0,0x70000005,0x0,0x70000005,0x0,0x70000005,0x0,0x70000005,0x0,0x70000005,0x0,0x70000005,0x0,0x70000005,0x0,0x70000005,0x0,0x70000005,0x0,0x70000005,0x0,0x70000005,0x0,0x70000005,0x0,0x70000005,0x0,0x70000005,0x0,0x70000005,0x0,0x70000005,0x0,0x70000005,0x0,0x70000005,0x0,0x70000005,0x0,0x70000005,0x0,0x70000005,0x0,0x70000005,0x0,0x70000005,0x0,0x70000005,0x0,0x70000005,0x0,0x70000005,0x0,0x70000005,0x0,0x70000005,0x0,0x70000005,0x0,0x70000005,0x0,0x70000005,0x0,0x70000005,0x0,0x70000005,0x0,0x70000005,0x0,0x70000005,0x0,0x70000005,0x0,0x70000005,0x0,0x70000005,0x0,0x70000005,0x0,0x70000005,0x0,0x70000005,0x0,0x70000005,0x0,0x70000005,0x0,0x70000005,0x0,0x70000005,0x0,0x70000005,0x0,0x70000005,0x0,0x70000005,0x0,0x70000005,0x0,0x70000005,0x0,0x70000005,0x0,0x70000005,0x0,0x70000005,0x0,0x70000005,0x0,0x70000005,0x0,0x70000005,0x0,0x70000005,0x0,0x70000005,0x0,0x70000005,0x0,0x70000005,0x0,0x70000005,0x0,0x70000005,0x0,0x70000005,0x0,0x70000005,0x0,0x70000005,0x0,0x70000005,0x0,0x70000005,0x0,0x70000005,0x0,0x70000005,0x0,0x70000005,0x0,0x70000005,0x0,0x70000005,0x0,0x70000005,0x0,0x70000005,0x0,0x70000005,0x0,0x70000005,0x0,0x70000005,0x0,0x70000005,0x0,0x70000005,0x0,0x70000005,0x0,0x70000005,0x0,0x70000005,0x0,0x70000005,0x0,0x70000005,0x0,0x70000005,0x0,0x70000005,0x0,0x70000005,0x0,0x70000005,0x0,0x70000005,0x0,0x70000005,0x0,0x70000005,0x0,0x70000005,0x0,0x70000005,0x0,0x70000005,0x0,0x70000005,0x0,0x70000005,0x0,0x70000005,0x0,0x70000005,0x0,0x70000005,0x0,0x70000005,0x0,0x70000005,0x0,0x70000005,0x0,0x70000005,0x0,0x70000005,0x0,0x70000005,0x0,0x70000005,0x0,0x70000005,0x0,0x70000005,0x0,0x70000005,0x0,0x70000005,0x0,0x70000005,0x0,0x70000005,0x0,0x70000005,0x0,0x70000005,0x0,0x70000005,0x0,0x70000005,0x0,0x70000005,0x0,0x70000005,0x0,0x70000005,0x0,0x70000005,0x0,0x70000005,0x0,0x70000005,0x0,0x70000005,0x0,0x70000005,0x0,0x70000005,0x0,0x70000005,0x0,0x70000005,0x0,0x70000005,0x0,0x70000005,0x0,0x70000005,0x0,0x70000005,0x0,0x70000005,0x0,0x70000005,0x0,0x70000005,0x0,0x70000005,0x0,0x70000005,0x0,0x70000005,0x0,0x70000005,0x0,0x70000005,0x0,0x70000005,0x0,0x70000005,0x0,0x70000005,0x0,0x70000005,0x0,0x70000005,0x0,0x70000005,0x0,0x70000005,0x0,0x70000005,0x0,0x70000005,0x0,0x70000005,0x0,0x70000005,0x0,0x70000005,0x0,0x70000005,0x0,0x70000005,0x0,0x70000005,0x0,0x70000005,0x0,0x70000005,0x0,0x70000005,0x0,0x70000005,0x0,0x70000005,0x0,0x70000005,0x0,0x70000005,0x0,0x70000005,0x0,0x70000005,0x0,0x70000005,0x0,0x70000005,0x0,0x70000005,0x0,0x70000005,0x0,0x70000005,0x0,0x70000005,0x0,0x70000005,0x0,0x70000005,0x0,0x70000005,0x0,0x70000005,0x0,0x70000005,0x0,0x70000005,0x0,0x70000005,0x0,0x70000005,0x0,0x70000005,0x0,0x70000005,0x0,0x70000005,0x0,0x70000005,0x0,0x70000005,0x0,0x70000005,0x0,0x70000005,0x0,0x70000005,0x0,0x70000005,0x0,0x70000005,0x0,0x70000005,0x0,0x70000005,0x0,0x70000005,0x0,0x70000005,0x0,0x70000005,0x0,0x70000005,0x0,0x70000005,0x0,0x70000005,0x0,0x70000005,0x0,0x70000005,0x0,0x70000005,0x0,0x70000005,0x0,0x70000005,0x0,0x70000005,0x0,0x70000005,0x0,0x70000005,0x0,0x70000005,0x0,0x70000005,0x0,0x70000005,0x0,0x70000005,0x0,0x70000005,0x0,0x70000005,0x0,0x70000005,0x0,0x70000005,0x0,0x70000005,0x0,0x70000005,0x0,0x70000005,0x0,0x70000005,0x0,0x70000005,0x0,0x70000005,0x0,0x70000005,0x0,0x70000005,0x0,0x70000005,0x0,0x70000005,0x0,0x70000005,0x0,0x70000005,0x0,0x70000005,0x0,0x70000005,0x0,0x70000005,0x0,0x70000005,0x0,0x70000005,0x0,0x70000005,0x0,0x70000005,0x0,0x70000005,0x0,0x70000005,0x0,0x70000005,0x0,0x70000005,0x0,0x70000005,0x0,0x70000005,0x0,0x70000005,0x0,0x70000005,0x0,0x70000005,0x0,0x70000005,0x0,0x70000005,0x0,0x70000005,0x0,0x70000005,0x0,0x70000005,0x0,0x70000005,0x0,0x70000005,0x0,0x70000005,0x0,0x70000005,0x0,0x70000005,0x0,0x70000005,0x0,0x70000005,0x0,0x70000005,0x0,0x70000005,0x0,0x70000005,0x0,0x70000005,0x0,0x70000005,0x0,0x70000005,0x0,0x70000005,0x0,0x70000005,0x0,0x70000005,0x0,0x70000005,0x0,0x70000005,0x0,0x70000005,0x0,0x70000005,0x0,0x70000005,0x0,0x70000005,0x0,0x70000005,0x0,0x70000005,0x0,0x70000005,0x0,0x70000005,0x0,0x70000005,0x0,0x70000005,0x0,0x70000005,0x0,0x70000005,0x0,0x70000005,0x0,0x70000005,0x0,0x70000005,0x0,0x70000005,0x0,0x70000005,0x0,0x70000005,0x0,0x70000005,0x0,0x70000005,0x0,0x70000005,0x0,0x70000005,0x0,0x70000005,0x0,0x70000005,0x0,0x70000005,0x0,0x70000005,0x0,0x70000005,0x0,0x70000005,0x0,0x70000005,0x0,0x70000005,0x0,0x70000005,0x0,0x70000005,0x0,0x70000005,0x0,0x70000005,0x0,0x70000005,0x0,0x70000005,0x0,0x70000005,0x0,0x70000005,0x0,0x70000005,0x0,0x70000005,0x0,0x70000005,0x0,0x70000005,0x0,0x70000005,0x0,0x70000005,0x0,0x70000005,0x0,0x70000005,0x0,0x70000005,0x0,0x70000005,0x0,0x70000005,0x0,0x70000005,0x0,0x70000005,0x0,0x70000005,0x0,0x70000005,0x0,0x70000005,0x0,0x70000005,0x0,0x70000005,0x0,0x70000005,0x0,0x70000005,0x0,0x70000005,0x0,0x70000005,0x0,0x70000005,0x0,0x70000005,0x0,0x70000005,0x0,0x70000005,0x0,0x70000005,0x0,0x70000005,0x0,0x70000005,0x0,0x70000005,0x0,0x70000005,0x0,0x70000005,0x0,0x70000005,0x0,0x70000005,0x0,0x70000005,0x0,0x70000005,0x0,0x70000005,0x0,0x70000005,0x0,0x70000005,0x0,0x70000005,0x0,0x70000005,0x0,0x70000005,0x0,0x70000005,0x0,0x70000005,0x0,0x70000005,0x0,0x70000005,0x0,0x70000005,0x0,0x70000005,0x0,0x70000005,0x0,0x70000005,0x0,0x70000005,0x0,0x70000005,0x0,0x70000005,0x0,0x70000005,0x0,0x70000005,0x0,0x70000005,0x0,0x70000005,0x0,0x70000005,0x0,0x70000005,0x0,0x70000005,0x0,0x70000005,0x0,0x70000005,0x0,0x70000005,0x0,0x70000005,0x0,0x70000005,0x0,0x70000005,0x0,0x70000005,0x0,0x70000005,0x0,0x70000005,0x0,0x70000005,0x0,0x70000005,0x0,0x70000005,0x0,0x70000005,0x0,0x70000005,0x0,0x70000005,0x0,0x70000005,0x0,0x70000005,0x0,0x70000005,0x0,0x70000005,0x0,0x70000005,0x0,0x70000005,0x0,0x70000005,0x0,0x70000005,0x0,0x70000005,0x0,0x70000005,0x0,0x70000005,0x0,0x70000005,0x0,0x70000005,0x0,0x70000005,0x0,0x70000005,0x0,0x70000005,0x0,0x70000005,0x0,0x70000005,0x0,0x70000005,0x0,0x70000005,0x0,0x70000005,0x0,0x70000005,0x0,0x70000005,0x0,0x70000005,0x0,0x70000005,0x0,0x70000005,0x0,0x70000005,0x0,0x70000005,0x0,0x70000005,0x0,0x70000005,0x0,0x70000005,0x0,0x70000005,0x0,0x70000005,0x0,0x70000005,0x0,0x70000005,0x0,0x70000005,0x0,0x70000005,0x0,0x70000005,0x0,0x70000005,0x0,0x70000005,0x0,0x70000005,0x0,0x70000005,0x0,0x70000005,0x0,0x70000005,0x0,0x70000005,0x0,0x70000005,0x0,0x70000005,0x0,0x70000005,0x0,0x70000005,0x0,0x70000005,0x0,0x70000005,0x0,0x70000005,0x0,0x70000005,0x0,0x70000005,0x0,0x70000005,0x0,0x70000005,0x0,0x70000005,0x0,0x70000005,0x0,0x70000005,0x0,0x70000005,0x0,0x70000005,0x0,0x70000005,0x0,0x70000005,0x0,0x70000005,0x0,0x70000005,0x0,0x70000005,0x0,0x70000005,0x0,0x70000005,0x0,0x70000005,0x0,0x70000005,0x0,0x70000005,0x0,0x70000005,0x0,0x70000005,0x0,0x70000005,0x0,0x70000005,0x0,0x70000005,0x0,0x70000005,0x0,0x70000005,0x0,0x70000005,0x0,0x70000005,0x0,0x70000005,0x0,0x70000005,0x0,0x70000005,0x0,0x70000005,0x0,0x70000005,0x0,0x70000005,0x0,0x70000005,0x0,0x70000005,0x0,0x70000005,0x0,0x70000005,0x0,0x70000005,0x0,0x70000005,0x0,0x70000005,0x0,0x70000005,0x0,0x70000005,0x0,0x70000005,0x0,0x70000005,0x0,0x70000005,0x0,0x70000005,0x0,0x70000005,0x0,0x70000005,0x0,0x70000005,0x0,0x70000005,0x0,0x70000005,0x0,0x70000005,0x0,0x70000005,0x0,0x70000005,0x0,0x70000005,0x0,0x70000005,0x0,0x70000005,0x0,0x70000005,0x0,0x70000005,0x0,0x70000005,0x0,0x70000005,0x0,0x70000005,0x0,0x70000005,0x0,0x70000005,0x0,0x70000005,0x0,0x70000005,0x0,0x70000005,0x0,0x70000005,0x0,0x70000005,0x0,0x70000005,0x0,0x70000005,0x0,0x70000005,0x0,0x70000005,0x0,0x70000005,0x0,0x70000005,0x0,0x70000005,0x0,0x70000005,0x0,0x70000005,0x0,0x70000005,0x0,0x70000005,0x0,0x70000005,0x0,0x70000005,0x0,0x70000005,0x0,0x70000005,0x0,0x70000005,0x0,0x70000005,0x0,0x70000005,0x0,0x70000005,0x0,0x70000005,0x0,0x70000005,0x0,0x70000005,0x0,0x70000005,0x0,0x70000005,0x0,0x70000005,0x0,0x70000005,0x0,0x70000005,0x0,0x70000005,0x0,0x70000005,0x0,0x70000005,0x0,0x70000005,0x0,0x70000005,0x0,0x70000005,0x0,0x70000005,0x0,0x70000005,0x0,0x70000005,0x0,0x70000005,0x0,0x70000005,0x0,0x70000005,0x0,0x70000005,0x0,0x70000005,0x0,0x70000005,0x0,0x70000005,0x0,0x70000005,0x0,0x70000005,0x0,0x70000005,0x0,0x70000005,0x0,0x70000005,0x0,0x70000005,0x0,0x70000005,0x0,0x70000005,0x0,0x70000005,0x0,0x70000005,0x0,0x70000005,0x0,0x70000005,0x0,0x70000005,0x0,0x70000005,0x0,0x70000005,0x0,0x70000005,0x0,0x70000005,0x0,0x70000005,0x0,0x70000005,0x0,0x70000005,0x0,0x70000005,0x0,0x70000005,0x0,0x70000005,0x0,0x70000005,0x0,0x70000005,0x0,0x70000005,0x0,0x70000005,0x0,0x70000005,0x0,0x70000005,0x0,0x70000005,0x0,0x70000005,0x0,0x70000005,0x0,0x70000005,0x0,0x70000005,0x0,0x70000005,0x0,0x70000005,0x0,0x70000005,0x0,0x70000005,0x0,0x70000005,0x0,0x70000005,0x0,0x70000005,0x0,0x70000005,0x0,0x70000005,0x0,0x70000005,0x0,0x70000005,0x0,0x70000005,0x0,0x70000005,0x0,0x70000005,0x0,0x70000005,0x0,0x70000005,0x0,0x70000005,0x0,0x70000005,0x0,0x70000005,0x0,0x70000005,0x0,0x70000005,0x0,0x70000005,0x0,0x70000005,0x0,0x70000005,0x0,0x70000005,0x0,0x70000005,0x0,0x70000005,0x0,0x70000005,0x0,0x70000005,0x0,0x70000005,0x0,0x70000005,0x0,0x70000005,0x0,0x70000005,0x0,0x70000005,0x0,0x70000005,0x0,0x70000005,0x0,0x70000005,0x0,0x70000005,0x0,0x70000005,0x0,0x70000005,0x0,0x70000005,0x0,0x70000005,0x0,0x70000005,0x0,0x70000005,0x0,0x70000005,0x0,0x70000005,0x0,0x70000005,0x0,0x70000005,0x0,0x70000005,0x0,0x70000005,0x0,0x70000005,0x0,0x70000005,0x0,0x70000005,0x0,0x70000005,0x0,0x70000005,0x0,0x70000005,0x0,0x70000005,0x0,0x70000005,0x0,0x70000005,0x0,0x70000005,0x0,0x70000005,0x0,0x70000005,0x0,0x70000005,0x0,0x70000005,0x0,0x70000005,0x0,0x70000005,0x0,0x70000005,0x0,0x70000005,0x0,0x70000005,0x0,0x70000005,0x0,0x70000005,0x0,0x70000005,0x0,0x70000005,0x0,0x70000005,0x0,0x70000005,0x0,0x70000005,0x0,0x70000005,0x0,0x70000005,0x0,0x70000005,0x0,0x70000005,0x0,0x70000005,0x0,0x70000005,0x0,0x70000005,0x0,0x70000005,0x0,0x70000005,0x0,0x70000005,0x0,0x70000005,0x0,0x70000005,0x0,0x70000005,0x0,0x70000005,0x0,0x70000005,0x0,0x70000005,0x0,0x70000005,0x0,0x70000005,0x0,0x70000005,0x0,0x70000005,0x0,0x70000005,0x0,0x70000005,0x0,0x70000005,0x0,0x70000005,0x0,0x70000005,0x0,0x70000005,0x0,0x70000005,0x0,0x70000005,0x0,0x70000005,0x0,0x70000005,0x0,0x70000005,0x0,0x70000005,0x0,0x70000005,0x0,0x70000005,0x0,0x70000005,0x0,0x70000005,0x0,0x70000005,0x0,0x70000005,0x0,0x70000005,0x0,0x70000005,0x0,0x70000005,0x0,0x70000005,0x0,0x70000005,0x0,0x70000005,0x0,0x70000005,0x0,0x70000005,0x0,0x70000005,0x0,0x70000005,0x0,0x70000005,0x0,0x70000005,0x0,0x70000005,0x0,0x70000005,0x0,0x70000005,0x0,0x70000005,0x0,0x70000005,0x0,0x70000005,0x0,0x70000005,0x0,0x70000005,0x0,0x70000005,0x0,0x70000005,0x0,0x70000005,0x0,0x70000005,0x0,0x70000005,0x0,0x70000005,0x0,0x70000005,0x0,0x70000005,0x0,0x70000005,0x0,0x70000005,0x0,0x70000005,0x0,0x70000005,0x0,0x70000005,0x0,0x70000005,0x0,0x70000005,0x0,0x70000005,0x0,0x70000005,0x0,0x70000005,0x0,0x70000005,0x0,0x70000005,0x0,0x70000005,0x0,0x70000005,0x0,0x70000005,0x0,0x70000005,0x0,0x70000005,0x0,0x70000005,0x0,0x70000005,0x0,0x70000005,0x0,0x70000005,0x0,0x70000005,0x0,0x70000005,0x0,0x70000005,0x0,0x70000005,0x0,0x70000005,0x0,0x70000005,0x0,0x70000005,0x0,0x70000005,0x0,0x70000005,0x0,0x70000005,0x0,0x70000005,0x0,0x70000005,0x0,0x70000005,0x0,0x70000005,0x0},
  {0x220021,0x1,0x220012,0x1,0x220026,0x1,0x220017,0x1,0x22002b,0x1,0x22001c,0x1,0x320021,0x1,0x320012,0x1,0x320026,0x1,0x320017,0x1,0x32002b,0x1,0x32001c,0x1,0x20002,0x1,0x20005,0x1,0x2000a,0x1,0x2000d,0x1,0x420021,0x1,0x420012,0x1,0x420026,0x1,0x420017,0x1,0x42002b,0x1,0x42001c,0x1,0x120002,0x1,0x120005,0x1,0x12000a,0x1,0x12000d,0x1,0x520021,0x1,0x520012,0x1,0x520026,0x1,0x520017,0x1,0x52002b,0x1,0x52001c,0x1,0x220002,0x1,0x220005,0x1,0x22000a,0x1,0x22000d,0x1,0x620021,0x1,0x620012,0x1,0x620026,0x1,0x620017,0x1,0x62002b,0x1,0x62001c,0x1,0x320002,0x1,0x320005,0x1,0x32000a,0x1,0x32000d,0x1,0x720021,0x1,0x720012,0x1,0x720026,0x1,0x720017,0x1,0x72002b,0x1,0x72001c,0x1,0x420002,0x1,0x420005,0x1,0x42000a,0x1,0x42000d,0x1,0x820021,0x1,0x820012,0x1,0x820026,0x1,0x820017,0x1,0x82002b,0x1,0x82001c,0x1,0x520002,0x1,0x520005,0x1,0x52000a,0x1,0x52000d,0x1,0x920021,0x1,0x920012,0x1,0x920026,0x1,0x920017,0x1,0x92002b,0x1,0x92001c,0x1,0x620002,0x1,0x620005,0x1,0x62000a,0x1,0x62000d,0x1,0xa20021,0x1,0xa20012,0x1,0xa20026,0x1,0xa20017,0x1,0xa2002b,0x1,0xa2001c,0x1,0x720002,0x1,0x720005,0x1,0x72000a,0x1,0x72000d,0x1,0xb20021,0x1,0xb20012,0x1,0xb20026,0x1,0xb20017,0x1,0xb2002b,0x1,0xb2001c,0x1,0x820002,0x1,0x820005,0x1,0x82000a,0x1,0x82000d,0x1,0x20020002,0x1200501,0x20020203,0x2200501,0x20020600,0x4200501,0xc20021,0x1,0xc20012,0x1,0xc20026,0x1,0xc20017,0x1,0xc2002b,0x1,0xc2001c,0x1,0x920002,0x1,0x920005,0x1,0x92000a,0x1,0x92000d,0x1,0xd20021,0x1,0xd20012,0x1,0xd20026,0x1,0xd20017,0x1,0xd2002b,0x1,0xd2001c,0x1,0xa20002,0x1,0xa20005,0x1,0xa2000a,0x1,0xa2000d,0x1,0xe20021,0x1,0xe20012,0x1,0xe20026,0x1,0xe20017,0x1,0xe2002b,0x1,0xe2001c,0x1,0xb20002,0x1,0xb20005,0x1,0xb2000a,0x1,0x20120002,0x9200d01,0x20120203,0xa200d01,0x20120600,0xc200d01,0x20220002,0x11201501,0x20220203,0x12201501,0x20220600,0x14201501,0xb2000d,0x1,0xc20002,0x1,0xc20005,0x1,0xc2000a,0x1,0xc2000d,0x1,0xf20021,0x1,0xf20012,0x1,0xf20026,0x1,0xf20017,0x1,0xf2002b,0x1,0xf2001c,0x1,0xd20002,0x1,0xd20005,0x1,0xd2000a,0x1,0xd2000d,0x1,0xe20002,0x1,0xe20005,0x1,0xe2000a,0x1,0xe2000d,0x1,0xf20002,0x1,0xf20005,0x1,0xf2000a,0x1,0xf2000d,0x1,0x20220003,0x11201501,0x20220200,0x12201501,0x20220400,0x13201501,0x30108000,0xd100e08,0x30108008,0xd100e08,0x30108010,0xd100e08,0x20320002,0x19201d01,0x20320203,0x1a201d01,0x20320600,0x1c201d01,0x30208018,0x15101608,0x30208020,0x15101608,0x30208028,0x15101608,0x10400020,0x12208,0x10400028,0x12208,0x10400030,0x12208,0x10400038,0x12208,0x10400040,0x12308,0x10400048,0x12308,0x10400050,0x12308,0x10400058,0x12308,0x10400060,0x12408,0x10400068,0x12408,0x10400070,0x12408,0x10400078,0x12408,0x20320200,0x1a201d01,0x20320401,0x1b201d01,0x20320602,0x1c201d01,0xc20024,0x1,0xc20015,0x1,0xc20029,0x1,0xc2001a,0x1,0xc2002e,0x1,0xc2001f,0x1,0x920000,0x1,0x920008,0x1,0x4022000b,0x16801701,0x4022000a,0x16801701,0x4022000c,0x16801701,0x20420600,0x24202501,0x20420603,0x24202501,0x10500000,0x12908,0x5001c402,0x70e0801,0x5001c603,0x70e0801,0x5001c202,0x70e0801,0x5001c203,0x70e0801,0x5001c403,0x70e0801,0x5001c600,0x70e0801,0x5001c601,0x70e0801,0x30308000,0x1d101e08,0x30308008,0x1d101e08,0x30308010,0x1d101e08,0x30308018,0x1d101e08,0x30308020,0x1d101e08,0x20520601,0x2c202d01,0x20520602,0x2c202d01,0x5011c000,0xf0e1001,0x5011c201,0xf0e1001,0x5011c402,0xf0e1001,0x5011c603,0xf0e1001,0x40320001,0x1e801f01,0x40320006,0x1e801f01,0x4032000b,0x1e801f01,0x5021c000,0x170e1801,0x5021c201,0x170e1801,0x5021c402,0x170e1801,0x5021c603,0x170e1801,0x40320002,0x1e801f01,0x40320003,0x1e801f01,0x70000005,0x0,0x70000005,0x0,0x70000005,0x0,0x70000005,0x0,0x70000005,0x0,0x70000005,0x0,0x70000005,0x0,0x70000005,0x0,0x70000005,0x0,0x70000005,0x0,0x70000005,0x0,0x70000005,0x0,0x70000005,0x0,0x70000005,0x0,0x70000005,0x0,0x70000005,0x0,0x70000005,0x0,0x70000005,0x0,0x70000005,0x0,0x70000005,0x0,0x70000005,0x0,0x70000005,0x0,0x70000005,0x0,0x70000005,0x0,0x70000005,0x0,0x70000005,0x0,0x70000005,0x0,0x70000005,0x0,0x70000005,0x0,0x70000005,0x0,0x70000005,0x0,0x70000005,0x0,0x70000005,0x0,0x70000005,0x0,0x70000005,0x0,0x70000005,0x0,0x70000005,0x0,0x70000005,0x0,0x70000005,0x0,0x70000005,0x0,0x70000005,0x0,0x70000005,0x0,0x70000005,0x0,0x70000005,0x0,0x70000005,0x0,0x70000005,0x0,0x70000005,0x0,0x70000005,0x0,0x70000005,0x0,0x70000005,0x0,0x70000005,0x0,0x70000005,0x0,0x70000005,0x0,0x70000005,0x0,0x70000005,0x0,0x70000005,0x0,0x70000005,0x0,0x70000005,0x0,0x70000005,0x0,0x70000005,0x0,0x70000005,0x0,0x70000005,0x0,0x70000005,0x0,0x70000005,0x0,0x70000005,0x0,0x70000005,0x0,0x70000005,0x0,0x70000005,0x0,0x70000005,0x0,0x70000005,0x0,0x70000005,0x0,0x70000005,0x0,0x70000005,0x0,0x70000005,0x0,0x70000005,0x0,0x70000005,0x0,0x70000005,0x0,0x70000005,0x0,0x70000005,0x0,0x70000005,0x0,0x70000005,0x0,0x70000005,0x0,0x70000005,0x0,0x70000005,0x0,0x70000005,0x0,0x70000005,0x0,0x70000005,0x0,0x70000005,0x0,0x70000005,0x0,0x70000005,0x0,0x70000005,0x0,0x70000005,0x0,0x70000005,0x0,0x70000005,0x0,0x70000005,0x0,0x70000005,0x0,0x70000005,0x0,0x70000005,0x0,0x70000005,0x0,0x70000005,0x0,0x70000005,0x0,0x70000005,0x0,0x70000005,0x0,0x70000005,0x0,0x70000005,0x0,0x70000005,0x0,0x70000005,0x0,0x70000005,0x0,0x70000005,0x0,0x70000005,0x0,0x70000005,0x0,0x70000005,0x0,0x70000005,0x0,0x70000005,0x0,0x70000005,0x0,0x70000005,0x0,0x70000005,0x0,0x70000005,0x0,0x70000005,0x0,0x70000005,0x0,0x70000005,0x0,0x70000005,0x0,0x70000005,0x0,0x70000005,0x0,0x70000005,0x0,0x70000005,0x0,0x70000005,0x0,0x70000005,0x0,0x70000005,0x0,0x70000005,0x0,0x70000005,0x0,0x70000005,0x0,0x70000005,0x0,0x70000005,0x0,0x70000005,0x0,0x70000005,0x0,0x70000005,0x0,0x70000005,0x0,0x70000005,0x0,0x70000005,0x0,0x70000005,0x0,0x70000005,0x0,0x70000005,0x0,0x70000005,0x0,0x70000005,0x0,0x70000005,0x0,0x70000005,0x0,0x70000005,0x0,0x70000005,0x0,0x70000005,0x0,0x70000005,0x0,0x70000005,0x0,0x70000005,0x0,0x70000005,0x0,0x70000005,0x0,0x70000005,0x0,0x70000005,0x0,0x70000005,0x0,0x70000005,0x0,0x70000005,0x0,0x70000005,0x0,0x70000005,0x0,0x70000005,0x0,0x70000005,0x0,0x70000005,0x0,0x70000005,0x0,0x70000005,0x0,0x70000005,0x0,0x70000005,0x0,0x70000005,0x0,0x70000005,0x0,0x70000005,0x0,0x70000005,0x0,0x70000005,0x0,0x70000005,0x0,0x70000005,0x0,0x70000005,0x0,0x70000005,0x0,0x70000005,0x0,0x70000005,0x0,0x70000005,0x0,0x70000005,0x0,0x70000005,0x0,0x70000005,0x0,0x70000005,0x0,0x70000005,0x0,0x70000005,0x0,0x70000005,0x0,0x70000005,0x0,0x70000005,0x0,0x70000005,0x0,0x70000005,0x0,0x70000005,0x0,0x70000005,0x0,0x70000005,0x0,0x70000005,0x0,0x70000005,0x0,0x70000005,0x0,0x70000005,0x0,0x70000005,0x0,0x70000005,0x0,0x70000005,0x0,0x70000005,0x0,0x70000005,0x0,0x70000005,0x0,0x70000005,0x0,0x70000005,0x0,0x70000005,0x0,0x70000005,0x0,0x70000005,0x0,0x70000005,0x0,0x70000005,0x0,0x70000005,0x0,0x70000005,0x0,0x70000005,0x0,0x70000005,0x0,0x70000005,0x0,0x70000005,0x0,0x70000005,0x0,0x70000005,0x0,0x70000005,0x0,0x70000005,0x0,0x70000005,0x0,0x70000005,0x0,0x70000005,0x0,0x70000005,0x0,0x70000005,0x0,0x70000005,0x0,0x70000005,0x0,0x70000005,0x0,0x70000005,0x0,0x70000005,0x0,0x70000005,0x0,0x70000005,0x0,0x70000005,0x0,0x70000005,0x0,0x70000005,0x0,0x70000005,0x0,0x70000005,0x0,0x70000005,0x0,0x70000005,0x0,0x70000005,0x0,0x70000005,0x0,0x70000005,0x0,0x70000005,0x0,0x70000005,0x0,0x70000005,0x0,0x70000005,0x0,0x70000005,0x0,0x70000005,0x0,0x70000005,0x0,0x70000005,0x0,0x70000005,0x0,0x70000005,0x0,0x70000005,0x0,0x70000005,0x0,0x70000005,0x0,0x70000005,0x0,0x70000005,0x0,0x70000005,0x0,0x70000005,0x0,0x70000005,0x0,0x70000005,0x0,0x70000005,0x0,0x70000005,0x0,0x70000005,0x0,0x70000005,0x0,0x70000005,0x0,0x70000005,0x0,0x70000005,0x0,0x70000005,0x0,0x70000005,0x0,0x70000005,0x0,0x70000005,0x0,0x70000005,0x0,0x70000005,0x0,0x70000005,0x0,0x70000005,0x0,0x70000005,0x0,0x70000005,0x0,0x70000005,0x0,0x70000005,0x0,0x70000005,0x0,0x70000005,0x0,0x70000005,0x0,0x70000005,0x0,0x70000005,0x0,0x70000005,0x0,0x70000005,0x0,0x70000005,0x0,0x70000005,0x0,0x70000005,0x0,0x70000005,0x0,0x70000005,0x0,0x70000005,0x0,0x70000005,0x0,0x70000005,0x0,0x70000005,0x0,0x70000005,0x0,0x70000005,0x0,0x70000005,0x0,0x70000005,0x0,0x70000005,0x0,0x70000005,0x0,0x70000005,0x0,0x70000005,0x0,0x70000005,0x0,0x70000005,0x0,0x70000005,0x0,0x70000005,0x0,0x70000005,0x0,0x70000005,0x0,0x70000005,0x0,0x70000005,0x0,0x70000005,0x0,0x70000005,0x0,0x70000005,0x0,0x70000005,0x0,0x70000005,0x0,0x70000005,0x0,0x70000005,0x0,0x70000005,0x0,0x70000005,0x0,0x70000005,0x0,0x70000005,0x0,0x70000005,0x0,0x70000005,0x0,0x70000005,0x0,0x70000005,0x0,0x70000005,0x0,0x70000005,0x0,0x70000005,0x0,0x70000005,0x0,0x70000005,0x0,0x70000005,0x0,0x70000005,0x0,0x70000005,0x0,0x70000005,0x0,0x70000005,0x0,0x70000005,0x0,0x70000005,0x0,0x70000005,0x0,0x70000005,0x0,0x70000005,0x0,0x70000005,0x0,0x70000005,0x0,0x70000005,0x0,0x70000005,0x0,0x70000005,0x0,0x70000005,0x0,0x70000005,0x0,0x70000005,0x0,0x70000005,0x0,0x70000005,0x0,0x70000005,0x0,0x70000005,0x0,0x70000005,0x0,0x70000005,0x0,0x70000005,0x0,0x70000005,0x0,0x70000005,0x0,0x70000005,0x0,0x70000005,0x0,0x70000005,0x0,0x70000005,0x0,0x70000005,0x0,0x70000005,0x0,0x70000005,0x0,0x70000005,0x0,0x70000005,0x0,0x70000005,0x0,0x70000005,0x0,0x70000005,0x0,0x70000005,0x0,0x70000005,0x0,0x70000005,0x0,0x70000005,0x0,0x70000005,0x0,0x70000005,0x0,0x70000005,0x0,0x70000005,0x0,0x70000005,0x0,0x70000005,0x0,0x70000005,0x0,0x70000005,0x0,0x70000005,0x0,0x70000005,0x0,0x70000005,0x0,0x70000005,0x0,0x70000005,0x0,0x70000005,0x0,0x70000005,0x0,0x70000005,0x0,0x70000005,0x0,0x70000005,0x0,0x70000005,0x0,0x70000005,0x0,0x70000005,0x0,0x70000005,0x0,0x70000005,0x0,0x70000005,0x0,0x70000005,0x0,0x70000005,0x0,0x70000005,0x0,0x70000005,0x0,0x70000005,0x0,0x70000005,0x0,0x70000005,0x0,0x70000005,0x0,0x70000005,0x0,0x70000005,0x0,0x70000005,0x0,0x70000005,0x0,0x70000005,0x0,0x70000005,0x0,0x70000005,0x0,0x70000005,0x0,0x70000005,0x0,0x70000005,0x0,0x70000005,0x0,0x70000005,0x0,0x70000005,0x0,0x70000005,0x0,0x70000005,0x0,0x70000005,0x0,0x70000005,0x0,0x70000005,0x0,0x70000005,0x0,0x70000005,0x0,0x70000005,0x0,0x70000005,0x0,0x70000005,0x0,0x70000005,0x0,0x70000005,0x0,0x70000005,0x0,0x70000005,0x0,0x70000005,0x0,0x70000005,0x0,0x70000005,0x0,0x70000005,0x0,0x70000005,0x0,0x70000005,0x0,0x70000005,0x0,0x70000005,0x0,0x70000005,0x0,0x70000005,0x0,0x70000005,0x0,0x70000005,0x0,0x70000005,0x0,0x70000005,0x0,0x70000005,0x0,0x70000005,0x0,0x70000005,0x0,0x70000005,0x0,0x70000005,0x0,0x70000005,0x0,0x70000005,0x0,0x70000005,0x0,0x70000005,0x0,0x70000005,0x0,0x70000005,0x0,0x70000005,0x0,0x70000005,0x0,0x70000005,0x0,0x70000005,0x0,0x70000005,0x0,0x70000005,0x0,0x70000005,0x0,0x70000005,0x0,0x70000005,0x0,0x70000005,0x0,0x70000005,0x0,0x70000005,0x0,0x70000005,0x0,0x70000005,0x0,0x70000005,0x0,0x70000005,0x0,0x70000005,0x0,0x70000005,0x0,0x70000005,0x0,0x70000005,0x0,0x70000005,0x0,0x70000005,0x0,0x70000005,0x0,0x70000005,0x0,0x70000005,0x0,0x70000005,0x0,0x70000005,0x0,0x70000005,0x0,0x70000005,0x0,0x70000005,0x0,0x70000005,0x0,0x70000005,0x0,0x70000005,0x0,0x70000005,0x0,0x70000005,0x0,0x70000005,0x0,0x70000005,0x0,0x70000005,0x0,0x70000005,0x0,0x70000005,0x0,0x70000005,0x0,0x70000005,0x0,0x70000005,0x0,0x70000005,0x0,0x70000005,0x0,0x70000005,0x0,0x70000005,0x0,0x70000005,0x0,0x70000005,0x0,0x70000005,0x0,0x70000005,0x0,0x70000005,0x0,0x70000005,0x0,0x70000005,0x0,0x70000005,0x0,0x70000005,0x0,0x70000005,0x0,0x70000005,0x0,0x70000005,0x0,0x70000005,0x0,0x70000005,0x0,0x70000005,0x0,0x70000005,0x0,0x70000005,0x0,0x70000005,0x0,0x70000005,0x0,0x70000005,0x0,0x70000005,0x0,0x70000005,0x0,0x70000005,0x0,0x70000005,0x0,0x70000005,0x0,0x70000005,0x0,0x70000005,0x0,0x70000005,0x0,0x70000005,0x0,0x70000005,0x0,0x70000005,0x0,0x70000005,0x0,0x70000005,0x0,0x70000005,0x0,0x70000005,0x0,0x70000005,0x0,0x70000005,0x0,0x70000005,0x0,0x70000005,0x0,0x70000005,0x0,0x70000005,0x0,0x70000005,0x0,0x70000005,0x0,0x70000005,0x0,0x70000005,0x0,0x70000005,0x0,0x70000005,0x0,0x70000005,0x0,0x70000005,0x0,0x70000005,0x0,0x70000005,0x0,0x70000005,0x0,0x70000005,0x0,0x70000005,0x0,0x70000005,0x0,0x70000005,0x0,0x70000005,0x0,0x70000005,0x0,0x70000005,0x0,0x70000005,0x0,0x70000005,0x0,0x70000005,0x0,0x70000005,0x0,0x70000005,0x0,0x70000005,0x0,0x70000005,0x0,0x70000005,0x0,0x70000005,0x0,0x70000005,0x0,0x70000005,0x0,0x70000005,0x0,0x70000005,0x0,0x70000005,0x0,0x70000005,0x0,0x70000005,0x0,0x70000005,0x0,0x70000005,0x0,0x70000005,0x0,0x70000005,0x0,0x70000005,0x0,0x70000005,0x0,0x70000005,0x0,0x70000005,0x0,0x70000005,0x0,0x70000005,0x0,0x70000005,0x0,0x70000005,0x0,0x70000005,0x0,0x70000005,0x0,0x70000005,0x0,0x70000005,0x0,0x70000005,0x0,0x70000005,0x0,0x70000005,0x0,0x70000005,0x0,0x70000005,0x0,0x70000005,0x0,0x70000005,0x0,0x70000005,0x0,0x70000005,0x0,0x70000005,0x0,0x70000005,0x0,0x70000005,0x0,0x70000005,0x0,0x70000005,0x0,0x70000005,0x0,0x70000005,0x0,0x70000005,0x0,0x70000005,0x0,0x70000005,0x0,0x70000005,0x0,0x70000005,0x0,0x70000005,0x0,0x70000005,0x0,0x70000005,0x0,0x70000005,0x0,0x70000005,0x0,0x70000005,0x0,0x70000005,0x0,0x70000005,0x0,0x70000005,0x0,0x70000005,0x0,0x70000005,0x0,0x70000005,0x0,0x70000005,0x0,0x70000005,0x0,0x70000005,0x0,0x70000005,0x0,0x70000005,0x0,0x70000005,0x0,0x70000005,0x0,0x70000005,0x0,0x70000005,0x0,0x70000005,0x0,0x70000005,0x0,0x70000005,0x0,0x70000005,0x0,0x70000005,0x0,0x70000005,0x0,0x70000005,0x0,0x70000005,0x0,0x70000005,0x0,0x70000005,0x0,0x70000005,0x0,0x70000005,0x0,0x70000005,0x0,0x70000005,0x0,0x70000005,0x0,0x70000005,0x0,0x70000005,0x0,0x70000005,0x0,0x70000005,0x0,0x70000005,0x0,0x70000005,0x0,0x70000005,0x0,0x70000005,0x0,0x70000005,0x0,0x70000005,0x0,0x70000005,0x0,0x70000005,0x0,0x70000005,0x0,0x70000005,0x0,0x70000005,0x0,0x70000005,0x0,0x70000005,0x0,0x70000005,0x0,0x70000005,0x0,0x70000005,0x0,0x70000005,0x0,0x70000005,0x0,0x70000005,0x0,0x70000005,0x0,0x70000005,0x0,0x70000005,0x0,0x70000005,0x0,0x70000005,0x0,0x70000005,0x0,0x70000005,0x0,0x70000005,0x0,0x70000005,0x0,0x70000005,0x0,0x70000005,0x0,0x70000005,0x0,0x70000005,0x0,0x70000005,0x0,0x70000005,0x0,0x70000005,0x0,0x70000005,0x0,0x70000005,0x0,0x70000005,0x0,0x70000005,0x0,0x70000005,0x0,0x70000005,0x0,0x70000005,0x0,0x70000005,0x0,0x70000005,0x0,0x70000005,0x0,0x70000005,0x0,0x70000005,0x0,0x70000005,0x0,0x70000005,0x0,0x70000005,0x0,0x70000005,0x0,0x70000005,0x0,0x70000005,0x0,0x70000005,0x0,0x70000005,0x0,0x70000005,0x0,0x70000005,0x0,0x70000005,0x0,0x70000005,0x0,0x70000005,0x0,0x70000005,0x0,0x70000005,0x0,0x70000005,0x0,0x70000005,0x0,0x70000005,0x0,0x70000005,0x0,0x70000005,0x0,0x70000005,0x0,0x70000005,0x0,0x70000005,0x0,0x70000005,0x0,0x70000005,0x0,0x70000005,0x0,0x70000005,0x0,0x70000005,0x0,0x70000005,0x0,0x70000005,0x0,0x70000005,0x0,0x70000005,0x0,0x70000005,0x0,0x70000005,0x0,0x70000005,0x0,0x70000005,0x0,0x70000005,0x0,0x70000005,0x0,0x70000005,0x0,0x70000005,0x0,0x70000005,0x0,0x70000005,0x0,0x70000005,0x0,0x70000005,0x0,0x70000005,0x0,0x70000005,0x0,0x70000005,0x0,0x70000005,0x0,0x70000005,0x0,0x70000005,0x0,0x70000005,0x0,0x70000005,0x0,0x70000005,0x0,0x70000005,0x0,0x70000005,0x0,0x70000005,0x0,0x70000005,0x0,0x70000005,0x0,0x70000005,0x0,0x70000005,0x0,0x70000005,0x0,0x70000005,0x0,0x70000005,0x0,0x70000005,0x0,0x70000005,0x0,0x70000005,0x0,0x70000005,0x0,0x70000005,0x0,0x70000005,0x0,0x70000005,0x0,0x70000005,0x0,0x70000005,0x0,0x70000005,0x0,0x70000005,0x0,0x70000005,0x0,0x70000005,0x0,0x70000005,0x0,0x70000005,0x0,0x70000005,0x0,0x70000005,0x0,0x70000005,0x0,0x70000005,0x0,0x70000005,0x0,0x70000005,0x0,0x70000005,0x0,0x70000005,0x0,0x70000005,0x0,0x70000005,0x0,0x70000005,0x0,0x70000005,0x0,0x70000005,0x0,0x70000005,0x0,0x70000005,0x0,0x70000005,0x0,0x70000005,0x0,0x70000005,0x0,0x70000005,0x0,0x70000005,0x0,0x70000005,0x0,0x70000005,0x0,0x70000005,0x0,0x70000005,0x0,0x70000005,0x0,0x70000005,0x0,0x70000005,0x0,0x70000005,0x0,0x70000005,0x0,0x70000005,0x0,0x70000005,0x0,0x70000005,0x0,0x70000005,0x0,0x70000005,0x0,0x70000005,0x0,0x70000005,0x0,0x70000005,0x0,0x70000005,0x0,0x70000005,0x0,0x70000005,0x0,0x70000005,0x0,0x70000005,0x0,0x70000005,0x0,0x70000005,0x0,0x70000005,0x0,0x70000005,0x0,0x70000005,0x0,0x70000005,0x0,0x70000005,0x0,0x70000005,0x0,0x70000005,0x0,0x70000005,0x0,0x70000005,0x0,0x70000005,0x0,0x70000005,0x0,0x70000005,0x0,0x70000005,0x0,0x70000005,0x0,0x70000005,0x0,0x70000005,0x0,0x70000005,0x0,0x70000005,0x0,0x70000005,0x0,0x70000005,0x0,0x70000005,0x0,0x70000005,0x0,0x70000005,0x0,0x70000005,0x0,0x70000005,0x0,0x70000005,0x0,0x70000005,0x0,0x70000005,0x0,0x70000005,0x0,0x70000005,0x0,0x70000005,0x0,0x70000005,0x0,0x70000005,0x0,0x70000005,0x0,0x70000005,0x0,0x70000005,0x0,0x70000005,0x0,0x70000005,0x0,0x70000005,0x0,0x70000005,0x0,0x70000005,0x0,0x70000005,0x0,0x70000005,0x0,0x70000005,0x0,0x70000005,0x0,0x70000005,0x0,0x70000005,0x0,0x70000005,0x0,0x70000005,0x0,0x70000005,0x0,0x70000005,0x0,0x70000005,0x0,0x70000005,0x0,0x70000005,0x0,0x70000005,0x0,0x70000005,0x0,0x70000005,0x0,0x70000005,0x0,0x70000005,0x0,0x70000005,0x0,0x70000005,0x0,0x70000005,0x0,0x70000005,0x0,0x70000005,0x0,0x70000005,0x0,0x70000005,0x0,0x70000005,0x0,0x70000005,0x0,0x70000005,0x0,0x70000005,0x0,0x70000005,0x0,0x70000005,0x0,0x70000005,0x0,0x70000005,0x0,0x70000005,0x0,0x70000005,0x0,0x70000005,0x0,0x70000005,0x0,0x70000005,0x0,0x70000005,0x0,0x70000005,0x0,0x70000005,0x0,0x70000005,0x0,0x70000005,0x0,0x70000005,0x0,0x70000005,0x0,0x70000005,0x0,0x70000005,0x0,0x70000005,0x0,0x70000005,0x0,0x70000005,0x0,0x70000005,0x0,0x70000005,0x0,0x70000005,0x0,0x70000005,0x0,0x70000005,0x0,0x70000005,0x0,0x70000005,0x0,0x70000005,0x0,0x70000005,0x0,0x70000005,0x0,0x70000005,0x0,0x70000005,0x0,0x70000005,0x0,0x70000005,0x0,0x70000005,0x0,0x70000005,0x0,0x70000005,0x0,0x70000005,0x0,0x70000005,0x0,0x70000005,0x0,0x70000005,0x0,0x70000005,0x0,0x70000005,0x0,0x70000005,0x0,0x70000005,0x0,0x70000005,0x0,0x70000005,0x0,0x70000005,0x0,0x70000005,0x0,0x70000005,0x0,0x70000005,0x0,0x70000005,0x0,0x70000005,0x0,0x70000005,0x0,0x70000005,0x0,0x70000005,0x0,0x70000005,0x0,0x70000005,0x0,0x70000005,0x0,0x70000005,0x0,0x70000005,0x0,0x70000005,0x0,0x70000005,0x0,0x70000005,0x0,0x70000005,0x0,0x70000005,0x0,0x70000005,0x0,0x70000005,0x0,0x70000005,0x0,0x70000005,0x0,0x70000005,0x0,0x70000005,0x0,0x70000005,0x0,0x70000005,0x0,0x70000005,0x0,0x70000005,0x0,0x70000005,0x0,0x70000005,0x0,0x70000005,0x0,0x70000005,0x0,0x70000005,0x0,0x70000005,0x0,0x70000005,0x0,0x70000005,0x0,0x70000005,0x0,0x70000005,0x0,0x70000005,0x0,0x70000005,0x0,0x70000005,0x0,0x70000005,0x0,0x70000005,0x0,0x70000005,0x0,0x70000005,0x0,0x70000005,0x0,0x70000005,0x0,0x70000005,0x0,0x70000005,0x0,0x70000005,0x0,0x70000005,0x0,0x70000005,0x0,0x70000005,0x0,0x70000005,0x0,0x70000005,0x0,0x70000005,0x0,0x70000005,0x0,0x70000005,0x0,0x70000005,0x0,0x70000005,0x0,0x70000005,0x0,0x70000005,0x0,0x70000005,0x0,0x70000005,0x0,0x70000005,0x0,0x70000005,0x0,0x70000005,0x0,0x70000005,0x0,0x70000005,0x0,0x70000005,0x0,0x70000005,0x0,0x70000005,0x0,0x70000005,0x0,0x70000005,0x0,0x70000005,0x0,0x70000005,0x0,0x70000005,0x0,0x70000005,0x0,0x70000005,0x0,0x70000005,0x0,0x70000005,0x0,0x70000005,0x0,0x70000005,0x0,0x70000005,0x0,0x70000005,0x0,0x70000005,0x0,0x70000005,0x0,0x70000005,0x0,0x70000005,0x0,0x70000005,0x0,0x70000005,0x0,0x70000005,0x0,0x70000005,0x0,0x70000005,0x0,0x70000005,0x0,0x70000005,0x0,0x70000005,0x0,0x70000005,0x0,0x70000005,0x0,0x70000005,0x0,0x70000005,0x0,0x70000005,0x0,0x70000005,0x0,0x70000005,0x0,0x70000005,0x0,0x70000005,0x0,0x70000005,0x0,0x70000005,0x0,0x70000005,0x0,0x70000005,0x0,0x70000005,0x0,0x70000005,0x0,0x70000005,0x0,0x70000005,0x0,0x70000005,0x0,0x70000005,0x0,0x70000005,0x0,0x70000005,0x0,0x70000005,0x0,0x70000005,0x0,0x70000005,0x0,0x70000005,0x0,0x70000005,0x0,0x70000005,0x0,0x70000005,0x0,0x70000005,0x0,0x70000005,0x0,0x70000005,0x0,0x70000005,0x0,0x70000005,0x0,0x70000005,0x0,0x70000005,0x0,0x70000005,0x0,0x70000005,0x0,0x70000005,0x0,0x70000005,0x0,0x70000005,0x0,0x70000005,0x0,0x70000005,0x0,0x70000005,0x0,0x70000005,0x0,0x70000005,0x0,0x70000005,0x0,0x70000005,0x0,0x70000005,0x0,0x70000005,0x0,0x70000005,0x0,0x70000005,0x0,0x70000005,0x0,0x70000005,0x0,0x70000005,0x0,0x70000005,0x0,0x70000005,0x0,0x70000005,0x0,0x70000005,0x0,0x70000005,0x0,0x70000005,0x0,0x70000005,0x0,0x70000005,0x0,0x70000005,0x0,0x70000005,0x0,0x70000005,0x0,0x70000005,0x0,0x70000005,0x0,0x70000005,0x0,0x70000005,0x0,0x70000005,0x0,0x70000005,0x0,0x70000005,0x0,0x70000005,0x0,0x70000005,0x0,0x70000005,0x0,0x70000005,0x0,0x70000005,0x0,0x70000005,0x0,0x70000005,0x0,0x70000005,0x0,0x70000005,0x0,0x70000005,0x0,0x70000005,0x0,0x70000005,0x0,0x70000005,0x0,0x70000005,0x0,0x70000005,0x0,0x70000005,0x0,0x70000005,0x0,0x70000005,0x0,0x70000005,0x0,0x70000005,0x0,0x70000005,0x0,0x70000005,0x0,0x70000005,0x0,0x70000005,0x0,0x70000005,0x0,0x70000005,0x0,0x70000005,0x0,0x70000005,0x0,0x70000005,0x0,0x70000005,0x0,0x70000005,0x0,0x70000005,0x0,0x70000005,0x0,0x70000005,0x0,0x70000005,0x0,0x70000005,0x0,0x70000005,0x0,0x70000005,0x0,0x70000005,0x0,0x70000005,0x0,0x70000005,0x0,0x70000005,0x0,0x70000005,0x0,0x70000005,0x0,0x70000005,0x0,0x70000005,0x0,0x70000005,0x0,0x70000005,0x0,0x70000005,0x0,0x70000005,0x0,0x70000005,0x0,0x70000005,0x0,0x70000005,0x0,0x70000005,0x0,0x70000005,0x0,0x70000005,0x0,0x70000005,0x0,0x70000005,0x0,0x70000005,0x0,0x70000005,0x0,0x70000005,0x0,0x70000005,0x0,0x70000005,0x0,0x70000005,0x0,0x70000005,0x0,0x70000005,0x0,0x70000005,0x0,0x70000005,0x0,0x70000005,0x0,0x70000005,0x0,0x70000005,0x0,0x70000005,0x0,0x70000005,0x0,0x70000005,0x0,0x70000005,0x0,0x70000005,0x0,0x70000005,0x0,0x70000005,0x0,0x70000005,0x0,0x70000005,0x0,0x70000005,0x0,0x70000005,0x0,0x70000005,0x0,0x70000005,0x0,0x70000005,0x0,0x70000005,0x0,0x70000005,0x0,0x70000005,0x0,0x70000005,0x0,0x70000005,0x0,0x70000005,0x0,0x70000005,0x0,0x70000005,0x0,0x70000005,0x0,0x70000005,0x0,0x70000005,0x0,0x70000005,0x0,0x70000005,0x0,0x70000005,0x0,0x70000005,0x0,0x70000005,0x0,0x70000005,0x0,0x70000005,0x0,0x70000005,0x0,0x70000005,0x0,0x70000005,0x0,0x70000005,0x0,0x70000005,0x0,0x70000005,0x0,0x70000005,0x0,0x70000005,0x0,0x70000005,0x0,0x70000005,0x0,0x70000005,0x0,0x70000005,0x0,0x70000005,0x0,0x70000005,0x0,0x70000005,0x0,0x70000005,0x0,0x70000005,0x0,0x70000005,0x0,0x70000005,0x0,0x70000005,0x0,0x70000005,0x0,0x70000005,0x0,0x70000005,0x0,0x70000005,0x0,0x70000005,0x0,0x70000005,0x0,0x70000005,0x0,0x70000005,0x0,0x70000005,0x0,0x70000005,0x0,0x70000005,0x0,0x70000005,0x0,0x70000005,0x0,0x70000005,0x0,0x70000005,0x0,0x70000005,0x0,0x70000005,0x0,0x70000005,0x0,0x70000005,0x0,0x70000005,0x0,0x70000005,0x0,0x70000005,0x0,0x70000005,0x0,0x70000005,0x0,0x70000005,0x0,0x70000005,0x0,0x70000005,0x0,0x70000005,0x0,0x70000005,0x0,0x70000005,0x0,0x70000005,0x0,0x70000005,0x0,0x70000005,0x0,0x70000005,0x0,0x70000005,0x0,0x70000005,0x0,0x70000005,0x0,0x70000005,0x0,0x70000005,0x0,0x70000005,0x0,0x70000005,0x0,0x70000005,0x0,0x70000005,0x0,0x70000005,0x0,0x70000005,0x0,0x70000005,0x0,0x70000005,0x0,0x70000005,0x0,0x70000005,0x0,0x70000005,0x0,0x70000005,0x0,0x70000005,0x0,0x70000005,0x0,0x70000005,0x0},
  {0x220022,0x1,0x220013,0x1,0x220027,0x1,0x220018,0x1,0x22002c,0x1,0x22001d,0x1,0x320022,0x1,0x320013,0x1,0x320027,0x1,0x320018,0x1,0x32002c,0x1,0x32001d,0x1,0x20003,0x1,0x20006,0x1,0x2000b,0x1,0x2000e,0x1,0x420022,0x1,0x420013,0x1,0x420027,0x1,0x420018,0x1,0x42002c,0x1,0x42001d,0x1,0x120003,0x1,0x120006,0x1,0x12000b,0x1,0x12000e,0x1,0x520022,0x1,0x520013,0x1,0x520027,0x1,0x520018,0x1,0x52002c,0x1,0x52001d,0x1,0x220003,0x1,0x220006,0x1,0x22000b,0x1,0x22000e,0x1,0x620022,0x1,0x620013,0x1,0x620027,0x1,0x620018,0x1,0x62002c,0x1,0x62001d,0x1,0x320003,0x1,0x320006,0x1,0x32000b,0x1,0x32000e,0x1,0x720022,0x1,0x720013,0x1,0x720027,0x1,0x720018,0x1,0x72002c,0x1,0x72001d,0x1,0x420003,0x1,0x420006,0x1,0x42000b,0x1,0x42000e,0x1,0x820022,0x1,0x820013,0x1,0x820027,0x1,0x820018,0x1,0x82002c,0x1,0x82001d,0x1,0x520003,0x1,0x520006,0x1,0x52000b,0x1,0x52000e,0x1,0x920022,0x1,0x920013,0x1,0x920027,0x1,0x920018,0x1,0x92002c,0x1,0x92001d,0x1,0x620003,0x1,0x620006,0x1,0x62000b,0x1,0x62000e,0x1,0xa20022,0x1,0xa20013,0x1,0xa20027,0x1,0xa20018,0x1,0xa2002c,0x1,0xa2001d,0x1,0x720003,0x1,0x720006,0x1,0x72000b,0x1,0x72000e,0x1,0xb20022,0x1,0xb20013,0x1,0xb20027,0x1,0xb20018,0x1,0xb2002c,0x1,0xb2001d,0x1,0x820003,0x1,0x820006,0x1,0x82000b,0x1,0x82000e,0x1,0x20020003,0x1200501,0x20020400,0x3200501,0x20020601,0x4200501,0xc20022,0x1,0xc20013,0x1,0xc20027,0x1,0xc20018,0x1,0xc2002c,0x1,0xc2001d,0x1,0x920003,0x1,0x920006,0x1,0x92000b,0x1,0x92000e,0x1,0xd20022,0x1,0xd20013,0x1,0xd20027,0x1,0xd20018,0x1,0xd2002c,0x1,0xd2001d,0x1,0xa20003,0x1,0xa20006,0x1,0xa2000b,0x1,0xa2000e,0x1,0xe20022,0x1,0xe20013,0x1,0xe20027,0x1,0xe20018,0x1,0xe2002c,0x1,0xe2001d,0x1,0xb20003,0x1,0xb20006,0x1,0xb2000b,0x1,0x20120003,0x9200d01,0x20120400,0xb200d01,0x20120601,0xc200d01,0x20220601,0x14201501,0xb2000e,0x1,0xc20003,0x1,0xc20006,0x1,0xc2000b,0x1,0xc2000e,0x1,0xf20022,0x1,0xf20013,0x1,0xf20027,0x1,0xf20018,0x1,0xf2002c,0x1,0xf2001d,0x1,0xd20003,0x1,0xd20006,0x1,0xd2000b,0x1,0xd2000e,0x1,0xe20003,0x1,0xe20006,0x1,0xe2000b,0x1,0xe2000e,0x1,0xf20003,0x1,0xf20006,0x1,0xf2000b,0x1,0xf2000e,0x1,0x20220401,0x13201501,0x20220602,0x14201501,0xa20010,0x1,0xa20024,0x1,0xa20015,0x1,0x30108018,0xd100e08,0x30108020,0xd100e08,0x30108028,0xd100e08,0x20320003,0x19201d01,0x20320400,0x1b201d01,0x20320601,0x1c201d01,0xd20010,0x1,0xd20024,0x1,0xd20015,0x1,0xd20029,0x1,0xd2001a,0x1,0xd2002e,0x1,0xd2001f,0x1,0xa20000,0x1,0xa20008,0x1,0xe20010,0x1,0xe20024,0x1,0xe20015,0x1,0xe20029,0x1,0xe2001a,0x1,0xe2002e,0x1,0xe2001f,0x1,0xb20000,0x1,0xb20008,0x1,0xc20000,0x1,0xc20008,0x1,0xf20010,0x1,0xf20024,0x1,0xf20015,0x1,0xf20029,0x1,0xf2001a,0x1,0xf2002e,0x1,0x20420003,0x21202501,0x20420400,0x23202501,0x20420601,0x24202501,0x5011c001,0xf0e1001,0x5011c202,0xf0e1001,0x5011c403,0xf0e1001,0x40320007,0x1e801f01,0x4032000c,0x1e801f01,0x5021c001,0x170e1801,0x5021c202,0x170e1801,0x5021c403,0x170e1801,0x40320004,0x1e801f01,0x40320008,0x1e801f01,0x40320009,0x1e801f01,0x4032000d,0x1e801f01,0x5021c002,0x170e1801,0x5021c003,0x170e1801,0x5021c203,0x170e1801,0x5021c400,0x170e1801,0x5021c600,0x170e1801,0x5021c601,0x170e1801,0x10600000,0x13108,0x10600008,0x13108,0x10600010,0x13108,0x10600018,0x13108,0x10600020,0x13208,0x10600028,0x13208,0x10600030,0x13208,0x10600038,0x13208,0x10600040,0x13308,0x10600048,0x13308,0x10600050,0x13308,0x70000005,0x0,0x70000005,0x0,0x70000005,0x0,0x70000005,0x0,0x70000005,0x0,0x70000005,0x0,0x70000005,0x0,0x70000005,0x0,0x70000005,0x0,0x70000005,0x0,0x70000005,0x0,0x70000005,0x0,0x70000005,0x0,0x70000005,0x0,0x70000005,0x0,0x70000005,0x0,0x70000005,0x0,0x70000005,0x0,0x70000005,0x0,0x70000005,0x0,0x70000005,0x0,0x70000005,0x0,0x70000005,0x0,0x70000005,0x0,0x70000005,0x0,0x70000005,0x0,0x70000005,0x0,0x70000005,0x0,0x70000005,0x0,0x70000005,0x0,0x70000005,0x0,0x70000005,0x0,0x70000005,0x0,0x70000005,0x0,0x70000005,0x0,0x70000005,0x0,0x70000005,0x0,0x70000005,0x0,0x70000005,0x0,0x70000005,0x0,0x70000005,0x0,0x70000005,0x0,0x70000005,0x0,0x70000005,0x0,0x70000005,0x0,0x70000005,0x0,0x70000005,0x0,0x70000005,0x0,0x70000005,0x0,0x70000005,0x0,0x70000005,0x0,0x70000005,0x0,0x70000005,0x0,0x70000005,0x0,0x70000005,0x0,0x70000005,0x0,0x70000005,0x0,0x70000005,0x0,0x70000005,0x0,0x70000005,0x0,0x70000005,0x0,0x70000005,0x0,0x70000005,0x0,0x70000005,0x0,0x70000005,0x0,0x70000005,0x0,0x70000005,0x0,0x70000005,0x0,0x70000005,0x0,0x70000005,0x0,0x70000005,0x0,0x70000005,0x0,0x70000005,0x0,0x70000005,0x0,0x70000005,0x0,0x70000005,0x0,0x70000005,0x0,0x70000005,0x0,0x70000005,0x0,0x70000005,0x0,0x70000005,0x0,0x70000005,0x0,0x70000005,0x0,0x70000005,0x0,0x70000005,0x0,0x70000005,0x0,0x70000005,0x0,0x70000005,0x0,0x70000005,0x0,0x70000005,0x0,0x70000005,0x0,0x70000005,0x0,0x70000005,0x0,0x70000005,0x0,0x70000005,0x0,0x70000005,0x0,0x70000005,0x0,0x70000005,0x0,0x70000005,0x0,0x70000005,0x0,0x70000005,0x0,0x70000005,0x0,0x70000005,0x0,0x70000005,0x0,0x70000005,0x0,0x70000005,0x0,0x70000005,0x0,0x70000005,0x0,0x70000005,0x0,0x70000005,0x0,0x70000005,0x0,0x70000005,0x0,0x70000005,0x0,0x70000005,0x0,0x70000005,0x0,0x70000005,0x0,0x70000005,0x0,0x70000005,0x0,0x70000005,0x0,0x70000005,0x0,0x70000005,0x0,0x70000005,0x0,0x70000005,0x0,0x70000005,0x0,0x70000005,0x0,0x70000005,0x0,0x70000005,0x0,0x70000005,0x0,0x70000005,0x0,0x70000005,0x0,0x70000005,0x0,0x70000005,0x0,0x70000005,0x0,0x70000005,0x0,0x70000005,0x0,0x70000005,0x0,0x70000005,0x0,0x70000005,0x0,0x70000005,0x0,0x70000005,0x0,0x70000005,0x0,0x70000005,0x0,0x70000005,0x0,0x70000005,0x0,0x70000005,0x0,0x70000005,0x0,0x70000005,0x0,0x70000005,0x0,0x70000005,0x0,0x70000005,0x0,0x70000005,0x0,0x70000005,0x0,0x70000005,0x0,0x70000005,0x0,0x70000005,0x0,0x70000005,0x0,0x70000005,0x0,0x70000005,0x0,0x70000005,0x0,0x70000005,0x0,0x70000005,0x0,0x70000005,0x0,0x70000005,0x0,0x70000005,0x0,0x70000005,0x0,0x70000005,0x0,0x70000005,0x0,0x70000005,0x0,0x70000005,0x0,0x70000005,0x0,0x70000005,0x0,0x70000005,0x0,0x70000005,0x0,0x70000005,0x0,0x70000005,0x0,0x70000005,0x0,0x70000005,0x0,0x70000005,0x0,0x70000005,0x0,0x70000005,0x0,0x70000005,0x0,0x70000005,0x0,0x70000005,0x0,0x70000005,0x0,0x70000005,0x0,0x70000005,0x0,0x70000005,0x0,0x70000005,0x0,0x70000005,0x0,0x70000005,0x0,0x70000005,0x0,0x70000005,0x0,0x70000005,0x0,0x70000005,0x0,0x70000005,0x0,0x70000005,0x0,0x70000005,0x0,0x70000005,0x0,0x70000005,0x0,0x70000005,0x0,0x70000005,0x0,0x70000005,0x0,0x70000005,0x0,0x70000005,0x0,0x70000005,0x0,0x70000005,0x0,0x70000005,0x0,0x70000005,0x0,0x70000005,0x0,0x70000005,0x0,0x70000005,0x0,0x70000005,0x0,0x70000005,0x0,0x70000005,0x0,0x70000005,0x0,0x70000005,0x0,0x70000005,0x0,0x70000005,0x0,0x70000005,0x0,0x70000005,0x0,0x70000005,0x0,0x70000005,0x0,0x70000005,0x0,0x70000005,0x0,0x70000005,0x0,0x70000005,0x0,0x70000005,0x0,0x70000005,0x0,0x70000005,0x0,0x70000005,0x0,0x70000005,0x0,0x70000005,0x0,0x70000005,0x0,0x70000005,0x0,0x70000005,0x0,0x70000005,0x0,0x70000005,0x0,0x70000005,0x0,0x70000005,0x0,0x70000005,0x0,0x70000005,0x0,0x70000005,0x0,0x70000005,0x0,0x70000005,0x0,0x70000005,0x0,0x70000005,0x0,0x70000005,0x0,0x70000005,0x0,0x70000005,0x0,0x70000005,0x0,0x70000005,0x0,0x70000005,0x0,0x70000005,0x0,0x70000005,0x0,0x70000005,0x0,0x70000005,0x0,0x70000005,0x0,0x70000005,0x0,0x70000005,0x0,0x70000005,0x0,0x70000005,0x0,0x70000005,0x0,0x70000005,0x0,0x70000005,0x0,0x70000005,0x0,0x70000005,0x0,0x70000005,0x0,0x70000005,0x0,0x70000005,0x0,0x70000005,0x0,0x70000005,0x0,0x70000005,0x0,0x70000005,0x0,0x70000005,0x0,0x70000005,0x0,0x70000005,0x0,0x70000005,0x0,0x70000005,0x0,0x70000005,0x0,0x70000005,0x0,0x70000005,0x0,0x70000005,0x0,0x70000005,0x0,0x70000005,0x0,0x70000005,0x0,0x70000005,0x0,0x70000005,0x0,0x70000005,0x0,0x70000005,0x0,0x70000005,0x0,0x70000005,0x0,0x70000005,0x0,0x70000005,0x0,0x70000005,0x0,0x70000005,0x0,0x70000005,0x0,0x70000005,0x0,0x70000005,0x0,0x70000005,0x0,0x70000005,0x0,0x70000005,0x0,0x70000005,0x0,0x70000005,0x0,0x70000005,0x0,0x70000005,0x0,0x70000005,0x0,0x70000005,0x0,0x70000005,0x0,0x70000005,0x0,0x70000005,0x0,0x70000005,0x0,0x70000005,0x0,0x70000005,0x0,0x70000005,0x0,0x70000005,0x0,0x70000005,0x0,0x70000005,0x0,0x70000005,0x0,0x70000005,0x0,0x70000005,0x0,0x70000005,0x0,0x70000005,0x0,0x70000005,0x0,0x70000005,0x0,0x70000005,0x0,0x70000005,0x0,0x70000005,0x0,0x70000005,0x0,0x70000005,0x0,0x70000005,0x0,0x70000005,0x0,0x70000005,0x0,0x70000005,0x0,0x70000005,0x0,0x70000005,0x0,0x70000005,0x0,0x70000005,0x0,0x70000005,0x0,0x70000005,0x0,0x70000005,0x0,0x70000005,0x0,0x70000005,0x0,0x70000005,0x0,0x70000005,0x0,0x70000005,0x0,0x70000005,0x0,0x70000005,0x0,0x70000005,0x0,0x70000005,0x0,0x70000005,0x0,0x70000005,0x0,0x70000005,0x0,0x70000005,0x0,0x70000005,0x0,0x70000005,0x0,0x70000005,0x0,0x70000005,0x0,0x70000005,0x0,0x70000005,0x0,0x70000005,0x0,0x70000005,0x0,0x70000005,0x0,0x70000005,0x0,0x70000005,0x0,0x70000005,0x0,0x70000005,0x0,0x70000005,0x0,0x70000005,0x0,0x70000005,0x0,0x70000005,0x0,0x70000005,0x0,0x70000005,0x0,0x70000005,0x0,0x70000005,0x0,0x70000005,0x0,0x70000005,0x0,0x70000005,0x0,0x70000005,0x0,0x70000005,0x0,0x70000005,0x0,0x70000005,0x0,0x70000005,0x0,0x70000005,0x0,0x70000005,0x0,0x70000005,0x0,0x70000005,0x0,0x70000005,0x0,0x70000005,0x0,0x70000005,0x0,0x70000005,0x0,0x70000005,0x0,0x70000005,0x0,0x70000005,0x0,0x70000005,0x0,0x70000005,0x0,0x70000005,0x0,0x70000005,0x0,0x70000005,0x0,0x70000005,0x0,0x70000005,0x0,0x70000005,0x0,0x70000005,0x0,0x70000005,0x0,0x70000005,0x0,0x70000005,0x0,0x70000005,0x0,0x70000005,0x0,0x70000005,0x0,0x70000005,0x0,0x70000005,0x0,0x70000005,0x0,0x70000005,0x0,0x70000005,0x0,0x70000005,0x0,0x70000005,0x0,0x70000005,0x0,0x70000005,0x0,0x70000005,0x0,0x70000005,0x0,0x70000005,0x0,0x70000005,0x0,0x70000005,0x0,0x70000005,0x0,0x70000005,0x0,0x70000005,0x0,0x70000005,0x0,0x70000005,0x0,0x70000005,0x0,0x70000005,0x0,0x70000005,0x0,0x70000005,0x0,0x70000005,0x0,0x70000005,0x0,0x70000005,0x0,0x70000005,0x0,0x70000005,0x0,0x70000005,0x0,0x70000005,0x0,0x70000005,0x0,0x70000005,0x0,0x70000005,0x0,0x70000005,0x0,0x70000005,0x0,0x70000005,0x0,0x70000005,0x0,0x70000005,0x0,0x70000005,0x0,0x70000005,0x0,0x70000005,0x0,0x70000005,0x0,0x70000005,0x0,0x70000005,0x0,0x70000005,0x0,0x70000005,0x0,0x70000005,0x0,0x70000005,0x0,0x70000005,0x0,0x70000005,0x0,0x70000005,0x0,0x70000005,0x0,0x70000005,0x0,0x70000005,0x0,0x70000005,0x0,0x70000005,0x0,0x70000005,0x0,0x70000005,0x0,0x70000005,0x0,0x70000005,0x0,0x70000005,0x0,0x70000005,0x0,0x70000005,0x0,0x70000005,0x0,0x70000005,0x0,0x70000005,0x0,0x70000005,0x0,0x70000005,0x0,0x70000005,0x0,0x70000005,0x0,0x70000005,0x0,0x70000005,0x0,0x70000005,0x0,0x70000005,0x0,0x70000005,0x0,0x70000005,0x0,0x70000005,0x0,0x70000005,0x0,0x70000005,0x0,0x70000005,0x0,0x70000005,0x0,0x70000005,0x0,0x70000005,0x0,0x70000005,0x0,0x70000005,0x0,0x70000005,0x0,0x70000005,0x0,0x70000005,0x0,0x70000005,0x0,0x70000005,0x0,0x70000005,0x0,0x70000005,0x0,0x70000005,0x0,0x70000005,0x0,0x70000005,0x0,0x70000005,0x0,0x70000005,0x0,0x70000005,0x0,0x70000005,0x0,0x70000005,0x0,0x70000005,0x0,0x70000005,0x0,0x70000005,0x0,0x70000005,0x0,0x70000005,0x0,0x70000005,0x0,0x70000005,0x0,0x70000005,0x0,0x70000005,0x0,0x70000005,0x0,0x70000005,0x0,0x70000005,0x0,0x70000005,0x0,0x70000005,0x0,0x70000005,0x0,0x70000005,0x0,0x70000005,0x0,0x70000005,0x0,0x70000005,0x0,0x70000005,0x0,0x70000005,0x0,0x70000005,0x0,0x70000005,0x0,0x70000005,0x0,0x70000005,0x0,0x70000005,0x0,0x70000005,0x0,0x70000005,0x0,0x70000005,0x0,0x70000005,0x0,0x70000005,0x0,0x70000005,0x0,0x70000005,0x0,0x70000005,0x0,0x70000005,0x0,0x70000005,0x0,0x70000005,0x0,0x70000005,0x0,0x70000005,0x0,0x70000005,0x0,0x70000005,0x0,0x70000005,0x0,0x70000005,0x0,0x70000005,0x0,0x70000005,0x0,0x70000005,0x0,0x70000005,0x0,0x70000005,0x0,0x70000005,0x0,0x70000005,0x0,0x70000005,0x0,0x70000005,0x0,0x70000005,0x0,0x70000005,0x0,0x70000005,0x0,0x70000005,0x0,0x70000005,0x0,0x70000005,0x0,0x70000005,0x0,0x70000005,0x0,0x70000005,0x0,0x70000005,0x0,0x70000005,0x0,0x70000005,0x0,0x70000005,0x0,0x70000005,0x0,0x70000005,0x0,0x70000005,0x0,0x70000005,0x0,0x70000005,0x0,0x70000005,0x0,0x70000005,0x0,0x70000005,0x0,0x70000005,0x0,0x70000005,0x0,0x70000005,0x0,0x70000005,0x0,0x70000005,0x0,0x70000005,0x0,0x70000005,0x0,0x70000005,0x0,0x70000005,0x0,0x70000005,0x0,0x70000005,0x0,0x70000005,0x0,0x70000005,0x0,0x70000005,0x0,0x70000005,0x0,0x70000005,0x0,0x70000005,0x0,0x70000005,0x0,0x70000005,0x0,0x70000005,0x0,0x70000005,0x0,0x70000005,0x0,0x70000005,0x0,0x70000005,0x0,0x70000005,0x0,0x70000005,0x0,0x70000005,0x0,0x70000005,0x0,0x70000005,0x0,0x70000005,0x0,0x70000005,0x0,0x70000005,0x0,0x70000005,0x0,0x70000005,0x0,0x70000005,0x0,0x70000005,0x0,0x70000005,0x0,0x70000005,0x0,0x70000005,0x0,0x70000005,0x0,0x70000005,0x0,0x70000005,0x0,0x70000005,0x0,0x70000005,0x0,0x70000005,0x0,0x70000005,0x0,0x70000005,0x0,0x70000005,0x0,0x70000005,0x0,0x70000005,0x0,0x70000005,0x0,0x70000005,0x0,0x70000005,0x0,0x70000005,0x0,0x70000005,0x0,0x70000005,0x0,0x70000005,0x0,0x70000005,0x0,0x70000005,0x0,0x70000005,0x0,0x70000005,0x0,0x70000005,0x0,0x70000005,0x0,0x70000005,0x0,0x70000005,0x0,0x70000005,0x0,0x70000005,0x0,0x70000005,0x0,0x70000005,0x0,0x70000005,0x0,0x70000005,0x0,0x70000005,0x0,0x70000005,0x0,0x70000005,0x0,0x70000005,0x0,0x70000005,0x0,0x70000005,0x0,0x70000005,0x0,0x70000005,0x0,0x70000005,0x0,0x70000005,0x0,0x70000005,0x0,0x70000005,0x0,0x70000005,0x0,0x70000005,0x0,0x70000005,0x0,0x70000005,0x0,0x70000005,0x0,0x70000005,0x0,0x70000005,0x0,0x70000005,0x0,0x70000005,0x0,0x70000005,0x0,0x70000005,0x0,0x70000005,0x0,0x70000005,0x0,0x70000005,0x0,0x70000005,0x0,0x70000005,0x0,0x70000005,0x0,0x70000005,0x0,0x70000005,0x0,0x70000005,0x0,0x70000005,0x0,0x70000005,0x0,0x70000005,0x0,0x70000005,0x0,0x70000005,0x0,0x70000005,0x0,0x70000005,0x0,0x70000005,0x0,0x70000005,0x0,0x70000005,0x0,0x70000005,0x0,0x70000005,0x0,0x70000005,0x0,0x70000005,0x0,0x70000005,0x0,0x70000005,0x0,0x70000005,0x0,0x70000005,0x0,0x70000005,0x0,0x70000005,0x0,0x70000005,0x0,0x70000005,0x0,0x70000005,0x0,0x70000005,0x0,0x70000005,0x0,0x70000005,0x0,0x70000005,0x0,0x70000005,0x0,0x70000005,0x0,0x70000005,0x0,0x70000005,0x0,0x70000005,0x0,0x70000005,0x0,0x70000005,0x0,0x70000005,0x0,0x70000005,0x0,0x70000005,0x0,0x70000005,0x0,0x70000005,0x0,0x70000005,0x0,0x70000005,0x0,0x70000005,0x0,0x70000005,0x0,0x70000005,0x0,0x70000005,0x0,0x70000005,0x0,0x70000005,0x0,0x70000005,0x0,0x70000005,0x0,0x70000005,0x0,0x70000005,0x0,0x70000005,0x0,0x70000005,0x0,0x70000005,0x0,0x70000005,0x0,0x70000005,0x0,0x70000005,0x0,0x70000005,0x0,0x70000005,0x0,0x70000005,0x0,0x70000005,0x0,0x70000005,0x0,0x70000005,0x0,0x70000005,0x0,0x70000005,0x0,0x70000005,0x0,0x70000005,0x0,0x70000005,0x0,0x70000005,0x0,0x70000005,0x0,0x70000005,0x0,0x70000005,0x0,0x70000005,0x0,0x70000005,0x0,0x70000005,0x0,0x70000005,0x0,0x70000005,0x0,0x70000005,0x0,0x70000005,0x0,0x70000005,0x0,0x70000005,0x0,0x70000005,0x0,0x70000005,0x0,0x70000005,0x0,0x70000005,0x0,0x70000005,0x0,0x70000005,0x0,0x70000005,0x0,0x70000005,0x0,0x70000005,0x0,0x70000005,0x0,0x70000005,0x0,0x70000005,0x0,0x70000005,0x0,0x70000005,0x0,0x70000005,0x0,0x70000005,0x0,0x70000005,0x0,0x70000005,0x0,0x70000005,0x0,0x70000005,0x0,0x70000005,0x0,0x70000005,0x0,0x70000005,0x0,0x70000005,0x0,0x70000005,0x0,0x70000005,0x0,0x70000005,0x0,0x70000005,0x0,0x70000005,0x0,0x70000005,0x0,0x70000005,0x0,0x70000005,0x0,0x70000005,0x0,0x70000005,0x0,0x70000005,0x0,0x70000005,0x0,0x70000005,0x0,0x70000005,0x0,0x70000005,0x0,0x70000005,0x0,0x70000005,0x0,0x70000005,0x0,0x70000005,0x0,0x70000005,0x0,0x70000005,0x0,0x70000005,0x0,0x70000005,0x0,0x70000005,0x0,0x70000005,0x0,0x70000005,0x0,0x70000005,0x0,0x70000005,0x0,0x70000005,0x0,0x70000005,0x0,0x70000005,0x0,0x70000005,0x0,0x70000005,0x0,0x70000005,0x0,0x70000005,0x0,0x70000005,0x0,0x70000005,0x0,0x70000005,0x0,0x70000005,0x0,0x70000005,0x0,0x70000005,0x0,0x70000005,0x0,0x70000005,0x0,0x70000005,0x0,0x70000005,0x0,0x70000005,0x0,0x70000005,0x0,0x70000005,0x0,0x70000005,0x0,0x70000005,0x0,0x70000005,0x0,0x70000005,0x0,0x70000005,0x0,0x70000005,0x0,0x70000005,0x0,0x70000005,0x0,0x70000005,0x0,0x70000005,0x0,0x70000005,0x0,0x70000005,0x0,0x70000005,0x0,0x70000005,0x0,0x70000005,0x0,0x70000005,0x0,0x70000005,0x0,0x70000005,0x0,0x70000005,0x0,0x70000005,0x0,0x70000005,0x0,0x70000005,0x0,0x70000005,0x0,0x70000005,0x0,0x70000005,0x0,0x70000005,0x0,0x70000005,0x0,0x70000005,0x0,0x70000005,0x0,0x70000005,0x0,0x70000005,0x0,0x70000005,0x0,0x70000005,0x0,0x70000005,0x0,0x70000005,0x0,0x70000005,0x0,0x70000005,0x0,0x70000005,0x0,0x70000005,0x0,0x70000005,0x0,0x70000005,0x0,0x70000005,0x0,0x70000005,0x0,0x70000005,0x0,0x70000005,0x0,0x70000005,0x0,0x70000005,0x0,0x70000005,0x0,0x70000005,0x0,0x70000005,0x0,0x70000005,0x0,0x70000005,0x0,0x70000005,0x0,0x70000005,0x0,0x70000005,0x0,0x70000005,0x0,0x70000005,0x0,0x70000005,0x0,0x70000005,0x0,0x70000005,0x0,0x70000005,0x0,0x70000005,0x0,0x70000005,0x0,0x70000005,0x0,0x70000005,0x0,0x70000005,0x0,0x70000005,0x0,0x70000005,0x0,0x70000005,0x0,0x70000005,0x0,0x70000005,0x0,0x70000005,0x0,0x70000005,0x0,0x70000005,0x0,0x70000005,0x0,0x70000005,0x0,0x70000005,0x0,0x70000005,0x0,0x70000005,0x0,0x70000005,0x0,0x70000005,0x0,0x70000005,0x0,0x70000005,0x0,0x70000005,0x0,0x70000005,0x0,0x70000005,0x0,0x70000005,0x0,0x70000005,0x0,0x70000005,0x0,0x70000005,0x0,0x70000005,0x0,0x70000005,0x0,0x70000005,0x0,0x70000005,0x0,0x70000005,0x0,0x70000005,0x0,0x70000005,0x0,0x70000005,0x0,0x70000005,0x0,0x70000005,0x0,0x70000005,0x0,0x70000005,0x0,0x70000005,0x0,0x70000005,0x0,0x70000005,0x0,0x70000005,0x0,0x70000005,0x0,0x70000005,0x0,0x70000005,0x0,0x70000005,0x0,0x70000005,0x0,0x70000005,0x0,0x70000005,0x0,0x70000005,0x0,0x70000005,0x0,0x70000005,0x0,0x70000005,0x0,0x70000005,0x0,0x70000005,0x0,0x70000005,0x0,0x70000005,0x0,0x70000005,0x0,0x70000005,0x0,0x70000005,0x0,0x70000005,0x0,0x70000005,0x0,0x70000005,0x0,0x70000005,0x0,0x70000005,0x0,0x70000005,0x0,0x70000005,0x0,0x70000005,0x0,0x70000005,0x0,0x70000005,0x0,0x70000005,0x0,0x70000005,0x0,0x70000005,0x0,0x70000005,0x0,0x70000005,0x0,0x70000005,0x0,0x70000005,0x0,0x70000005,0x0,0x70000005,0x0,0x70000005,0x0,0x70000005,0x0,0x70000005,0x0,0x70000005,0x0,0x70000005,0x0,0x70000005,0x0,0x70000005,0x0,0x70000005,0x0,0x70000005,0x0,0x70000005,0x0,0x70000005,0x0,0x70000005,0x0,0x70000005,0x0,0x70000005,0x0,0x70000005,0x0,0x70000005,0x0,0x70000005,0x0,0x70000005,0x0,0x70000005,0x0,0x70000005,0x0,0x70000005,0x0,0x70000005,0x0,0x70000005,0x0,0x70000005,0x0,0x70000005,0x0,0x70000005,0x0,0x70000005,0x0,0x70000005,0x0,0x70000005,0x0,0x70000005,0x0,0x70000005,0x0,0x70000005,0x0,0x70000005,0x0,0x70000005,0x0,0x70000005,0x0,0x70000005,0x0,0x70000005,0x0,0x70000005,0x0,0x70000005,0x0,0x70000005,0x0,0x70000005,0x0,0x70000005,0x0,0x70000005,0x0,0x70000005,0x0,0x70000005,0x0,0x70000005,0x0,0x70000005,0x0,0x70000005,0x0,0x70000005,0x0,0x70000005,0x0,0x70000005,0x0,0x70000005,0x0,0x70000005,0x0,0x70000005,0x0,0x70000005,0x0,0x70000005,0x0,0x70000005,0x0,0x70000005,0x0,0x70000005,0x0,0x70000005,0x0,0x70000005,0x0,0x70000005,0x0,0x70000005,0x0,0x70000005,0x0,0x70000005,0x0,0x70000005,0x0,0x70000005,0x0,0x70000005,0x0,0x70000005,0x0,0x70000005,0x0,0x70000005,0x0,0x70000005,0x0,0x70000005,0x0,0x70000005,0x0,0x70000005,0x0,0x70000005,0x0,0x70000005,0x0,0x70000005,0x0,0x70000005,0x0,0x70000005,0x0,0x70000005,0x0,0x70000005,0x0,0x70000005,0x0,0x70000005,0x0,0x70000005,0x0,0x70000005,0x0,0x70000005,0x0,0x70000005,0x0,0x70000005,0x0,0x70000005,0x0,0x70000005,0x0,0x70000005,0x0,0x70000005,0x0,0x70000005,0x0,0x70000005,0x0,0x70000005,0x0,0x70000005,0x0,0x70000005,0x0,0x70000005,0x0,0x70000005,0x0,0x70000005,0x0,0x70000005,0x0,0x70000005,0x0,0x70000005,0x0,0x70000005,0x0,0x70000005,0x0,0x70000005,0x0,0x70000005,0x0,0x70000005,0x0,0x70000005,0x0,0x70000005,0x0,0x70000005,0x0,0x70000005,0x0,0x70000005,0x0,0x70000005,0x0,0x70000005,0x0,0x70000005,0x0,0x70000005,0x0,0x70000005,0x0,0x70000005,0x0,0x70000005,0x0,0x70000005,0x0,0x70000005,0x0,0x70000005,0x0,0x70000005,0x0,0x70000005,0x0,0x70000005,0x0,0x70000005,0x0,0x70000005,0x0,0x70000005,0x0,0x70000005,0x0,0x70000005,0x0,0x70000005,0x0,0x70000005,0x0,0x70000005,0x0,0x70000005,0x0,0x70000005,0x0,0x70000005,0x0,0x70000005,0x0,0x70000005,0x0,0x70000005,0x0,0x70000005,0x0,0x70000005,0x0,0x70000005,0x0,0x70000005,0x0,0x70000005,0x0,0x70000005,0x0,0x70000005,0x0,0x70000005,0x0,0x70000005,0x0,0x70000005,0x0,0x70000005,0x0,0x70000005,0x0,0x70000005,0x0,0x70000005,0x0,0x70000005,0x0,0x70000005,0x0,0x70000005,0x0,0x70000005,0x0,0x70000005,0x0,0x70000005,0x0,0x70000005,0x0,0x70000005,0x0,0x70000005,0x0,0x70000005,0x0,0x70000005,0x0,0x70000005,0x0,0x70000005,0x0,0x70000005,0x0,0x70000005,0x0,0x70000005,0x0,0x70000005,0x0,0x70000005,0x0,0x70000005,0x0,0x70000005,0x0,0x70000005,0x0,0x70000005,0x0,0x70000005,0x0,0x70000005,0x0,0x70000005,0x0,0x70000005,0x0,0x70000005,0x0,0x70000005,0x0,0x70000005,0x0,0x70000005,0x0,0x70000005,0x0,0x70000005,0x0,0x70000005,0x0,0x70000005,0x0,0x70000005,0x0,0x70000005,0x0,0x70000005,0x0,0x70000005,0x0,0x70000005,0x0,0x70000005,0x0,0x70000005,0x0,0x70000005,0x0,0x70000005,0x0,0x70000005,0x0,0x70000005,0x0,0x70000005,0x0,0x70000005,0x0,0x70000005,0x0,0x70000005,0x0,0x70000005,0x0,0x70000005,0x0,0x70000005,0x0,0x70000005,0x0,0x70000005,0x0,0x70000005,0x0,0x70000005,0x0,0x70000005,0x0,0x70000005,0x0,0x70000005,0x0,0x70000005,0x0,0x70000005,0x0,0x70000005,0x0,0x70000005,0x0,0x70000005,0x0,0x70000005,0x0,0x70000005,0x0,0x70000005,0x0,0x70000005,0x0,0x70000005,0x0,0x70000005,0x0,0x70000005,0x0,0x70000005,0x0,0x70000005,0x0,0x70000005,0x0,0x70000005,0x0,0x70000005,0x0,0x70000005,0x0,0x70000005,0x0,0x70000005,0x0,0x70000005,0x0,0x70000005,0x0,0x70000005,0x0,0x70000005,0x0,0x70000005,0x0,0x70000005,0x0,0x70000005,0x0,0x70000005,0x0,0x70000005,0x0,0x70000005,0x0,0x70000005,0x0,0x70000005,0x0,0x70000005,0x0,0x70000005,0x0,0x70000005,0x0,0x70000005,0x0,0x70000005,0x0,0x70000005,0x0,0x70000005,0x0,0x70000005,0x0,0x70000005,0x0,0x70000005,0x0,0x70000005,0x0,0x70000005,0x0,0x70000005,0x0,0x70000005,0x0,0x70000005,0x0,0x70000005,0x0,0x70000005,0x0,0x70000005,0x0,0x70000005,0x0,0x70000005,0x0,0x70000005,0x0,0x70000005,0x0,0x70000005,0x0,0x70000005,0x0,0x70000005,0x0,0x70000005,0x0,0x70000005,0x0,0x70000005,0x0,0x70000005,0x0,0x70000005,0x0,0x70000005,0x0,0x70000005,0x0,0x70000005,0x0,0x70000005,0x0,0x70000005,0x0,0x70000005,0x0,0x70000005,0x0,0x70000005,0x0,0x70000005,0x0,0x70000005,0x0,0x70000005,0x0,0x70000005,0x0,0x70000005,0x0,0x70000005,0x0,0x70000005,0x0,0x70000005,0x0,0x70000005,0x0,0x70000005,0x0,0x70000005,0x0,0x70000005,0x0,0x70000005,0x0,0x70000005,0x0,0x70000005,0x0,0x70000005,0x0,0x70000005,0x0,0x70000005,0x0,0x70000005,0x0,0x70000005,0x0,0x70000005,0x0,0x70000005,0x0,0x70000005,0x0,0x70000005,0x0,0x70000005,0x0,0x70000005,0x0,0x70000005,0x0,0x70000005,0x0,0x70000005,0x0,0x70000005,0x0,0x70000005,0x0},
  {0x220023,0x1,0x220014,0x1,0x220028,0x1,0x220019,0x1,0x22002d,0x1,0x22001e,0x1,0x320023,0x1,0x320014,0x1,0x320028,0x1,0x320019,0x1,0x32002d,0x1,0x32001e,0x1,0x20004,0x1,0x20007,0x1,0x2000c,0x1,0x2000f,0x1,0x420023,0x1,0x420014,0x1,0x420028,0x1,0x420019,0x1,0x42002d,0x1,0x42001e,0x1,0x120004,0x1,0x120007,0x1,0x12000c,0x1,0x12000f,0x1,0x520023,0x1,0x520014,0x1,0x520028,0x1,0x520019,0x1,0x52002d,0x1,0x52001e,0x1,0x220004,0x1,0x220007,0x1,0x22000c,0x1,0x22000f,0x1,0x620023,0x1,0x620014,0x1,0x620028,0x1,0x620019,0x1,0x62002d,0x1,0x62001e,0x1,0x320004,0x1,0x320007,0x1,0x32000c,0x1,0x32000f,0x1,0x720023,0x1,0x720014,0x1,0x720028,0x1,0x720019,0x1,0x72002d,0x1,0x72001e,0x1,0x420004,0x1,0x420007,0x1,0x42000c,0x1,0x42000f,0x1,0x820023,0x1,0x820014,0x1,0x820028,0x1,0x820019,0x1,0x82002d,0x1,0x82001e,0x1,0x520004,0x1,0x520007,0x1,0x52000c,0x1,0x52000f,0x1,0x920023,0x1,0x920014,0x1,0x920028,0x1,0x920019,0x1,0x92002d,0x1,0x92001e,0x1,0x620004,0x1,0x620007,0x1,0x62000c,0x1,0x62000f,0x1,0xa20023,0x1,0xa20014,0x1,0xa20028,0x1,0xa20019,0x1,0xa2002d,0x1,0xa2001e,0x1,0x720004,0x1,0x720007,0x1,0x72000c,0x1,0x72000f,0x1,0xb20023,0x1,0xb20014,0x1,0xb20028,0x1,0xb20019,0x1,0xb2002d,0x1,0xb2001e,0x1,0x820004,0x1,0x820007,0x1,0x82000c,0x1,0x82000f,0x1,0x20020200,0x2200501,0x20020401,0x3200501,0x20020602,0x4200501,0xc20023,0x1,0xc20014,0x1,0xc20028,0x1,0xc20019,0x1,0xc2002d,0x1,0xc2001e,0x1,0x920004,0x1,0x920007,0x1,0x92000c,0x1,0x92000f,0x1,0xd20023,0x1,0xd20014,0x1,0xd20028,0x1,0xd20019,0x1,0xd2002d,0x1,0xd2001e,0x1,0xa20004,0x1,0xa20007,0x1,0xa2000c,0x1,0xa2000f,0x1,0xe20023,0x1,0xe20014,0x1,0xe20028,0x1,0xe20019,0x1,0xe2002d,0x1,0xe2001e,0x1,0xb20004,0x1,0xb20007,0x1,0xb2000c,0x1,0x20120200,0xa200d01,0x20120401,0xb200d01,0x20120602,0xc200d01,0xb2000f,0x1,0xc20004,0x1,0xc20007,0x1,0xc2000c,0x1,0xc2000f,0x1,0xf20023,0x1,0xf20014,0x1,0xf20028,0x1,0xf20019,0x1,0xf2002d,0x1,0xf2001e,0x1,0xd20004,0x1,0xd20007,0x1,0xd2000c,0x1,0xd2000f,0x1,0xe20004,0x1,0xe20007,0x1,0xe2000c,0x1,0xe2000f,0x1,0xf20004,0x1,0xf20007,0x1,0xf2000c,0x1,0xf2000f,0x1,0xa20029,0x1,0xa2001a,0x1,0xa2002e,0x1,0xa2001f,0x1,0x720000,0x1,0x720008,0x1,0x30108030,0xd100e08,0x30108038,0xd100e08,0x30108040,0xd100e08,0xf2001f,0x1,0xd20000,0x1,0xd20008,0x1,0xe20000,0x1,0xe20001,0x1,0xe20008,0x1,0xe20009,0x1,0xf20000,0x1,0xf20001,0x1,0xf20008,0x1,0xf20009,0x1,0x40120003,0xe800f01,0x40120008,0xe800f01,0x4012000d,0xe800f01,0x40120000,0xe800f01,0x40120001,0xe800f01,0x40120002,0xe800f01,0x40120004,0xe800f01,0x40120005,0xe800f01,0x40120006,0xe800f01,0x40120007,0xe800f01,0x40120009,0xe800f01,0x4012000a,0xe800f01,0x4012000b,0xe800f01,0x4012000c,0xe800f01,0x40220003,0x16801701,0x40220008,0x16801701,0x4022000d,0x16801701,0x40220000,0x16801701,0x20420200,0x22202501,0x20420401,0x23202501,0x20420602,0x24202501,0x10600058,0x13308,0x10600060,0x13408,0x10600068,0x13408,0x10600070,0x13408,0x10600078,0x13408,0x30408000,0x25102608,0x30408008,0x25102608,0x30408010,0x25102608,0x30408018,0x25102608,0x30408020,0x25102608,0x30408028,0x25102608,0x30408030,0x25102608,0x30408038,0x25102608,0x30408040,0x25102608,0x30408048,0x25102608,0x30408050,0x25102608,0x30408058,0x25102608,0x30408060,0x25102608,0x30408068,0x25102608,0x30408070,0x25102608,0x30408078,0x25102608,0x60008000,0x8100008,0x60008008,0x8100008,0x60008010,0x8100008,0x60008018,0x8100008,0x70000005,0x0,0x70000005,0x0,0x70000005,0x0,0x70000005,0x0,0x70000005,0x0,0x70000005,0x0,0x70000005,0x0,0x70000005,0x0,0x70000005,0x0,0x70000005,0x0,0x70000005,0x0,0x70000005,0x0,0x70000005,0x0,0x70000005,0x0,0x70000005,0x0,0x70000005,0x0,0x70000005,0x0,0x70000005,0x0,0x70000005,0x0,0x70000005,0x0,0x70000005,0x0,0x70000005,0x0,0x70000005,0x0,0x70000005,0x0,0x70000005,0x0,0x70000005,0x0,0x70000005,0x0,0x70000005,0x0,0x70000005,0x0,0x70000005,0x0,0x70000005,0x0,0x70000005,0x0,0x70000005,0x0,0x70000005,0x0,0x70000005,0x0,0x70000005,0x0,0x70000005,0x0,0x70000005,0x0,0x70000005,0x0,0x70000005,0x0,0x70000005,0x0,0x70000005,0x0,0x70000005,0x0,0x70000005,0x0,0x70000005,0x0,0x70000005,0x0,0x70000005,0x0,0x70000005,0x0,0x70000005,0x0,0x70000005,0x0,0x70000005,0x0,0x70000005,0x0,0x70000005,0x0,0x70000005,0x0,0x70000005,0x0,0x70000005,0x0,0x70000005,0x0,0x70000005,0x0,0x70000005,0x0,0x70000005,0x0,0x70000005,0x0,0x70000005,0x0,0x70000005,0x0,0x70000005,0x0,0x70000005,0x0,0x70000005,0x0,0x70000005,0x0,0x70000005,0x0,0x70000005,0x0,0x70000005,0x0,0x70000005,0x0,0x70000005,0x0,0x70000005,0x0,0x70000005,0x0,0x70000005,0x0,0x70000005,0x0,0x70000005,0x0,0x70000005,0x0,0x70000005,0x0,0x70000005,0x0,0x70000005,0x0,0x70000005,0x0,0x70000005,0x0,0x70000005,0x0,0x70000005,0x0,0x70000005,0x0,0x70000005,0x0,0x70000005,0x0,0x70000005,0x0,0x70000005,0x0,0x70000005,0x0,0x70000005,0x0,0x70000005,0x0,0x70000005,0x0,0x70000005,0x0,0x70000005,0x0,0x70000005,0x0,0x70000005,0x0,0x70000005,0x0,0x70000005,0x0,0x70000005,0x0,0x70000005,0x0,0x70000005,0x0,0x70000005,0x0,0x70000005,0x0,0x70000005,0x0,0x70000005,0x0,0x70000005,0x0,0x70000005,0x0,0x70000005,0x0,0x70000005,0x0,0x70000005,0x0,0x70000005,0x0,0x70000005,0x0,0x70000005,0x0,0x70000005,0x0,0x70000005,0x0,0x70000005,0x0,0x70000005,0x0,0x70000005,0x0,0x70000005,0x0,0x70000005,0x0,0x70000005,0x0,0x70000005,0x0,0x70000005,0x0,0x70000005,0x0,0x70000005,0x0,0x70000005,0x0,0x70000005,0x0,0x70000005,0x0,0x70000005,0x0,0x70000005,0x0,0x70000005,0x0,0x70000005,0x0,0x70000005,0x0,0x70000005,0x0,0x70000005,0x0,0x70000005,0x0,0x70000005,0x0,0x70000005,0x0,0x70000005,0x0,0x70000005,0x0,0x70000005,0x0,0x70000005,0x0,0x70000005,0x0,0x70000005,0x0,0x70000005,0x0,0x70000005,0x0,0x70000005,0x0,0x70000005,0x0,0x70000005,0x0,0x70000005,0x0,0x70000005,0x0,0x70000005,0x0,0x70000005,0x0,0x70000005,0x0,0x70000005,0x0,0x70000005,0x0,0x70000005,0x0,0x70000005,0x0,0x70000005,0x0,0x70000005,0x0,0x70000005,0x0,0x70000005,0x0,0x70000005,0x0,0x70000005,0x0,0x70000005,0x0,0x70000005,0x0,0x70000005,0x0,0x70000005,0x0,0x70000005,0x0,0x70000005,0x0,0x70000005,0x0,0x70000005,0x0,0x70000005,0x0,0x70000005,0x0,0x70000005,0x0,0x70000005,0x0,0x70000005,0x0,0x70000005,0x0,0x70000005,0x0,0x70000005,0x0,0x70000005,0x0,0x70000005,0x0,0x70000005,0x0,0x70000005,0x0,0x70000005,0x0,0x70000005,0x0,0x70000005,0x0,0x70000005,0x0,0x70000005,0x0,0x70000005,0x0,0x70000005,0x0,0x70000005,0x0,0x70000005,0x0,0x70000005,0x0,0x70000005,0x0,0x70000005,0x0,0x70000005,0x0,0x70000005,0x0,0x70000005,0x0,0x70000005,0x0,0x70000005,0x0,0x70000005,0x0,0x70000005,0x0,0x70000005,0x0,0x70000005,0x0,0x70000005,0x0,0x70000005,0x0,0x70000005,0x0,0x70000005,0x0,0x70000005,0x0,0x70000005,0x0,0x70000005,0x0,0x70000005,0x0,0x70000005,0x0,0x70000005,0x0,0x70000005,0x0,0x70000005,0x0,0x70000005,0x0,0x70000005,0x0,0x70000005,0x0,0x70000005,0x0,0x70000005,0x0,0x70000005,0x0,0x70000005,0x0,0x70000005,0x0,0x70000005,0x0,0x70000005,0x0,0x70000005,0x0,0x70000005,0x0,0x70000005,0x0,0x70000005,0x0,0x70000005,0x0,0x70000005,0x0,0x70000005,0x0,0x70000005,0x0,0x70000005,0x0,0x70000005,0x0,0x70000005,0x0,0x70000005,0x0,0x70000005,0x0,0x70000005,0x0,0x70000005,0x0,0x70000005,0x0,0x70000005,0x0,0x70000005,0x0,0x70000005,0x0,0x70000005,0x0,0x70000005,0x0,0x70000005,0x0,0x70000005,0x0,0x70000005,0x0,0x70000005,0x0,0x70000005,0x0,0x70000005,0x0,0x70000005,0x0,0x70000005,0x0,0x70000005,0x0,0x70000005,0x0,0x70000005,0x0,0x70000005,0x0,0x70000005,0x0,0x70000005,0x0,0x70000005,0x0,0x70000005,0x0,0x70000005,0x0,0x70000005,0x0,0x70000005,0x0,0x70000005,0x0,0x70000005,0x0,0x70000005,0x0,0x70000005,0x0,0x70000005,0x0,0x70000005,0x0,0x70000005,0x0,0x70000005,0x0,0x70000005,0x0,0x70000005,0x0,0x70000005,0x0,0x70000005,0x0,0x70000005,0x0,0x70000005,0x0,0x70000005,0x0,0x70000005,0x0,0x70000005,0x0,0x70000005,0x0,0x70000005,0x0,0x70000005,0x0,0x70000005,0x0,0x70000005,0x0,0x70000005,0x0,0x70000005,0x0,0x70000005,0x0,0x70000005,0x0,0x70000005,0x0,0x70000005,0x0,0x70000005,0x0,0x70000005,0x0,0x70000005,0x0,0x70000005,0x0,0x70000005,0x0,0x70000005,0x0,0x70000005,0x0,0x70000005,0x0,0x70000005,0x0,0x70000005,0x0,0x70000005,0x0,0x70000005,0x0,0x70000005,0x0,0x70000005,0x0,0x70000005,0x0,0x70000005,0x0,0x70000005,0x0,0x70000005,0x0,0x70000005,0x0,0x70000005,0x0,0x70000005,0x0,0x70000005,0x0,0x70000005,0x0,0x70000005,0x0,0x70000005,0x0,0x70000005,0x0,0x70000005,0x0,0x70000005,0x0,0x70000005,0x0,0x70000005,0x0,0x70000005,0x0,0x70000005,0x0,0x70000005,0x0,0x70000005,0x0,0x70000005,0x0,0x70000005,0x0,0x70000005,0x0,0x70000005,0x0,0x70000005,0x0,0x70000005,0x0,0x70000005,0x0,0x70000005,0x0,0x70000005,0x0,0x70000005,0x0,0x70000005,0x0,0x70000005,0x0,0x70000005,0x0,0x70000005,0x0,0x70000005,0x0,0x70000005,0x0,0x70000005,0x0,0x70000005,0x0,0x70000005,0x0,0x70000005,0x0,0x70000005,0x0,0x70000005,0x0,0x70000005,0x0,0x70000005,0x0,0x70000005,0x0,0x70000005,0x0,0x70000005,0x0,0x70000005,0x0,0x70000005,0x0,0x70000005,0x0,0x70000005,0x0,0x70000005,0x0,0x70000005,0x0,0x70000005,0x0,0x70000005,0x0,0x70000005,0x0,0x70000005,0x0,0x70000005,0x0,0x70000005,0x0,0x70000005,0x0,0x70000005,0x0,0x70000005,0x0,0x70000005,0x0,0x70000005,0x0,0x70000005,0x0,0x70000005,0x0,0x70000005,0x0,0x70000005,0x0,0x70000005,0x0,0x70000005,0x0,0x70000005,0x0,0x70000005,0x0,0x70000005,0x0,0x70000005,0x0,0x70000005,0x0,0x70000005,0x0,0x70000005,0x0,0x70000005,0x0,0x70000005,0x0,0x70000005,0x0,0x70000005,0x0,0x70000005,0x0,0x70000005,0x0,0x70000005,0x0,0x70000005,0x0,0x70000005,0x0,0x70000005,0x0,0x70000005,0x0,0x70000005,0x0,0x70000005,0x0,0x70000005,0x0,0x70000005,0x0,0x70000005,0x0,0x70000005,0x0,0x70000005,0x0,0x70000005,0x0,0x70000005,0x0,0x70000005,0x0,0x70000005,0x0,0x70000005,0x0,0x70000005,0x0,0x70000005,0x0,0x70000005,0x0,0x70000005,0x0,0x70000005,0x0,0x70000005,0x0,0x70000005,0x0,0x70000005,0x0,0x70000005,0x0,0x70000005,0x0,0x70000005,0x0,0x70000005,0x0,0x70000005,0x0,0x70000005,0x0,0x70000005,0x0,0x70000005,0x0,0x70000005,0x0,0x70000005,0x0,0x70000005,0x0,0x70000005,0x0,0x70000005,0x0,0x70000005,0x0,0x70000005,0x0,0x70000005,0x0,0x70000005,0x0,0x70000005,0x0,0x70000005,0x0,0x70000005,0x0,0x70000005,0x0,0x70000005,0x0,0x70000005,0x0,0x70000005,0x0,0x70000005,0x0,0x70000005,0x0,0x70000005,0x0,0x70000005,0x0,0x70000005,0x0,0x70000005,0x0,0x70000005,0x0,0x70000005,0x0,0x70000005,0x0,0x70000005,0x0,0x70000005,0x0,0x70000005,0x0,0x70000005,0x0,0x70000005,0x0,0x70000005,0x0,0x70000005,0x0,0x70000005,0x0,0x70000005,0x0,0x70000005,0x0,0x70000005,0x0,0x70000005,0x0,0x70000005,0x0,0x70000005,0x0,0x70000005,0x0,0x70000005,0x0,0x70000005,0x0,0x70000005,0x0,0x70000005,0x0,0x70000005,0x0,0x70000005,0x0,0x70000005,0x0,0x70000005,0x0,0x70000005,0x0,0x70000005,0x0,0x70000005,0x0,0x70000005,0x0,0x70000005,0x0,0x70000005,0x0,0x70000005,0x0,0x70000005,0x0,0x70000005,0x0,0x70000005,0x0,0x70000005,0x0,0x70000005,0x0,0x70000005,0x0,0x70000005,0x0,0x70000005,0x0,0x70000005,0x0,0x70000005,0x0,0x70000005,0x0,0x70000005,0x0,0x70000005,0x0,0x70000005,0x0,0x70000005,0x0,0x70000005,0x0,0x70000005,0x0,0x70000005,0x0,0x70000005,0x0,0x70000005,0x0,0x70000005,0x0,0x70000005,0x0,0x70000005,0x0,0x70000005,0x0,0x70000005,0x0,0x70000005,0x0,0x70000005,0x0,0x70000005,0x0,0x70000005,0x0,0x70000005,0x0,0x70000005,0x0,0x70000005,0x0,0x70000005,0x0,0x70000005,0x0,0x70000005,0x0,0x70000005,0x0,0x70000005,0x0,0x70000005,0x0,0x70000005,0x0,0x70000005,0x0,0x70000005,0x0,0x70000005,0x0,0x70000005,0x0,0x70000005,0x0,0x70000005,0x0,0x70000005,0x0,0x70000005,0x0,0x70000005,0x0,0x70000005,0x0,0x70000005,0x0,0x70000005,0x0,0x70000005,0x0,0x70000005,0x0,0x70000005,0x0,0x70000005,0x0,0x70000005,0x0,0x70000005,0x0,0x70000005,0x0,0x70000005,0x0,0x70000005,0x0,0x70000005,0x0,0x70000005,0x0,0x70000005,0x0,0x70000005,0x0,0x70000005,0x0,0x70000005,0x0,0x70000005,0x0,0x70000005,0x0,0x70000005,0x0,0x70000005,0x0,0x70000005,0x0,0x70000005,0x0,0x70000005,0x0,0x70000005,0x0,0x70000005,0x0,0x70000005,0x0,0x70000005,0x0,0x70000005,0x0,0x70000005,0x0,0x70000005,0x0,0x70000005,0x0,0x70000005,0x0,0x70000005,0x0,0x70000005,0x0,0x70000005,0x0,0x70000005,0x0,0x70000005,0x0,0x70000005,0x0,0x70000005,0x0,0x70000005,0x0,0x70000005,0x0,0x70000005,0x0,0x70000005,0x0,0x70000005,0x0,0x70000005,0x0,0x70000005,0x0,0x70000005,0x0,0x70000005,0x0,0x70000005,0x0,0x70000005,0x0,0x70000005,0x0,0x70000005,0x0,0x70000005,0x0,0x70000005,0x0,0x70000005,0x0,0x70000005,0x0,0x70000005,0x0,0x70000005,0x0,0x70000005,0x0,0x70000005,0x0,0x70000005,0x0,0x70000005,0x0,0x70000005,0x0,0x70000005,0x0,0x70000005,0x0,0x70000005,0x0,0x70000005,0x0,0x70000005,0x0,0x70000005,0x0,0x70000005,0x0,0x70000005,0x0,0x70000005,0x0,0x70000005,0x0,0x70000005,0x0,0x70000005,0x0,0x70000005,0x0,0x70000005,0x0,0x70000005,0x0,0x70000005,0x0,0x70000005,0x0,0x70000005,0x0,0x70000005,0x0,0x70000005,0x0,0x70000005,0x0,0x70000005,0x0,0x70000005,0x0,0x70000005,0x0,0x70000005,0x0,0x70000005,0x0,0x70000005,0x0,0x70000005,0x0,0x70000005,0x0,0x70000005,0x0,0x70000005,0x0,0x70000005,0x0,0x70000005,0x0,0x70000005,0x0,0x70000005,0x0,0x70000005,0x0,0x70000005,0x0,0x70000005,0x0,0x70000005,0x0,0x70000005,0x0,0x70000005,0x0,0x70000005,0x0,0x70000005,0x0,0x70000005,0x0,0x70000005,0x0,0x70000005,0x0,0x70000005,0x0,0x70000005,0x0,0x70000005,0x0,0x70000005,0x0,0x70000005,0x0,0x70000005,0x0,0x70000005,0x0,0x70000005,0x0,0x70000005,0x0,0x70000005,0x0,0x70000005,0x0,0x70000005,0x0,0x70000005,0x0,0x70000005,0x0,0x70000005,0x0,0x70000005,0x0,0x70000005,0x0,0x70000005,0x0,0x70000005,0x0,0x70000005,0x0,0x70000005,0x0,0x70000005,0x0,0x70000005,0x0,0x70000005,0x0,0x70000005,0x0,0x70000005,0x0,0x70000005,0x0,0x70000005,0x0,0x70000005,0x0,0x70000005,0x0,0x70000005,0x0,0x70000005,0x0,0x70000005,0x0,0x70000005,0x0,0x70000005,0x0,0x70000005,0x0,0x70000005,0x0,0x70000005,0x0,0x70000005,0x0,0x70000005,0x0,0x70000005,0x0,0x70000005,0x0,0x70000005,0x0,0x70000005,0x0,0x70000005,0x0,0x70000005,0x0,0x70000005,0x0,0x70000005,0x0,0x70000005,0x0,0x70000005,0x0,0x70000005,0x0,0x70000005,0x0,0x70000005,0x0,0x70000005,0x0,0x70000005,0x0,0x70000005,0x0,0x70000005,0x0,0x70000005,0x0,0x70000005,0x0,0x70000005,0x0,0x70000005,0x0,0x70000005,0x0,0x70000005,0x0,0x70000005,0x0,0x70000005,0x0,0x70000005,0x0,0x70000005,0x0,0x70000005,0x0,0x70000005,0x0,0x70000005,0x0,0x70000005,0x0,0x70000005,0x0,0x70000005,0x0,0x70000005,0x0,0x70000005,0x0,0x70000005,0x0,0x70000005,0x0,0x70000005,0x0,0x70000005,0x0,0x70000005,0x0,0x70000005,0x0,0x70000005,0x0,0x70000005,0x0,0x70000005,0x0,0x70000005,0x0,0x70000005,0x0,0x70000005,0x0,0x70000005,0x0,0x70000005,0x0,0x70000005,0x0,0x70000005,0x0,0x70000005,0x0,0x70000005,0x0,0x70000005,0x0,0x70000005,0x0,0x70000005,0x0,0x70000005,0x0,0x70000005,0x0,0x70000005,0x0,0x70000005,0x0,0x70000005,0x0,0x70000005,0x0,0x70000005,0x0,0x70000005,0x0,0x70000005,0x0,0x70000005,0x0,0x70000005,0x0,0x70000005,0x0,0x70000005,0x0,0x70000005,0x0,0x70000005,0x0,0x70000005,0x0,0x70000005,0x0,0x70000005,0x0,0x70000005,0x0,0x70000005,0x0,0x70000005,0x0,0x70000005,0x0,0x70000005,0x0,0x70000005,0x0,0x70000005,0x0,0x70000005,0x0,0x70000005,0x0,0x70000005,0x0,0x70000005,0x0,0x70000005,0x0,0x70000005,0x0,0x70000005,0x0,0x70000005,0x0,0x70000005,0x0,0x70000005,0x0,0x70000005,0x0,0x70000005,0x0,0x70000005,0x0,0x70000005,0x0,0x70000005,0x0,0x70000005,0x0,0x70000005,0x0,0x70000005,0x0,0x70000005,0x0,0x70000005,0x0,0x70000005,0x0,0x70000005,0x0,0x70000005,0x0,0x70000005,0x0,0x70000005,0x0,0x70000005,0x0,0x70000005,0x0,0x70000005,0x0,0x70000005,0x0,0x70000005,0x0,0x70000005,0x0,0x70000005,0x0,0x70000005,0x0,0x70000005,0x0,0x70000005,0x0,0x70000005,0x0,0x70000005,0x0,0x70000005,0x0,0x70000005,0x0,0x70000005,0x0,0x70000005,0x0,0x70000005,0x0,0x70000005,0x0,0x70000005,0x0,0x70000005,0x0,0x70000005,0x0,0x70000005,0x0,0x70000005,0x0,0x70000005,0x0,0x70000005,0x0,0x70000005,0x0,0x70000005,0x0,0x70000005,0x0,0x70000005,0x0,0x70000005,0x0,0x70000005,0x0,0x70000005,0x0,0x70000005,0x0,0x70000005,0x0,0x70000005,0x0,0x70000005,0x0,0x70000005,0x0,0x70000005,0x0,0x70000005,0x0,0x70000005,0x0,0x70000005,0x0,0x70000005,0x0,0x70000005,0x0,0x70000005,0x0,0x70000005,0x0,0x70000005,0x0,0x70000005,0x0,0x70000005,0x0,0x70000005,0x0,0x70000005,0x0,0x70000005,0x0,0x70000005,0x0,0x70000005,0x0,0x70000005,0x0,0x70000005,0x0,0x70000005,0x0,0x70000005,0x0,0x70000005,0x0,0x70000005,0x0,0x70000005,0x0,0x70000005,0x0,0x70000005,0x0,0x70000005,0x0,0x70000005,0x0,0x70000005,0x0,0x70000005,0x0,0x70000005,0x0,0x70000005,0x0,0x70000005,0x0,0x70000005,0x0,0x70000005,0x0,0x70000005,0x0,0x70000005,0x0,0x70000005,0x0,0x70000005,0x0,0x70000005,0x0,0x70000005,0x0,0x70000005,0x0,0x70000005,0x0,0x70000005,0x0,0x70000005,0x0,0x70000005,0x0,0x70000005,0x0,0x70000005,0x0,0x70000005,0x0,0x70000005,0x0,0x70000005,0x0,0x70000005,0x0,0x70000005,0x0,0x70000005,0x0,0x70000005,0x0,0x70000005,0x0,0x70000005,0x0,0x70000005,0x0,0x70000005,0x0,0x70000005,0x0,0x70000005,0x0,0x70000005,0x0,0x70000005,0x0,0x70000005,0x0,0x70000005,0x0,0x70000005,0x0,0x70000005,0x0,0x70000005,0x0,0x70000005,0x0,0x70000005,0x0,0x70000005,0x0,0x70000005,0x0,0x70000005,0x0,0x70000005,0x0,0x70000005,0x0,0x70000005,0x0,0x70000005,0x0,0x70000005,0x0,0x70000005,0x0,0x70000005,0x0,0x70000005,0x0,0x70000005,0x0,0x70000005,0x0,0x70000005,0x0,0x70000005,0x0,0x70000005,0x0,0x70000005,0x0,0x70000005,0x0,0x70000005,0x0,0x70000005,0x0,0x70000005,0x0,0x70000005,0x0,0x70000005,0x0,0x70000005,0x0,0x70000005,0x0,0x70000005,0x0,0x70000005,0x0,0x70000005,0x0,0x70000005,0x0,0x70000005,0x0,0x70000005,0x0,0x70000005,0x0,0x70000005,0x0,0x70000005,0x0,0x70000005,0x0,0x70000005,0x0,0x70000005,0x0,0x70000005,0x0,0x70000005,0x0,0x70000005,0x0,0x70000005,0x0,0x70000005,0x0,0x70000005,0x0,0x70000005,0x0,0x70000005,0x0,0x70000005,0x0,0x70000005,0x0,0x70000005,0x0,0x70000005,0x0,0x70000005,0x0,0x70000005,0x0,0x70000005,0x0,0x70000005,0x0,0x70000005,0x0,0x70000005,0x0,0x70000005,0x0,0x70000005,0x0,0x70000005,0x0,0x70000005,0x0,0x70000005,0x0,0x70000005,0x0,0x70000005,0x0,0x70000005,0x0,0x70000005,0x0,0x70000005,0x0,0x70000005,0x0,0x70000005,0x0,0x70000005,0x0,0x70000005,0x0,0x70000005,0x0,0x70000005,0x0,0x70000005,0x0,0x70000005,0x0,0x70000005,0x0,0x70000005,0x0,0x70000005,0x0,0x70000005,0x0,0x70000005,0x0,0x70000005,0x0,0x70000005,0x0,0x70000005,0x0,0x70000005,0x0,0x70000005,0x0,0x70000005,0x0,0x70000005,0x0,0x70000005,0x0,0x70000005,0x0,0x70000005,0x0,0x70000005,0x0,0x70000005,0x0,0x70000005,0x0,0x70000005,0x0,0x70000005,0x0,0x70000005,0x0,0x70000005,0x0,0x70000005,0x0,0x70000005,0x0,0x70000005,0x0,0x70000005,0x0,0x70000005,0x0,0x70000005,0x0,0x70000005,0x0,0x70000005,0x0,0x70000005,0x0,0x70000005,0x0,0x70000005,0x0,0x70000005,0x0,0x70000005,0x0,0x70000005,0x0,0x70000005,0x0,0x70000005,0x0,0x70000005,0x0,0x70000005,0x0,0x70000005,0x0,0x70000005,0x0,0x70000005,0x0,0x70000005,0x0,0x70000005,0x0,0x70000005,0x0,0x70000005,0x0,0x70000005,0x0,0x70000005,0x0,0x70000005,0x0,0x70000005,0x0,0x70000005,0x0,0x70000005,0x0,0x70000005,0x0,0x70000005,0x0,0x70000005,0x0,0x70000005,0x0,0x70000005,0x0,0x70000005,0x0,0x70000005,0x0,0x70000005,0x0,0x70000005,0x0,0x70000005,0x0,0x70000005,0x0,0x70000005,0x0,0x70000005,0x0,0x70000005,0x0,0x70000005,0x0,0x70000005,0x0,0x70000005,0x0,0x70000005,0x0,0x70000005,0x0,0x70000005,0x0,0x70000005,0x0,0x70000005,0x0,0x70000005,0x0,0x70000005,0x0,0x70000005,0x0,0x70000005,0x0,0x70000005,0x0,0x70000005,0x0,0x70000005,0x0,0x70000005,0x0,0x70000005,0x0,0x70000005,0x0,0x70000005,0x0,0x70000005,0x0,0x70000005,0x0,0x70000005,0x0,0x70000005,0x0,0x70000005,0x0,0x70000005,0x0,0x70000005,0x0,0x70000005,0x0,0x70000005,0x0,0x70000005,0x0,0x70000005,0x0,0x70000005,0x0,0x70000005,0x0,0x70000005,0x0,0x70000005,0x0,0x70000005,0x0,0x70000005,0x0,0x70000005,0x0,0x70000005,0x0,0x70000005,0x0,0x70000005,0x0,0x70000005,0x0,0x70000005,0x0,0x70000005,0x0,0x70000005,0x0,0x70000005,0x0,0x70000005,0x0,0x70000005,0x0,0x70000005,0x0,0x70000005,0x0,0x70000005,0x0,0x70000005,0x0,0x70000005,0x0,0x70000005,0x0,0x70000005,0x0,0x70000005,0x0,0x70000005,0x0,0x70000005,0x0,0x70000005,0x0,0x70000005,0x0,0x70000005,0x0,0x70000005,0x0,0x70000005,0x0,0x70000005,0x0,0x70000005,0x0,0x70000005,0x0,0x70000005,0x0,0x70000005,0x0,0x70000005,0x0,0x70000005,0x0,0x70000005,0x0,0x70000005,0x0,0x70000005,0x0,0x70000005,0x0,0x70000005,0x0,0x70000005,0x0,0x70000005,0x0,0x70000005,0x0,0x70000005,0x0,0x70000005,0x0,0x70000005,0x0,0x70000005,0x0,0x70000005,0x0,0x70000005,0x0,0x70000005,0x0,0x70000005,0x0,0x70000005,0x0,0x70000005,0x0,0x70000005,0x0,0x70000005,0x0,0x70000005,0x0,0x70000005,0x0,0x70000005,0x0,0x70000005,0x0,0x70000005,0x0,0x70000005,0x0,0x70000005,0x0,0x70000005,0x0,0x70000005,0x0,0x70000005,0x0,0x70000005,0x0,0x70000005,0x0,0x70000005,0x0,0x70000005,0x0,0x70000005,0x0,0x70000005,0x0,0x70000005,0x0,0x70000005,0x0,0x70000005,0x0,0x70000005,0x0,0x70000005,0x0,0x70000005,0x0,0x70000005,0x0,0x70000005,0x0,0x70000005,0x0,0x70000005,0x0,0x70000005,0x0,0x70000005,0x0,0x70000005,0x0,0x70000005,0x0,0x70000005,0x0,0x70000005,0x0,0x70000005,0x0,0x70000005,0x0,0x70000005,0x0,0x70000005,0x0,0x70000005,0x0,0x70000005,0x0,0x70000005,0x0,0x70000005,0x0,0x70000005,0x0,0x70000005,0x0,0x70000005,0x0,0x70000005,0x0,0x70000005,0x0,0x70000005,0x0,0x70000005,0x0,0x70000005,0x0,0x70000005,0x0,0x70000005,0x0,0x70000005,0x0,0x70000005,0x0,0x70000005,0x0,0x70000005,0x0,0x70000005,0x0,0x70000005,0x0,0x70000005,0x0,0x70000005,0x0,0x70000005,0x0,0x70000005,0x0,0x70000005,0x0,0x70000005,0x0,0x70000005,0x0,0x70000005,0x0,0x70000005,0x0,0x70000005,0x0,0x70000005,0x0,0x70000005,0x0,0x70000005,0x0,0x70000005,0x0,0x70000005,0x0,0x70000005,0x0,0x70000005,0x0,0x70000005,0x0,0x70000005,0x0,0x70000005,0x0,0x70000005,0x0,0x70000005,0x0,0x70000005,0x0,0x70000005,0x0,0x70000005,0x0,0x70000005,0x0,0x70000005,0x0,0x70000005,0x0,0x70000005,0x0,0x70000005,0x0,0x70000005,0x0,0x70000005,0x0,0x70000005,0x0,0x70000005,0x0,0x70000005,0x0,0x70000005,0x0,0x70000005,0x0,0x70000005,0x0,0x70000005,0x0,0x70000005,0x0,0x70000005,0x0,0x70000005,0x0,0x70000005,0x0,0x70000005,0x0,0x70000005,0x0,0x70000005,0x0,0x70000005,0x0,0x70000005,0x0,0x70000005,0x0,0x70000005,0x0,0x70000005,0x0,0x70000005,0x0,0x70000005,0x0,0x70000005,0x0,0x70000005,0x0,0x70000005,0x0,0x70000005,0x0,0x70000005,0x0,0x70000005,0x0,0x70000005,0x0,0x70000005,0x0,0x70000005,0x0,0x70000005,0x0,0x70000005,0x0,0x70000005,0x0,0x70000005,0x0,0x70000005,0x0,0x70000005,0x0,0x70000005,0x0,0x70000005,0x0,0x70000005,0x0,0x70000005,0x0,0x70000005,0x0,0x70000005,0x0,0x70000005,0x0,0x70000005,0x0,0x70000005,0x0,0x70000005,0x0,0x70000005,0x0,0x70000005,0x0,0x70000005,0x0,0x70000005,0x0,0x70000005,0x0,0x70000005,0x0,0x70000005,0x0,0x70000005,0x0,0x70000005,0x0,0x70000005,0x0,0x70000005,0x0,0x70000005,0x0,0x70000005,0x0,0x70000005,0x0,0x70000005,0x0,0x70000005,0x0,0x70000005,0x0,0x70000005,0x0,0x70000005,0x0,0x70000005,0x0,0x70000005,0x0,0x70000005,0x0,0x70000005,0x0,0x70000005,0x0,0x70000005,0x0,0x70000005,0x0,0x70000005,0x0,0x70000005,0x0,0x70000005,0x0,0x70000005,0x0,0x70000005,0x0,0x70000005,0x0,0x70000005,0x0,0x70000005,0x0,0x70000005,0x0,0x70000005,0x0,0x70000005,0x0,0x70000005,0x0,0x70000005,0x0,0x70000005,0x0,0x70000005,0x0,0x70000005,0x0,0x70000005,0x0},
  {0x60008020,0x8100008,0x60008028,0x8100008,0x60008030,0x8100008,0x60008038,0x8100008,0x60008040,0x8100008,0x60008048,0x8100008,0x60008050,0x8100008,0x60008058,0x8100008,0x60008060,0x8100008,0x60008068,0x8100008,0x60008070,0x8100008,0x60008078,0x8100008,0x40420000,0x26802701,0x40420001,0x26802701,0x40420002,0x26802701,0x40420003,0x26802701,0x40420004,0x26802701,0x40420005,0x26802701,0x40420006,0x26802701,0x40420007,0x26802701,0x40420008,0x26802701,0x40420009,0x26802701,0x4042000a,0x26802701,0x4042000b,0x26802701,0x4042000c,0x26802701,0x4042000d,0x26802701,0x20620000,0x31203501,0x20620001,0x31203501,0x20620002,0x31203501,0x20620003,0x31203501,0x20620200,0x32203501,0x20620201,0x32203501,0x20620202,0x32203501,0x20620203,0x32203501,0x20620400,0x33203501,0x20620401,0x33203501,0x20620402,0x33203501,0x20620403,0x33203501,0x20620600,0x34203501,0x20620601,0x34203501,0x20620602,0x34203501,0x20620603,0x34203501,0x60108000,0x10100008,0x60108008,0x10100008,0x60108010,0x10100008,0x60108018,0x10100008,0x60108020,0x10100008,0x60108028,0x10100008,0x60108030,0x10100008,0x60108038,0x10100008,0x60108040,0x10100008,0x60108048,0x10100008,0x60108050,0x10100008,0x60108058,0x10100008,0x60108060,0x10100008,0x60108068,0x10100008,0x60108070,0x10100008,0x60108078,0x10100008,0x30508000,0x2d102e08,0x30508008,0x2d102e08,0x30508010,0x2d102e08,0x30508018,0x2d102e08,0x30508020,0x2d102e08,0x30508028,0x2d102e08,0x30508030,0x2d102e08,0x30508038,0x2d102e08,0x30508040,0x2d102e08,0x30508048,0x2d102e08,0x30508050,0x2d102e08,0x30508058,0x2d102e08,0x30508060,0x2d102e08,0x30508068,0x2d102e08,0x30508070,0x2d102e08,0x30508078,0x2d102e08,0x60208000,0x18100008,0x60208008,0x18100008,0x60208010,0x18100008,0x60208018,0x18100008,0x60208020,0x18100008,0x60208028,0x18100008,0x60208030,0x18100008,0x60208038,0x18100008,0x60208040,0x18100008,0x60208048,0x18100008,0x60208050,0x18100008,0x60208058,0x18100008,0x60208060,0x18100008,0x60208068,0x18100008,0x60208070,0x18100008,0x60208078,0x18100008,0x10700000,0x13908,0x10700008,0x13908,0x10700010,0x13908,0x10700018,0x13908,0x10700020,0x13a08,0x10700028,0x13a08,0x10700030,0x13a08,0x10700038,0x13a08,0x10700040,0x13b08,0x10700048,0x13b08,0x10700050,0x13b08,0x10700058,0x13b08,0x10700060,0x13c08,0x10700068,0x13c08,0x10700070,0x13c08,0x10700078,0x13c08,0x5031c000,0x1f0e2001,0x5031c001,0x1f0e2001,0x5031c002,0x1f0e2001,0x5031c003,0x1f0e2001,0x5031c200,0x1f0e2001,0x5031c201,0x1f0e2001,0x5031c202,0x1f0e2001,0x5031c203,0x1f0e2001,0x5031c400,0x1f0e2001,0x5031c401,0x1f0e2001,0x5031c402,0x1f0e2001,0x5031c403,0x1f0e2001,0x5031c600,0x1f0e2001,0x5031c601,0x1f0e2001,0x5031c602,0x1f0e2001,0x5031c603,0x1f0e2001,0x40520000,0x2e802f01,0x40520001,0x2e802f01,0x40520002,0x2e802f01,0x40520003,0x2e802f01,0x40520004,0x2e802f01,0x40520005,0x2e802f01,0x40520006,0x2e802f01,0x40520007,0x2e802f01,0x40520008,0x2e802f01,0x40520009,0x2e802f01,0x4052000a,0x2e802f01,0x4052000b,0x2e802f01,0x4052000c,0x2e802f01,0x4052000d,0x2e802f01,0x20720000,0x39203d01,0x20720001,0x39203d01,0x20720002,0x39203d01,0x20720003,0x39203d01,0x20720200,0x3a203d01,0x20720201,0x3a203d01,0x20720202,0x3a203d01,0x20720203,0x3a203d01,0x20720400,0x3b203d01,0x20720401,0x3b203d01,0x20720402,0x3b203d01,0x20720403,0x3b203d01,0x20720600,0x3c203d01,0x20720601,0x3c203d01,0x20720602,0x3c203d01,0x20720603,0x3c203d01,0x5041c000,0x270e2801,0x5041c001,0x270e2801,0x5041c002,0x270e2801,0x5041c003,0x270e2801,0x5041c200,0x270e2801,0x5041c201,0x270e2801,0x5041c202,0x270e2801,0x5041c203,0x270e2801,0x5041c400,0x270e2801,0x5041c401,0x270e2801,0x5041c402,0x270e2801,0x5041c403,0x270e2801,0x5041c600,0x270e2801,0x5041c601,0x270e2801,0x5041c602,0x270e2801,0x5041c603,0x270e2801,0x30608000,0x35103608,0x30608008,0x35103608,0x30608010,0x35103608,0x30608018,0x35103608,0x30608020,0x35103608,0x30608028,0x35103608,0x30608030,0x35103608,0x30608038,0x35103608,0x30608040,0x35103608,0x30608048,0x35103608,0x30608050,0x35103608,0x30608058,0x35103608,0x30608060,0x35103608,0x30608068,0x35103608,0x30608070,0x35103608,0x30608078,0x35103608,0x10800000,0x14108,0x10800008,0x14108,0x10800010,0x14108,0x10800018,0x14108,0x10800020,0x14208,0x10800028,0x14208,0x10800030,0x14208,0x10800038,0x14208,0x10800040,0x14308,0x10800048,0x14308,0x10800050,0x14308,0x10800058,0x14308,0x10800060,0x14408,0x10800068,0x14408,0x10800070,0x14408,0x10800078,0x14408,0x40620000,0x36803701,0x40620001,0x36803701,0x40620002,0x36803701,0x40620003,0x36803701,0x40620004,0x36803701,0x40620005,0x36803701,0x40620006,0x36803701,0x40620007,0x36803701,0x40620008,0x36803701,0x40620009,0x36803701,0x4062000a,0x36803701,0x4062000b,0x36803701,0x4062000c,0x36803701,0x4062000d,0x36803701,0x60308000,0x20100008,0x60308008,0x20100008,0x60308010,0x20100008,0x60308018,0x20100008,0x60308020,0x20100008,0x60308028,0x20100008,0x60308030,0x20100008,0x60308038,0x20100008,0x60308040,0x20100008,0x60308048,0x20100008,0x60308050,0x20100008,0x60308058,0x20100008,0x60308060,0x20100008,0x60308068,0x20100008,0x60308070,0x20100008,0x60308078,0x20100008,0x5051c000,0x2f0e3001,0x5051c001,0x2f0e3001,0x5051c002,0x2f0e3001,0x5051c003,0x2f0e3001,0x5051c200,0x2f0e3001,0x5051c201,0x2f0e3001,0x5051c202,0x2f0e3001,0x5051c203,0x2f0e3001,0x5051c400,0x2f0e3001,0x5051c401,0x2f0e3001,0x5051c402,0x2f0e3001,0x5051c403,0x2f0e3001,0x5051c600,0x2f0e3001,0x5051c601,0x2f0e3001,0x5051c602,0x2f0e3001,0x5051c603,0x2f0e3001,0x20820000,0x41204501,0x20820001,0x41204501,0x20820002,0x41204501,0x20820003,0x41204501,0x20820200,0x42204501,0x20820201,0x42204501,0x20820202,0x42204501,0x20820203,0x42204501,0x20820400,0x43204501,0x20820401,0x43204501,0x20820402,0x43204501,0x20820403,0x43204501,0x20820600,0x44204501,0x20820601,0x44204501,0x20820602,0x44204501,0x20820603,0x44204501,0x60408000,0x28100008,0x60408008,0x28100008,0x60408010,0x28100008,0x60408018,0x28100008,0x60408020,0x28100008,0x60408028,0x28100008,0x60408030,0x28100008,0x60408038,0x28100008,0x60408040,0x28100008,0x60408048,0x28100008,0x60408050,0x28100008,0x60408058,0x28100008,0x60408060,0x28100008,0x60408068,0x28100008,0x60408070,0x28100008,0x60408078,0x28100008,0x30708000,0x3d103e08,0x30708008,0x3d103e08,0x30708010,0x3d103e08,0x30708018,0x3d103e08,0x30708020,0x3d103e08,0x30708028,0x3d103e08,0x30708030,0x3d103e08,0x30708038,0x3d103e08,0x30708040,0x3d103e08,0x30708048,0x3d103e08,0x30708050,0x3d103e08,0x30708058,0x3d103e08,0x30708060,0x3d103e08,0x30708068,0x3d103e08,0x30708070,0x3d103e08,0x30708078,0x3d103e08,0x10900000,0x14908,0x10900008,0x14908,0x10900010,0x14908,0x10900018,0x14908,0x10900020,0x14a08,0x10900028,0x14a08,0x10900030,0x14a08,0x10900038,0x14a08,0x10900040,0x14b08,0x10900048,0x14b08,0x10900050,0x14b08,0x10900058,0x14b08,0x10900060,0x14c08,0x10900068,0x14c08,0x10900070,0x14c08,0x10900078,0x14c08,0x40720000,0x3e803f01,0x40720001,0x3e803f01,0x40720002,0x3e803f01,0x40720003,0x3e803f01,0x40720004,0x3e803f01,0x40720005,0x3e803f01,0x40720006,0x3e803f01,0x40720007,0x3e803f01,0x40720008,0x3e803f01,0x40720009,0x3e803f01,0x4072000a,0x3e803f01,0x4072000b,0x3e803f01,0x4072000c,0x3e803f01,0x4072000d,0x3e803f01,0x5061c000,0x370e3801,0x5061c001,0x370e3801,0x5061c002,0x370e3801,0x5061c003,0x370e3801,0x5061c200,0x370e3801,0x5061c201,0x370e3801,0x5061c202,0x370e3801,0x5061c203,0x370e3801,0x5061c400,0x370e3801,0x5061c401,0x370e3801,0x5061c402,0x370e3801,0x5061c403,0x370e3801,0x5061c600,0x370e3801,0x5061c601,0x370e3801,0x5061c602,0x370e3801,0x5061c603,0x370e3801,0x20920000,0x49204d01,0x20920001,0x49204d01,0x20920002,0x49204d01,0x20920003,0x49204d01,0x20920200,0x4a204d01,0x20920201,0x4a204d01,0x20920202,0x4a204d01,0x20920203,0x4a204d01,0x20920400,0x4b204d01,0x20920401,0x4b204d01,0x20920402,0x4b204d01,0x20920403,0x4b204d01,0x20920600,0x4c204d01,0x20920601,0x4c204d01,0x20920602,0x4c204d01,0x20920603,0x4c204d01,0x60508000,0x30100008,0x60508008,0x30100008,0x60508010,0x30100008,0x60508018,0x30100008,0x60508020,0x30100008,0x60508028,0x30100008,0x60508030,0x30100008,0x60508038,0x30100008,0x60508040,0x30100008,0x60508048,0x30100008,0x60508050,0x30100008,0x60508058,0x30100008,0x60508060,0x30100008,0x60508068,0x30100008,0x60508070,0x30100008,0x60508078,0x30100008,0x30808000,0x45104608,0x30808008,0x45104608,0x30808010,0x45104608,0x30808018,0x45104608,0x30808020,0x45104608,0x30808028,0x45104608,0x30808030,0x45104608,0x30808038,0x45104608,0x30808040,0x45104608,0x30808048,0x45104608,0x30808050,0x45104608,0x30808058,0x45104608,0x30808060,0x45104608,0x30808068,0x45104608,0x30808070,0x45104608,0x30808078,0x45104608,0x10a00000,0x15108,0x10a00008,0x15108,0x10a00010,0x15108,0x10a00018,0x15108,0x10a00020,0x15208,0x10a00028,0x15208,0x10a00030,0x15208,0x10a00038,0x15208,0x10a00040,0x15308,0x10a00048,0x15308,0x10a00050,0x15308,0x10a00058,0x15308,0x10a00060,0x15408,0x10a00068,0x15408,0x10a00070,0x15408,0x10a00078,0x15408,0x40820000,0x46804701,0x40820001,0x46804701,0x40820002,0x46804701,0x40820003,0x46804701,0x40820004,0x46804701,0x40820005,0x46804701,0x40820006,0x46804701,0x40820007,0x46804701,0x40820008,0x46804701,0x40820009,0x46804701,0x4082000a,0x46804701,0x4082000b,0x46804701,0x4082000c,0x46804701,0x4082000d,0x46804701,0x5071c000,0x3f0e4001,0x5071c001,0x3f0e4001,0x5071c002,0x3f0e4001,0x5071c003,0x3f0e4001,0x5071c200,0x3f0e4001,0x5071c201,0x3f0e4001,0x5071c202,0x3f0e4001,0x5071c203,0x3f0e4001,0x5071c400,0x3f0e4001,0x5071c401,0x3f0e4001,0x5071c402,0x3f0e4001,0x5071c403,0x3f0e4001,0x5071c600,0x3f0e4001,0x5071c601,0x3f0e4001,0x5071c602,0x3f0e4001,0x5071c603,0x3f0e4001,0x20a20000,0x51205501,0x20a20001,0x51205501,0x20a20002,0x51205501,0x20a20003,0x51205501,0x20a20200,0x52205501,0x20a20201,0x52205501,0x20a20202,0x52205501,0x20a20203,0x52205501,0x20a20400,0x53205501,0x20a20401,0x53205501,0x20a20402,0x53205501,0x20a20403,0x53205501,0x20a20600,0x54205501,0x20a20601,0x54205501,0x20a20602,0x54205501,0x20a20603,0x54205501,0x60608000,0x38100008,0x60608008,0x38100008,0x60608010,0x38100008,0x60608018,0x38100008,0x60608020,0x38100008,0x60608028,0x38100008,0x60608030,0x38100008,0x60608038,0x38100008,0x60608040,0x38100008,0x60608048,0x38100008,0x60608050,0x38100008,0x60608058,0x38100008,0x60608060,0x38100008,0x60608068,0x38100008,0x60608070,0x38100008,0x60608078,0x38100008,0x30908000,0x4d104e08,0x30908008,0x4d104e08,0x30908010,0x4d104e08,0x30908018,0x4d104e08,0x30908020,0x4d104e08,0x30908028,0x4d104e08,0x30908030,0x4d104e08,0x30908038,0x4d104e08,0x30908040,0x4d104e08,0x30908048,0x4d104e08,0x30908050,0x4d104e08,0x30908058,0x4d104e08,0x30908060,0x4d104e08,0x30908068,0x4d104e08,0x30908070,0x4d104e08,0x30908078,0x4d104e08,0x10b00000,0x15908,0x10b00008,0x15908,0x10b00010,0x15908,0x10b00018,0x15908,0x10b00020,0x15a08,0x10b00028,0x15a08,0x10b00030,0x15a08,0x10b00038,0x15a08,0x10b00040,0x15b08,0x10b00048,0x15b08,0x10b00050,0x15b08,0x10b00058,0x15b08,0x10b00060,0x15c08,0x10b00068,0x15c08,0x10b00070,0x15c08,0x10b00078,0x15c08,0x40920000,0x4e804f01,0x40920001,0x4e804f01,0x40920002,0x4e804f01,0x40920003,0x4e804f01,0x40920004,0x4e804f01,0x40920005,0x4e804f01,0x40920006,0x4e804f01,0x40920007,0x4e804f01,0x40920008,0x4e804f01,0x40920009,0x4e804f01,0x4092000a,0x4e804f01,0x4092000b,0x4e804f01,0x4092000c,0x4e804f01,0x4092000d,0x4e804f01,0x5081c000,0x470e4801,0x5081c001,0x470e4801,0x5081c002,0x470e4801,0x5081c003,0x470e4801,0x5081c200,0x470e4801,0x5081c201,0x470e4801,0x5081c202,0x470e4801,0x5081c203,0x470e4801,0x5081c400,0x470e4801,0x5081c401,0x470e4801,0x5081c402,0x470e4801,0x5081c403,0x470e4801,0x5081c600,0x470e4801,0x5081c601,0x470e4801,0x5081c602,0x470e4801,0x5081c603,0x470e4801,0x21b20000,0x59205d01,0x21b20001,0x59205e01,0x21b20002,0x59205f01,0x21b20003,0x59206001,0x21b20004,0x59206101,0x21b20005,0x59206201,0x21b20006,0x59206301,0x21b20007,0x59206401,0x21b20200,0x5a205d01,0x21b20201,0x5a205e01,0x21b20202,0x5a205f01,0x21b20203,0x5a206001,0x21b20204,0x5a206101,0x21b20205,0x5a206201,0x21b20206,0x5a206301,0x21b20207,0x5a206401,0x21b20400,0x5b205d01,0x21b20401,0x5b205e01,0x21b20402,0x5b205f01,0x21b20403,0x5b206001,0x21b20404,0x5b206101,0x21b20405,0x5b206201,0x21b20406,0x5b206301,0x21b20407,0x5b206401,0x21b20600,0x5c205d01,0x21b20601,0x5c205e01,0x21b20602,0x5c205f01,0x21b20603,0x5c206001,0x21b20604,0x5c206101,0x21b20605,0x5c206201,0x21b20606,0x5c206301,0x21b20607,0x5c206401,0x60708000,0x40100008,0x60708008,0x40100008,0x60708010,0x40100008,0x60708018,0x40100008,0x60708020,0x40100008,0x60708028,0x40100008,0x60708030,0x40100008,0x60708038,0x40100008,0x60708040,0x40100008,0x60708048,0x40100008,0x60708050,0x40100008,0x60708058,0x40100008,0x60708060,0x40100008,0x60708068,0x40100008,0x60708070,0x40100008,0x60708078,0x40100008,0x30a08000,0x55105608,0x30a08008,0x55105608,0x30a08010,0x55105608,0x30a08018,0x55105608,0x30a08020,0x55105608,0x30a08028,0x55105608,0x30a08030,0x55105608,0x30a08038,0x55105608,0x30a08040,0x55105608,0x30a08048,0x55105608,0x30a08050,0x55105608,0x30a08058,0x55105608,0x30a08060,0x55105608,0x30a08068,0x55105608,0x30a08070,0x55105608,0x30a08078,0x55105608,0x10c00000,0x16f04,0x10c00004,0x16f04,0x10c00008,0x16f04,0x10c0000c,0x16f04,0x10c00010,0x16f04,0x10c00014,0x16f04,0x10c00018,0x16f04,0x10c0001c,0x16f04,0x10c00020,0x17004,0x10c00024,0x17004,0x10c00028,0x17004,0x10c0002c,0x17004,0x10c00030,0x17004,0x10c00034,0x17004,0x10c00038,0x17004,0x10c0003c,0x17004,0x10c00040,0x17104,0x10c00044,0x17104,0x10c00048,0x17104,0x10c0004c,0x17104,0x10c00050,0x17104,0x10c00054,0x17104,0x10c00058,0x17104,0x10c0005c,0x17104,0x10c00060,0x17204,0x10c00064,0x17204,0x10c00068,0x17204,0x10c0006c,0x17204,0x10c00070,0x17204,0x10c00074,0x17204,0x10c00078,0x17204,0x10c0007c,0x17204,0x40a20000,0x56805701,0x40a20001,0x56805701,0x40a20002,0x56805701,0x40a20003,0x56805701,0x40a20004,0x56805701,0x40a20005,0x56805701,0x40a20006,0x56805701,0x40a20007,0x56805701,0x40a20008,0x56805701,0x40a20009,0x56805701,0x40a2000a,0x56805701,0x40a2000b,0x56805701,0x40a2000c,0x56805701,0x40a2000d,0x56805701,0x38b08000,0x5d046508,0x38b08008,0x5d046508,0x38b08010,0x5d046508,0x38b08018,0x5d046508,0x38b08020,0x5d046508,0x38b08028,0x5d046508,0x38b08030,0x5d046508,0x38b08038,0x5d046508,0x38b08040,0x5d046508,0x38b08048,0x5d046508,0x38b08050,0x5d046508,0x38b08058,0x5d046508,0x38b08060,0x5d046508,0x38b08068,0x5d046508,0x38b08070,0x5d046508,0x38b08078,0x5d046508,0x5091c000,0x4f0e5001,0x5091c001,0x4f0e5001,0x5091c002,0x4f0e5001,0x5091c003,0x4f0e5001,0x5091c200,0x4f0e5001,0x5091c201,0x4f0e5001,0x5091c202,0x4f0e5001,0x5091c203,0x4f0e5001,0x5091c400,0x4f0e5001,0x5091c401,0x4f0e5001,0x5091c402,0x4f0e5001,0x5091c403,0x4f0e5001,0x5091c600,0x4f0e5001,0x5091c601,0x4f0e5001,0x5091c602,0x4f0e5001,0x5091c603,0x4f0e5001,0x21c20000,0x6f207301,0x21c20001,0x6f207401,0x21c20002,0x6f207501,0x21c20003,0x6f207601,0x21c20004,0x6f207701,0x21c20005,0x6f207801,0x21c20006,0x6f207901,0x21c20007,0x6f207a01,0x21c20200,0x70207301,0x21c20201,0x70207401,0x21c20202,0x70207501,0x21c20203,0x70207601,0x21c20204,0x70207701,0x21c20205,0x70207801,0x21c20206,0x70207901,0x21c20207,0x70207a01,0x21c20400,0x71207301,0x21c20401,0x71207401,0x21c20402,0x71207501,0x21c20403,0x71207601,0x21c20404,0x71207701,0x21c20405,0x71207801,0x21c20406,0x71207901,0x21c20407,0x71207a01,0x21c20600,0x72207301,0x21c20601,0x72207401,0x21c20602,0x72207501,0x21c20603,0x72207601,0x21c20604,0x72207701,0x21c20605,0x72207801,0x21c20606,0x72207901,0x21c20607,0x72207a01,0x60808000,0x48100008,0x60808008,0x48100008,0x60808010,0x48100008,0x60808018,0x48100008,0x60808020,0x48100008,0x60808028,0x48100008,0x60808030,0x48100008,0x60808038,0x48100008,0x60808040,0x48100008,0x60808048,0x48100008,0x60808050,0x48100008,0x60808058,0x48100008,0x60808060,0x48100008,0x60808068,0x48100008,0x60808070,0x48100008,0x60808078,0x48100008,0x41b20000,0x65806601,0x41b20001,0x65806601,0x41b20002,0x65806601,0x41b20003,0x65806601,0x41b20004,0x65806601,0x41b20005,0x65806601,0x41b20006,0x65806601,0x41b20007,0x65806601,0x41b20008,0x65806601,0x41b20009,0x65806601,0x41b2000a,0x65806601,0x41b2000b,0x65806601,0x41b2000c,0x65806601,0x41b2000d,0x65806601,0x41b2000e,0x65806601,0x41b2000f,0x65806601,0x41b20010,0x65806601,0x41b20011,0x65806601,0x41b20012,0x65806601,0x41b20013,0x65806601,0x41b20014,0x65806601,0x41b20015,0x65806601,0x41b20016,0x65806601,0x41b20017,0x65806601,0x41b20018,0x65806601,0x41b20019,0x65806601,0x41b2001a,0x65806601,0x41b2001b,0x65806601,0x10d00000,0x18504,0x10d00004,0x18504,0x10d00008,0x18504,0x10d0000c,0x18504,0x10d00010,0x18604,0x10d00014,0x18604,0x10d00018,0x18604,0x10d0001c,0x18604,0x10d00020,0x18704,0x10d00024,0x18704,0x10d00028,0x18704,0x10d0002c,0x18704,0x10d00030,0x18804,0x10d00034,0x18804,0x10d00038,0x18804,0x10d0003c,0x18804,0x10d00040,0x18904,0x10d00044,0x18904,0x10d00048,0x18904,0x10d0004c,0x18904,0x10d00050,0x18a04,0x10d00054,0x18a04,0x10d00058,0x18a04,0x10d0005c,0x18a04,0x10d00060,0x18b04,0x10d00064,0x18b04,0x10d00068,0x18b04,0x10d0006c,0x18b04,0x10d00070,0x18c04,0x10d00074,0x18c04,0x10d00078,0x18c04,0x10d0007c,0x18c04,0x38c08000,0x73047b08,0x38c08008,0x73047b08,0x38c08010,0x73047b08,0x38c08018,0x73047b08,0x38c08020,0x73047b08,0x38c08028,0x73047b08,0x38c08030,0x73047b08,0x38c08038,0x73047b08,0x38c08040,0x73047b08,0x38c08048,0x73047b08,0x38c08050,0x73047b08,0x38c08058,0x73047b08,0x38c08060,0x73047b08,0x38c08068,0x73047b08,0x38c08070,0x73047b08,0x38c08078,0x73047b08,0x50a1c000,0x570e5801,0x50a1c001,0x570e5801,0x50a1c002,0x570e5801,0x50a1c003,0x570e5801,0x50a1c200,0x570e5801,0x50a1c201,0x570e5801,0x50a1c202,0x570e5801,0x50a1c203,0x570e5801,0x50a1c400,0x570e5801,0x50a1c401,0x570e5801,0x50a1c402,0x570e5801,0x50a1c403,0x570e5801,0x50a1c600,0x570e5801,0x50a1c601,0x570e5801,0x50a1c602,0x570e5801,0x50a1c603,0x570e5801,0x21d10000,0x85108d01,0x21d10001,0x85108e01,0x21d10002,0x85108f01,0x21d10003,0x85109001,0x21d10004,0x85109101,0x21d10005,0x85109201,0x21d10006,0x85109301,0x21d10007,0x85109401,0x21d10200,0x86108d01,0x21d10201,0x86108e01,0x21d10202,0x86108f01,0x21d10203,0x86109001,0x21d10204,0x86109101,0x21d10205,0x86109201,0x21d10206,0x86109301,0x21d10207,0x86109401,0x21d10400,0x87108d01,0x21d10401,0x87108e01,0x21d10402,0x87108f01,0x21d10403,0x87109001,0x21d10404,0x87109101,0x21d10405,0x87109201,0x21d10406,0x87109301,0x21d10407,0x87109401,0x21d10600,0x88108d01,0x21d10601,0x88108e01,0x21d10602,0x88108f01,0x21d10603,0x88109001,0x21d10604,0x88109101,0x21d10605,0x88109201,0x21d10606,0x88109301,0x21d10607,0x88109401,0x21d10800,0x89108d01,0x21d10801,0x89108e01,0x21d10802,0x89108f01,0x21d10803,0x89109001,0x21d10804,0x89109101,0x21d10805,0x89109201,0x21d10806,0x89109301,0x21d10807,0x89109401,0x21d10a00,0x8a108d01,0x21d10a01,0x8a108e01,0x21d10a02,0x8a108f01,0x21d10a03,0x8a109001,0x21d10a04,0x8a109101,0x21d10a05,0x8a109201,0x21d10a06,0x8a109301,0x21d10a07,0x8a109401,0x21d10c00,0x8b108d01,0x21d10c01,0x8b108e01,0x21d10c02,0x8b108f01,0x21d10c03,0x8b109001,0x21d10c04,0x8b109101,0x21d10c05,0x8b109201,0x21d10c06,0x8b109301,0x21d10c07,0x8b109401,0x21d10e00,0x8c108d01,0x21d10e01,0x8c108e01,0x21d10e02,0x8c108f01,0x21d10e03,0x8c109001,0x21d10e04,0x8c109101,0x21d10e05,0x8c109201,0x21d10e06,0x8c109301,0x21d10e07,0x8c109401,0x60908000,0x50100008,0x60908008,0x50100008,0x60908010,0x50100008,0x60908018,0x50100008,0x60908020,0x50100008,0x60908028,0x50100008,0x60908030,0x50100008,0x60908038,0x50100008,0x60908040,0x50100008,0x60908048,0x50100008,0x60908050,0x50100008,0x60908058,0x50100008,0x60908060,0x50100008,0x60908068,0x50100008,0x60908070,0x50100008,0x60908078,0x50100008,0x51b1c000,0x661c6701,0x51b1c001,0x661c6801,0x51b1c002,0x661c6901,0x51b1c003,0x661c6a01,0x51b1c004,0x661c6b01,0x51b1c005,0x661c6c01,0x51b1c006,0x661c6d01,0x51b1c007,0x661c6e01,0x51b1c200,0x661c6701,0x51b1c201,0x661c6801,0x51b1c202,0x661c6901,0x51b1c203,0x661c6a01,0x51b1c204,0x661c6b01,0x51b1c205,0x661c6c01,0x51b1c206,0x661c6d01,0x51b1c207,0x661c6e01,0x51b1c400,0x661c6701,0x51b1c401,0x661c6801,0x51b1c402,0x661c6901,0x51b1c403,0x661c6a01,0x51b1c404,0x661c6b01,0x51b1c405,0x661c6c01,0x51b1c406,0x661c6d01,0x51b1c407,0x661c6e01,0x51b1c600,0x661c6701,0x51b1c601,0x661c6801,0x51b1c602,0x661c6901,0x51b1c603,0x661c6a01,0x51b1c604,0x661c6b01,0x51b1c605,0x661c6c01,0x51b1c606,0x661c6d01,0x51b1c607,0x661c6e01,0x41c20000,0x7b807c01,0x41c20001,0x7b807c01,0x41c20002,0x7b807c01,0x41c20003,0x7b807c01,0x41c20004,0x7b807c01,0x41c20005,0x7b807c01,0x41c20006,0x7b807c01,0x41c20007,0x7b807c01,0x41c20008,0x7b807c01,0x41c20009,0x7b807c01,0x41c2000a,0x7b807c01,0x41c2000b,0x7b807c01,0x41c2000c,0x7b807c01,0x41c2000d,0x7b807c01,0x41c2000e,0x7b807c01,0x41c2000f,0x7b807c01,0x41c20010,0x7b807c01,0x41c20011,0x7b807c01,0x41c20012,0x7b807c01,0x41c20013,0x7b807c01,0x41c20014,0x7b807c01,0x41c20015,0x7b807c01,0x41c20016,0x7b807c01,0x41c20017,0x7b807c01,0x41c20018,0x7b807c01,0x41c20019,0x7b807c01,0x41c2001a,0x7b807c01,0x41c2001b,0x7b807c01,0x10e00000,0x1a604,0x10e00004,0x1a604,0x10e00008,0x1a604,0x10e0000c,0x1a604,0x10e00010,0x1a704,0x10e00014,0x1a704,0x10e00018,0x1a704,0x10e0001c,0x1a704,0x10e00020,0x1a804,0x10e00024,0x1a804,0x10e00028,0x1a804,0x10e0002c,0x1a804,0x10e00030,0x1a904,0x10e00034,0x1a904,0x10e00038,0x1a904,0x10e0003c,0x1a904,0x10e00040,0x1aa04,0x10e00044,0x1aa04,0x10e00048,0x1aa04,0x10e0004c,0x1aa04,0x10e00050,0x1ab04,0x10e00054,0x1ab04,0x10e00058,0x1ab04,0x10e0005c,0x1ab04,0x10e00060,0x1ac04,0x10e00064,0x1ac04,0x10e00068,0x1ac04,0x10e0006c,0x1ac04,0x10e00070,0x1ad04,0x10e00074,0x1ad04,0x10e00078,0x1ad04,0x10e0007c,0x1ad04,0x38d10000,0x8d089504,0x38d10004,0x8d089604,0x38d10008,0x8d089704,0x38d1000c,0x8d089804,0x38d10010,0x8d089904,0x38d10014,0x8d089a04,0x38d10018,0x8d089b04,0x38d1001c,0x8d089c04,0x38d10020,0x8d089504,0x38d10024,0x8d089604,0x38d10028,0x8d089704,0x38d1002c,0x8d089804,0x38d10030,0x8d089904,0x38d10034,0x8d089a04,0x38d10038,0x8d089b04,0x38d1003c,0x8d089c04,0x38d10040,0x8d089504,0x38d10044,0x8d089604,0x38d10048,0x8d089704,0x38d1004c,0x8d089804,0x38d10050,0x8d089904,0x38d10054,0x8d089a04,0x38d10058,0x8d089b04,0x38d1005c,0x8d089c04,0x38d10060,0x8d089504,0x38d10064,0x8d089604,0x38d10068,0x8d089704,0x38d1006c,0x8d089804,0x38d10070,0x8d089904,0x38d10074,0x8d089a04,0x38d10078,0x8d089b04,0x38d1007c,0x8d089c04,0x70000006,0x0,0x70000006,0x0,0x70000006,0x0,0x70000006,0x0,0x70000006,0x0,0x70000006,0x0,0x70000006,0x0,0x70000006,0x0,0x70000006,0x0,0x70000006,0x0,0x70000006,0x0,0x70000006,0x0,0x70000006,0x0,0x70000006,0x0,0x70000006,0x0,0x70000006,0x0,0x70000006,0x0,0x70000006,0x0,0x70000006,0x0,0x70000006,0x0,0x70000006,0x0,0x70000006,0x0,0x70000006,0x0,0x70000006,0x0,0x70000006,0x0,0x70000006,0x0,0x70000006,0x0,0x70000006,0x0,0x70000006,0x0,0x70000006,0x0,0x70000006,0x0,0x70000006,0x0,0x68b08000,0x67040008,0x68b08008,0x67040008,0x68b08010,0x67040008,0x68b08018,0x67040008,0x68b08020,0x67040008,0x68b08028,0x67040008,0x68b08030,0x67040008,0x68b08038,0x67040008,0x68b08040,0x67040008,0x68b08048,0x67040008,0x68b08050,0x67040008,0x68b08058,0x67040008,0x68b08060,0x67040008,0x68b08068,0x67040008,0x68b08070,0x67040008,0x68b08078,0x67040008,0x49d20000,0x95109d01,0x49d20001,0x95109d01,0x49d20002,0x95109d01,0x49d20003,0x95109d01,0x49d20004,0x95109d01,0x49d20005,0x95109d01,0x49d20006,0x95109d01,0x49d20007,0x95109d01,0x49d20008,0x95109d01,0x49d20009,0x95109d01,0x49d2000a,0x95109d01,0x49d2000b,0x95109d01,0x49d2000c,0x95109d01,0x49d2000d,0x95109d01,0x49d2000e,0x95109d01,0x49d2000f,0x95109d01,0x49d20010,0x95109d01,0x49d20011,0x95109d01,0x49d20012,0x95109d01,0x49d20013,0x95109d01,0x49d20014,0x95109d01,0x49d20015,0x95109d01,0x49d20016,0x95109d01,0x49d20017,0x95109d01,0x49d20018,0x95109d01,0x49d20019,0x95109d01,0x49d2001a,0x95109d01,0x49d2001b,0x95109d01,0x21e10000,0xa610ae01,0x21e10001,0xa610af01,0x21e10002,0xa610b001,0x21e10003,0xa610b101,0x21e10004,0xa610b201,0x21e10005,0xa610b301,0x21e10006,0xa610b401,0x21e10007,0xa610b501,0x21e10200,0xa710ae01,0x21e10201,0xa710af01,0x21e10202,0xa710b001,0x21e10203,0xa710b101,0x21e10204,0xa710b201,0x21e10205,0xa710b301,0x21e10206,0xa710b401,0x21e10207,0xa710b501,0x21e10400,0xa810ae01,0x21e10401,0xa810af01,0x21e10402,0xa810b001,0x21e10403,0xa810b101,0x21e10404,0xa810b201,0x21e10405,0xa810b301,0x21e10406,0xa810b401,0x21e10407,0xa810b501,0x21e10600,0xa910ae01,0x21e10601,0xa910af01,0x21e10602,0xa910b001,0x21e10603,0xa910b101,0x21e10604,0xa910b201,0x21e10605,0xa910b301,0x21e10606,0xa910b401,0x21e10607,0xa910b501,0x21e10800,0xaa10ae01,0x21e10801,0xaa10af01,0x21e10802,0xaa10b001,0x21e10803,0xaa10b101,0x21e10804,0xaa10b201,0x21e10805,0xaa10b301,0x21e10806,0xaa10b401,0x21e10807,0xaa10b501,0x60a08000,0x58100008,0x60a08008,0x58100008,0x60a08010,0x58100008,0x60a08018,0x58100008,0x60a08020,0x58100008,0x60a08028,0x58100008,0x60a08030,0x58100008,0x60a08038,0x58100008,0x60a08040,0x58100008,0x60a08048,0x58100008,0x60a08050,0x58100008,0x60a08058,0x58100008,0x60a08060,0x58100008,0x60a08068,0x58100008,0x60a08070,0x58100008,0x60a08078,0x58100008,0x21e10a00,0xab10ae01,0x21e10a01,0xab10af01,0x21e10a02,0xab10b001,0x21e10a03,0xab10b101,0x21e10a04,0xab10b201,0x21e10a05,0xab10b301,0x21e10a06,0xab10b401,0x21e10a07,0xab10b501,0x21e10c00,0xac10ae01,0x21e10c01,0xac10af01,0x21e10c02,0xac10b001,0x21e10c03,0xac10b101,0x21e10c04,0xac10b201,0x21e10c05,0xac10b301,0x21e10c06,0xac10b401,0x21e10c07,0xac10b501,0x21e10e00,0xad10ae01,0x21e10e01,0xad10af01,0x21e10e02,0xad10b001,0x21e10e03,0xad10b101,0x21e10e04,0xad10b201,0x21e10e05,0xad10b301,0x21e10e06,0xad10b401,0x21e10e07,0xad10b501,0x51c1c000,0x7c1c7d01,0x51c1c001,0x7c1c7e01,0x51c1c002,0x7c1c7f01,0x51c1c003,0x7c1c8001,0x51c1c004,0x7c1c8101,0x51c1c005,0x7c1c8201,0x51c1c006,0x7c1c8301,0x51c1c007,0x7c1c8401,0x51c1c200,0x7c1c7d01,0x51c1c201,0x7c1c7e01,0x51c1c202,0x7c1c7f01,0x51c1c203,0x7c1c8001,0x51c1c204,0x7c1c8101,0x51c1c205,0x7c1c8201,0x51c1c206,0x7c1c8301,0x51c1c207,0x7c1c8401,0x51c1c400,0x7c1c7d01,0x51c1c401,0x7c1c7e01,0x51c1c402,0x7c1c7f01,0x51c1c403,0x7c1c8001,0x51c1c404,0x7c1c8101,0x51c1c405,0x7c1c8201,0x51c1c406,0x7c1c8301,0x51c1c407,0x7c1c8401,0x51c1c600,0x7c1c7d01,0x51c1c601,0x7c1c7e01,0x51c1c602,0x7c1c7f01,0x51c1c603,0x7c1c8001,0x51c1c604,0x7c1c8101,0x51c1c605,0x7c1c8201,0x51c1c606,0x7c1c8301,0x51c1c607,0x7c1c8401,0x70000006,0x0,0x70000006,0x0,0x70000006,0x0,0x70000006,0x0,0x70000006,0x0,0x70000006,0x0,0x70000006,0x0,0x70000006,0x0,0x70000006,0x0,0x70000006,0x0,0x70000006,0x0,0x70000006,0x0,0x70000006,0x0,0x70000006,0x0,0x70000006,0x0,0x70000006,0x0,0x70000006,0x0,0x70000006,0x0,0x70000006,0x0,0x70000006,0x0,0x70000006,0x0,0x70000006,0x0,0x70000006,0x0,0x70000006,0x0,0x70000006,0x0,0x70000006,0x0,0x70000006,0x0,0x70000006,0x0,0x70000006,0x0,0x70000006,0x0,0x70000006,0x0,0x70000006,0x0,0x70000006,0x0,0x70000006,0x0,0x70000006,0x0,0x70000006,0x0,0x70000006,0x0,0x70000006,0x0,0x70000006,0x0,0x70000006,0x0,0x70000006,0x0,0x70000006,0x0,0x70000006,0x0,0x70000006,0x0,0x70000006,0x0,0x70000006,0x0,0x70000006,0x0,0x70000006,0x0,0x70000006,0x0,0x70000006,0x0,0x70000006,0x0,0x70000006,0x0,0x70000006,0x0,0x70000006,0x0,0x70000006,0x0,0x70000006,0x0,0x70000006,0x0,0x70000006,0x0,0x70000006,0x0,0x70000006,0x0,0x70000006,0x0,0x70000006,0x0,0x70000006,0x0,0x70000006,0x0,0x38e10000,0xae08b604,0x38e10004,0xae08b704,0x38e10008,0xae08b804,0x38e1000c,0xae08b904,0x38e10010,0xae08ba04,0x38e10014,0xae08bb04,0x38e10018,0xae08bc04,0x38e1001c,0xae08bd04,0x38e10020,0xae08b604,0x38e10024,0xae08b704,0x38e10028,0xae08b804,0x38e1002c,0xae08b904,0x38e10030,0xae08ba04,0x38e10034,0xae08bb04,0x38e10038,0xae08bc04,0x38e1003c,0xae08bd04,0x38e10040,0xae08b604,0x38e10044,0xae08b704,0x38e10048,0xae08b804,0x38e1004c,0xae08b904,0x38e10050,0xae08ba04,0x38e10054,0xae08bb04,0x38e10058,0xae08bc04,0x38e1005c,0xae08bd04,0x38e10060,0xae08b604,0x38e10064,0xae08b704,0x38e10068,0xae08b804,0x38e1006c,0xae08b904,0x38e10070,0xae08ba04,0x38e10074,0xae08bb04,0x38e10078,0xae08bc04,0x38e1007c,0xae08bd04,0x51d10000,0x9d1c9e01,0x51d10001,0x9d1c9f01,0x51d10002,0x9d1ca001,0x51d10003,0x9d1ca101,0x51d10004,0x9d1ca201,0x51d10005,0x9d1ca301,0x51d10006,0x9d1ca401,0x51d10007,0x9d1ca501,0x51d10200,0x9d1c9e01,0x51d10201,0x9d1c9f01,0x51d10202,0x9d1ca001,0x51d10203,0x9d1ca101,0x51d10204,0x9d1ca201,0x51d10205,0x9d1ca301,0x51d10206,0x9d1ca401,0x51d10207,0x9d1ca501,0x51d10400,0x9d1c9e01,0x51d10401,0x9d1c9f01,0x51d10402,0x9d1ca001,0x51d10403,0x9d1ca101,0x51d10404,0x9d1ca201,0x51d10405,0x9d1ca301,0x51d10406,0x9d1ca401,0x51d10407,0x9d1ca501,0x51d10600,0x9d1c9e01,0x51d10601,0x9d1c9f01,0x51d10602,0x9d1ca001,0x51d10603,0x9d1ca101,0x51d10604,0x9d1ca201,0x51d10605,0x9d1ca301,0x51d10606,0x9d1ca401,0x51d10607,0x9d1ca501,0x51d10800,0x9d1c9e01,0x51d10801,0x9d1c9f01,0x51d10802,0x9d1ca001,0x51d10803,0x9d1ca101,0x51d10804,0x9d1ca201,0x51d10805,0x9d1ca301,0x51d10806,0x9d1ca401,0x51d10807,0x9d1ca501,0x51d10a00,0x9d1c9e01,0x51d10a01,0x9d1c9f01,0x51d10a02,0x9d1ca001,0x51d10a03,0x9d1ca101,0x51d10a04,0x9d1ca201,0x51d10a05,0x9d1ca301,0x51d10a06,0x9d1ca401,0x51d10a07,0x9d1ca501,0x51d10c00,0x9d1c9e01,0x51d10c01,0x9d1c9f01,0x51d10c02,0x9d1ca001,0x51d10c03,0x9d1ca101,0x51d10c04,0x9d1ca201,0x51d10c05,0x9d1ca301,0x51d10c06,0x9d1ca401,0x51d10c07,0x9d1ca501,0x49e20000,0xb610be01,0x49e20001,0xb610be01,0x49e20002,0xb610be01,0x49e20003,0xb610be01,0x49e20004,0xb610be01,0x49e20005,0xb610be01,0x49e20006,0xb610be01,0x49e20007,0xb610be01,0x49e20008,0xb610be01,0x49e20009,0xb610be01,0x49e2000a,0xb610be01,0x49e2000b,0xb610be01,0x49e2000c,0xb610be01,0x49e2000d,0xb610be01,0x49e2000e,0xb610be01,0x49e2000f,0xb610be01,0x49e20010,0xb610be01,0x49e20011,0xb610be01,0x49e20012,0xb610be01,0x49e20013,0xb610be01,0x49e20014,0xb610be01,0x49e20015,0xb610be01,0x49e20016,0xb610be01,0x49e20017,0xb610be01,0x49e20018,0xb610be01,0x49e20019,0xb610be01,0x49e2001a,0xb610be01,0x49e2001b,0xb610be01,0x68c08000,0x7d040008,0x68c08008,0x7d040008,0x68c08010,0x7d040008,0x68c08018,0x7d040008,0x68c08020,0x7d040008,0x68c08028,0x7d040008,0x68c08030,0x7d040008,0x68c08038,0x7d040008,0x68c08040,0x7d040008,0x68c08048,0x7d040008,0x68c08050,0x7d040008,0x68c08058,0x7d040008,0x68c08060,0x7d040008,0x68c08068,0x7d040008,0x68c08070,0x7d040008,0x68c08078,0x7d040008,0x68d0e000,0x9e070004,0x68d0e004,0x9e070004,0x68d0e008,0x9e070004,0x68d0e00c,0x9e070004,0x68d0e010,0x9e070004,0x68d0e014,0x9e070004,0x68d0e018,0x9e070004,0x68d0e01c,0x9e070004,0x68d0e020,0x9e070004,0x68d0e024,0x9e070004,0x68d0e028,0x9e070004,0x68d0e02c,0x9e070004,0x68d0e030,0x9e070004,0x68d0e034,0x9e070004,0x68d0e038,0x9e070004,0x68d0e03c,0x9e070004,0x68d0e040,0x9e070004,0x68d0e044,0x9e070004,0x68d0e048,0x9e070004,0x68d0e04c,0x9e070004,0x68d0e050,0x9e070004,0x68d0e054,0x9e070004,0x68d0e058,0x9e070004,0x68d0e05c,0x9e070004,0x68d0e060,0x9e070004,0x68d0e064,0x9e070004,0x68d0e068,0x9e070004,0x68d0e06c,0x9e070004,0x68d0e070,0x9e070004,0x68d0e074,0x9e070004,0x68d0e078,0x9e070004,0x68d0e07c,0x9e070004,0x51e10000,0xbe1cbf01,0x51e10001,0xbe1cc001,0x51e10002,0xbe1cc101,0x51e10003,0xbe1cc201,0x51e10004,0xbe1cc301,0x51e10005,0xbe1cc401,0x51e10006,0xbe1cc501,0x51e10007,0xbe1cc601,0x51e10200,0xbe1cbf01,0x51e10201,0xbe1cc001,0x51e10202,0xbe1cc101,0x51e10203,0xbe1cc201,0x51e10204,0xbe1cc301,0x51e10205,0xbe1cc401,0x51e10206,0xbe1cc501,0x51e10207,0xbe1cc601,0x51e10400,0xbe1cbf01,0x51e10401,0xbe1cc001,0x51e10402,0xbe1cc101,0x51e10403,0xbe1cc201,0x51e10404,0xbe1cc301,0x51e10405,0xbe1cc401,0x51e10406,0xbe1cc501,0x51e10407,0xbe1cc601,0x51e10600,0xbe1cbf01,0x51e10601,0xbe1cc001,0x51e10602,0xbe1cc101,0x51e10603,0xbe1cc201,0x51e10604,0xbe1cc301,0x51e10605,0xbe1cc401,0x51e10606,0xbe1cc501,0x51e10607,0xbe1cc601,0x51e10800,0xbe1cbf01,0x51e10801,0xbe1cc001,0x51e10802,0xbe1cc101,0x51e10803,0xbe1cc201,0x51e10804,0xbe1cc301,0x51e10805,0xbe1cc401,0x51e10806,0xbe1cc501,0x51e10807,0xbe1cc601,0x51e10a00,0xbe1cbf01,0x51e10a01,0xbe1cc001,0x51e10a02,0xbe1cc101,0x51e10a03,0xbe1cc201,0x51e10a04,0xbe1cc301,0x51e10a05,0xbe1cc401,0x51e10a06,0xbe1cc501,0x51e10a07,0xbe1cc601,0x51e10c00,0xbe1cbf01,0x51e10c01,0xbe1cc001,0x51e10c02,0xbe1cc101,0x51e10c03,0xbe1cc201,0x51e10c04,0xbe1cc301,0x51e10c05,0xbe1cc401,0x51e10c06,0xbe1cc501,0x51e10c07,0xbe1cc601,0x68e0e000,0xbf070004,0x68e0e004,0xbf070004,0x68e0e008,0xbf070004,0x68e0e00c,0xbf070004,0x68e0e010,0xbf070004,0x68e0e014,0xbf070004,0x68e0e018,0xbf070004,0x68e0e01c,0xbf070004,0x68e0e020,0xbf070004,0x68e0e024,0xbf070004,0x68e0e028,0xbf070004,0x68e0e02c,0xbf070004,0x68e0e030,0xbf070004,0x68e0e034,0xbf070004,0x68e0e038,0xbf070004,0x68e0e03c,0xbf070004,0x68e0e040,0xbf070004,0x68e0e044,0xbf070004,0x68e0e048,0xbf070004,0x68e0e04c,0xbf070004,0x68e0e050,0xbf070004,0x68e0e054,0xbf070004,0x68e0e058,0xbf070004,0x68e0e05c,0xbf070004,0x68e0e060,0xbf070004,0x68e0e064,0xbf070004,0x68e0e068,0xbf070004,0x68e0e06c,0xbf070004,0x68e0e070,0xbf070004,0x68e0e074,0xbf070004,0x68e0e078,0xbf070004,0x68e0e07c,0xbf070004,0xffffffff,0xffffffff},
  {0x10f00000,0x1c704,0x10f00004,0x1c704,0x10f00008,0x1c804,0x10f0000c,0x1c804,0x10f00010,0x1c904,0x10f00014,0x1c904,0x10f00018,0x1ca04,0x10f0001c,0x1ca04,0x10f00020,0x1cb04,0x10f00024,0x1cb04,0x10f00028,0x1cc04,0x10f0002c,0x1cc04,0x10f00030,0x1cd04,0x10f00034,0x1cd04,0x10f00038,0x1ce04,0x10f0003c,0x1ce04,0x10f00040,0x1cf04,0x10f00044,0x1cf04,0x10f00048,0x1d004,0x10f0004c,0x1d004,0x10f00050,0x1d104,0x10f00054,0x1d104,0x10f00058,0x1d204,0x10f0005c,0x1d204,0x10f00060,0x1d304,0x10f00064,0x1d304,0x10f00068,0x1d404,0x10f0006c,0x1d404,0x10f00070,0x1d504,0x10f00074,0x1d504,0x10f00078,0x1d604,0x10f0007c,0x1d604,0x21f08000,0xc708d701,0x21f08001,0xc708d801,0x21f08002,0xc708d901,0x21f08003,0xc708da01,0x21f08004,0xc708db01,0x21f08005,0xc708dc01,0x21f08006,0xc708dd01,0x21f08007,0xc708de01,0x21f08200,0xc808d701,0x21f08201,0xc808d801,0x21f08202,0xc808d901,0x21f08203,0xc808da01,0x21f08204,0xc808db01,0x21f08205,0xc808dc01,0x21f08206,0xc808dd01,0x21f08207,0xc808de01,0x21f08400,0xc908d701,0x21f08401,0xc908d801,0x21f08402,0xc908d901,0x21f08403,0xc908da01,0x21f08404,0xc908db01,0x21f08405,0xc908dc01,0x21f08406,0xc908dd01,0x21f08407,0xc908de01,0x21f08600,0xca08d701,0x21f08601,0xca08d801,0x21f08602,0xca08d901,0x21f08603,0xca08da01,0x21f08604,0xca08db01,0x21f08605,0xca08dc01,0x21f08606,0xca08dd01,0x21f08607,0xca08de01,0x21f08800,0xcb08d701,0x21f08801,0xcb08d801,0x21f08802,0xcb08d901,0x21f08803,0xcb08da01,0x21f08804,0xcb08db01,0x21f08805,0xcb08dc01,0x21f08806,0xcb08dd01,0x21f08807,0xcb08de01,0x21f08a00,0xcc08d701,0x21f08a01,0xcc08d801,0x21f08a02,0xcc08d901,0x21f08a03,0xcc08da01,0x21f08a04,0xcc08db01,0x21f08a05,0xcc08dc01,0x21f08a06,0xcc08dd01,0x21f08a07,0xcc08de01,0x21f08c00,0xcd08d701,0x21f08c01,0xcd08d801,0x21f08c02,0xcd08d901,0x21f08c03,0xcd08da01,0x21f08c04,0xcd08db01,0x21f08c05,0xcd08dc01,0x21f08c06,0xcd08dd01,0x21f08c07,0xcd08de01,0x21f08e00,0xce08d701,0x21f08e01,0xce08d801,0x21f08e02,0xce08d901,0x21f08e03,0xce08da01,0x21f08e04,0xce08db01,0x21f08e05,0xce08dc01,0x21f08e06,0xce08dd01,0x21f08e07,0xce08de01,0x21f09000,0xcf08d701,0x21f09001,0xcf08d801,0x21f09002,0xcf08d901,0x21f09003,0xcf08da01,0x21f09004,0xcf08db01,0x21f09005,0xcf08dc01,0x21f09006,0xcf08dd01,0x21f09007,0xcf08de01,0x21f09200,0xd008d701,0x21f09201,0xd008d801,0x21f09202,0xd008d901,0x21f09203,0xd008da01,0x21f09204,0xd008db01,0x21f09205,0xd008dc01,0x21f09206,0xd008dd01,0x21f09207,0xd008de01,0x21f09400,0xd108d701,0x21f09401,0xd108d801,0x21f09402,0xd108d901,0x21f09403,0xd108da01,0x21f09404,0xd108db01,0x21f09405,0xd108dc01,0x21f09406,0xd108dd01,0x21f09407,0xd108de01,0x21f09600,0xd208d701,0x21f09601,0xd208d801,0x21f09602,0xd208d901,0x21f09603,0xd208da01,0x21f09604,0xd208db01,0x21f09605,0xd208dc01,0x21f09606,0xd208dd01,0x21f09607,0xd208de01,0x21f09800,0xd308d701,0x21f09801,0xd308d801,0x21f09802,0xd308d901,0x21f09803,0xd308da01,0x21f09804,0xd308db01,0x21f09805,0xd308dc01,0x21f09806,0xd308dd01,0x21f09807,0xd308de01,0x21f09a00,0xd408d701,0x21f09a01,0xd408d801,0x21f09a02,0xd408d901,0x21f09a03,0xd408da01,0x21f09a04,0xd408db01,0x21f09a05,0xd408dc01,0x21f09a06,0xd408dd01,0x21f09a07,0xd408de01,0x21f09c00,0xd508d701,0x21f09c01,0xd508d801,0x21f09c02,0xd508d901,0x21f09c03,0xd508da01,0x21f09c04,0xd508db01,0x21f09c05,0xd508dc01,0x21f09c06,0xd508dd01,0x21f09c07,0xd508de01,0x21f09e00,0xd608d701,0x21f09e01,0xd608d801,0x21f09e02,0xd608d901,0x21f09e03,0xd608da01,0x21f09e04,0xd608db01,0x21f09e05,0xd608dc01,0x21f09e06,0xd608dd01,0x21f09e07,0xd608de01,0x38f20000,0xd710df02,0x38f20002,0xd710e002,0x38f20004,0xd710e102,0x38f20006,0xd710e202,0x38f20008,0xd710e302,0x38f2000a,0xd710e402,0x38f2000c,0xd710e502,0x38f2000e,0xd710e602,0x38f20010,0xd710df02,0x38f20012,0xd710e002,0x38f20014,0xd710e102,0x38f20016,0xd710e202,0x38f20018,0xd710e302,0x38f2001a,0xd710e402,0x38f2001c,0xd710e502,0x38f2001e,0xd710e602,0x38f20020,0xd710df02,0x38f20022,0xd710e002,0x38f20024,0xd710e102,0x38f20026,0xd710e202,0x38f20028,0xd710e302,0x38f2002a,0xd710e402,0x38f2002c,0xd710e502,0x38f2002e,0xd710e602,0x38f20030,0xd710df02,0x38f20032,0xd710e002,0x38f20034,0xd710e102,0x38f20036,0xd710e202,0x38f20038,0xd710e302,0x38f2003a,0xd710e402,0x38f2003c,0xd710e502,0x38f2003e,0xd710e602,0x38f20040,0xd710e702,0x38f20042,0xd710e802,0x38f20044,0xd710e902,0x38f20046,0xd710ea02,0x38f20048,0xd710eb02,0x38f2004a,0xd710ec02,0x38f2004c,0xd710ed02,0x38f2004e,0xd710ee02,0x38f20050,0xd710e702,0x38f20052,0xd710e802,0x38f20054,0xd710e902,0x38f20056,0xd710ea02,0x38f20058,0xd710eb02,0x38f2005a,0xd710ec02,0x38f2005c,0xd710ed02,0x38f2005e,0xd710ee02,0x38f20060,0xd710e702,0x38f20062,0xd710e802,0x38f20064,0xd710e902,0x38f20066,0xd710ea02,0x38f20068,0xd710eb02,0x38f2006a,0xd710ec02,0x38f2006c,0xd710ed02,0x38f2006e,0xd710ee02,0x38f20070,0xd710e702,0x38f20072,0xd710e802,0x38f20074,0xd710e902,0x38f20076,0xd710ea02,0x38f20078,0xd710eb02,0x38f2007a,0xd710ec02,0x38f2007c,0xd710ed02,0x38f2007e,0xd710ee02,0x4af20000,0xdf08ef01,0x4af20001,0xdf08f001,0x4af20002,0xdf08f101,0x4af20003,0xdf08f201,0x4af20004,0xdf08f301,0x4af20005,0xdf08f401,0x4af20006,0xdf08f501,0x4af20007,0xdf08f601,0x4af20008,0xdf08ef01,0x4af20009,0xdf08f001,0x4af2000a,0xdf08f101,0x4af2000b,0xdf08f201,0x4af2000c,0xdf08f301,0x4af2000d,0xdf08f401,0x4af2000e,0xdf08f501,0x4af2000f,0xdf08f601,0x4af20010,0xdf08ef01,0x4af20011,0xdf08f001,0x4af20012,0xdf08f101,0x4af20013,0xdf08f201,0x4af20014,0xdf08f301,0x4af20015,0xdf08f401,0x4af20016,0xdf08f501,0x4af20017,0xdf08f601,0x4af20018,0xdf08ef01,0x4af20019,0xdf08f001,0x4af2001a,0xdf08f101,0x4af2001b,0xdf08f201,0x4af2001c,0xdf08f301,0x4af2001d,0xdf08f401,0x4af2001e,0xdf08f501,0x4af2001f,0xdf08f601,0x4af20020,0xdf08ef01,0x4af20021,0xdf08f001,0x4af20022,0xdf08f101,0x4af20023,0xdf08f201,0x4af20024,0xdf08f301,0x4af20025,0xdf08f401,0x4af20026,0xdf08f501,0x4af20027,0xdf08f601,0x4af20028,0xdf08ef01,0x4af20029,0xdf08f001,0x4af2002a,0xdf08f101,0x4af2002b,0xdf08f201,0x4af2002c,0xdf08f301,0x4af2002d,0xdf08f401,0x4af2002e,0xdf08f501,0x4af2002f,0xdf08f601,0x4af20030,0xdf08ef01,0x4af20031,0xdf08f001,0x4af20032,0xdf08f101,0x4af20033,0xdf08f201,0x4af20034,0xdf08f301,0x4af20035,0xdf08f401,0x4af20036,0xdf08f501,0x4af20037,0xdf08f601,0x4ef20000,0xe708ef01,0x4ef20001,0xe708f001,0x4ef20002,0xe708f101,0x4ef20003,0xe708f201,0x4ef20004,0xe708f301,0x4ef20005,0xe708f401,0x4ef20006,0xe708f501,0x4ef20007,0xe708f601,0x4ef20008,0xe708ef01,0x4ef20009,0xe708f001,0x4ef2000a,0xe708f101,0x4ef2000b,0xe708f201,0x4ef2000c,0xe708f301,0x4ef2000d,0xe708f401,0x4ef2000e,0xe708f501,0x4ef2000f,0xe708f601,0x4ef20010,0xe708ef01,0x4ef20011,0xe708f001,0x4ef20012,0xe708f101,0x4ef20013,0xe708f201,0x4ef20014,0xe708f301,0x4ef20015,0xe708f401,0x4ef20016,0xe708f501,0x4ef20017,0xe708f601,0x4ef20018,0xe708ef01,0x4ef20019,0xe708f001,0x4ef2001a,0xe708f101,0x4ef2001b,0xe708f201,0x4ef2001c,0xe708f301,0x4ef2001d,0xe708f401,0x4ef2001e,0xe708f501,0x4ef2001f,0xe708f601,0x4ef20020,0xe708ef01,0x4ef20021,0xe708f001,0x4ef20022,0xe708f101,0x4ef20023,0xe708f201,0x4ef20024,0xe708f301,0x4ef20025,0xe708f401,0x4ef20026,0xe708f501,0x4ef20027,0xe708f601,0x4ef20028,0xe708ef01,0x4ef20029,0xe708f001,0x4ef2002a,0xe708f101,0x4ef2002b,0xe708f201,0x4ef2002c,0xe708f301,0x4ef2002d,0xe708f401,0x4ef2002e,0xe708f501,0x4ef2002f,0xe708f601,0x4ef20030,0xe708ef01,0x4ef20031,0xe708f001,0x4ef20032,0xe708f101,0x4ef20033,0xe708f201,0x4ef20034,0xe708f301,0x4ef20035,0xe708f401,0x4ef20036,0xe708f501,0x4ef20037,0xe708f601,0x59f08000,0xef0ef701,0x59f08001,0xef0ef801,0x59f08002,0xef0ef901,0x59f08003,0xef0efa01,0x59f08004,0xef0efb01,0x59f08005,0xef0efc01,0x59f08006,0xef0efd01,0x59f08007,0xef0efe01,0x59f08200,0xef0ef701,0x59f08201,0xef0ef801,0x59f08202,0xef0ef901,0x59f08203,0xef0efa01,0x59f08204,0xef0efb01,0x59f08205,0xef0efc01,0x59f08206,0xef0efd01,0x59f08207,0xef0efe01,0x59f08400,0xef0ef701,0x59f08401,0xef0ef801,0x59f08402,0xef0ef901,0x59f08403,0xef0efa01,0x59f08404,0xef0efb01,0x59f08405,0xef0efc01,0x59f08406,0xef0efd01,0x59f08407,0xef0efe01,0x59f08600,0xef0ef701,0x59f08601,0xef0ef801,0x59f08602,0xef0ef901,0x59f08603,0xef0efa01,0x59f08604,0xef0efb01,0x59f08605,0xef0efc01,0x59f08606,0xef0efd01,0x59f08607,0xef0efe01,0x59f08800,0xef0ef701,0x59f08801,0xef0ef801,0x59f08802,0xef0ef901,0x59f08803,0xef0efa01,0x59f08804,0xef0efb01,0x59f08805,0xef0efc01,0x59f08806,0xef0efd01,0x59f08807,0xef0efe01,0x59f08a00,0xef0ef701,0x59f08a01,0xef0ef801,0x59f08a02,0xef0ef901,0x59f08a03,0xef0efa01,0x59f08a04,0xef0efb01,0x59f08a05,0xef0efc01,0x59f08a06,0xef0efd01,0x59f08a07,0xef0efe01,0x59f08c00,0xef0ef701,0x59f08c01,0xef0ef801,0x59f08c02,0xef0ef901,0x59f08c03,0xef0efa01,0x59f08c04,0xef0efb01,0x59f08c05,0xef0efc01,0x59f08c06,0xef0efd01,0x59f08c07,0xef0efe01,0x59f08e00,0xef0ef701,0x59f08e01,0xef0ef801,0x59f08e02,0xef0ef901,0x59f08e03,0xef0efa01,0x59f08e04,0xef0efb01,0x59f08e05,0xef0efc01,0x59f08e06,0xef0efd01,0x59f08e07,0xef0efe01,0x59f09000,0xef0ef701,0x59f09001,0xef0ef801,0x59f09002,0xef0ef901,0x59f09003,0xef0efa01,0x59f09004,0xef0efb01,0x59f09005,0xef0efc01,0x59f09006,0xef0efd01,0x59f09007,0xef0efe01,0x59f09200,0xef0ef701,0x59f09201,0xef0ef801,0x59f09202,0xef0ef901,0x59f09203,0xef0efa01,0x59f09204,0xef0efb01,0x59f09205,0xef0efc01,0x59f09206,0xef0efd01,0x59f09207,0xef0efe01,0x59f09400,0xef0ef701,0x59f09401,0xef0ef801,0x59f09402,0xef0ef901,0x59f09403,0xef0efa01,0x59f09404,0xef0efb01,0x59f09405,0xef0efc01,0x59f09406,0xef0efd01,0x59f09407,0xef0efe01,0x59f09600,0xef0ef701,0x59f09601,0xef0ef801,0x59f09602,0xef0ef901,0x59f09603,0xef0efa01,0x59f09604,0xef0efb01,0x59f09605,0xef0efc01,0x59f09606,0xef0efd01,0x59f09607,0xef0efe01,0x59f09800,0xef0ef701,0x59f09801,0xef0ef801,0x59f09802,0xef0ef901,0x59f09803,0xef0efa01,0x59f09804,0xef0efb01,0x59f09805,0xef0efc01,0x59f09806,0xef0efd01,0x59f09807,0xef0efe01,0x59f09a00,0xef0ef701,0x59f09a01,0xef0ef801,0x59f09a02,0xef0ef901,0x59f09a03,0xef0efa01,0x59f09a04,0xef0efb01,0x59f09a05,0xef0efc01,0x59f09a06,0xef0efd01,0x59f09a07,0xef0efe01,0x68f1c000,0xf70e0002,0x68f1c002,0xf70e0002,0x68f1c004,0xf70e0002,0x68f1c006,0xf70e0002,0x68f1c008,0xf70e0002,0x68f1c00a,0xf70e0002,0x68f1c00c,0xf70e0002,0x68f1c00e,0xf70e0002,0x68f1c010,0xf70e0002,0x68f1c012,0xf70e0002,0x68f1c014,0xf70e0002,0x68f1c016,0xf70e0002,0x68f1c018,0xf70e0002,0x68f1c01a,0xf70e0002,0x68f1c01c,0xf70e0002,0x68f1c01e,0xf70e0002,0x68f1c020,0xf70e0002,0x68f1c022,0xf70e0002,0x68f1c024,0xf70e0002,0x68f1c026,0xf70e0002,0x68f1c028,0xf70e0002,0x68f1c02a,0xf70e0002,0x68f1c02c,0xf70e0002,0x68f1c02e,0xf70e0002,0x68f1c030,0xf70e0002,0x68f1c032,0xf70e0002,0x68f1c034,0xf70e0002,0x68f1c036,0xf70e0002,0x68f1c038,0xf70e0002,0x68f1c03a,0xf70e0002,0x68f1c03c,0xf70e0002,0x68f1c03e,0xf70e0002,0x68f1c040,0xf70e0002,0x68f1c042,0xf70e0002,0x68f1c044,0xf70e0002,0x68f1c046,0xf70e0002,0x68f1c048,0xf70e0002,0x68f1c04a,0xf70e0002,0x68f1c04c,0xf70e0002,0x68f1c04e,0xf70e0002,0x68f1c050,0xf70e0002,0x68f1c052,0xf70e0002,0x68f1c054,0xf70e0002,0x68f1c056,0xf70e0002,0x68f1c058,0xf70e0002,0x68f1c05a,0xf70e0002,0x68f1c05c,0xf70e0002,0x68f1c05e,0xf70e0002,0x68f1c060,0xf70e0002,0x68f1c062,0xf70e0002,0x68f1c064,0xf70e0002,0x68f1c066,0xf70e0002,0x68f1c068,0xf70e0002,0x68f1c06a,0xf70e0002,0x68f1c06c,0xf70e0002,0x68f1c06e,0xf70e0002,0x68f1c070,0xf70e0002,0x68f1c072,0xf70e0002,0x68f1c074,0xf70e0002,0x68f1c076,0xf70e0002,0x68f1c078,0xf70e0002,0x68f1c07a,0xf70e0002,0x68f1c07c,0xf70e0002,0x68f1c07e,0xf70e0002,0xffffffff,0xffffffff,0xffffffff,0xffffffff,0xffffffff,0xffffffff,0xffffffff,0xffffffff,0xffffffff,0xffffffff,0xffffffff,0xffffffff,0xffffffff,0xffffffff,0xffffffff,0xffffffff,0xffffffff,0xffffffff,0xffffffff,0xffffffff,0xffffffff,0xffffffff,0xffffffff,0xffffffff,0xffffffff,0xffffffff,0xffffffff,0xffffffff,0xffffffff,0xffffffff,0xffffffff,0xffffffff,0xffffffff,0xffffffff,0xffffffff,0xffffffff,0xffffffff,0xffffffff,0xffffffff,0xffffffff,0xffffffff,0xffffffff,0xffffffff,0xffffffff,0xffffffff,0xffffffff,0xffffffff,0xffffffff,0xffffffff,0xffffffff,0xffffffff,0xffffffff,0xffffffff,0xffffffff,0xffffffff,0xffffffff,0xffffffff,0xffffffff,0xffffffff,0xffffffff,0xffffffff,0xffffffff,0xffffffff,0xffffffff,0xffffffff,0xffffffff,0xffffffff,0xffffffff,0xffffffff,0xffffffff,0xffffffff,0xffffffff,0xffffffff,0xffffffff,0xffffffff,0xffffffff,0xffffffff,0xffffffff,0xffffffff,0xffffffff,0xffffffff,0xffffffff,0xffffffff,0xffffffff,0xffffffff,0xffffffff,0xffffffff,0xffffffff,0xffffffff,0xffffffff,0xffffffff,0xffffffff,0xffffffff,0xffffffff,0xffffffff,0xffffffff,0xffffffff,0xffffffff,0xffffffff,0xffffffff,0xffffffff,0xffffffff,0xffffffff,0xffffffff,0xffffffff,0xffffffff,0xffffffff,0xffffffff,0xffffffff,0xffffffff,0xffffffff,0xffffffff,0xffffffff,0xffffffff,0xffffffff,0xffffffff,0xffffffff,0xffffffff,0xffffffff,0xffffffff,0xffffffff,0xffffffff,0xffffffff,0xffffffff,0xffffffff,0xffffffff,0xffffffff,0xffffffff,0xffffffff,0xffffffff,0xffffffff,0xffffffff,0xffffffff,0xffffffff,0xffffffff,0xffffffff,0xffffffff,0xffffffff,0xffffffff,0xffffffff,0xffffffff,0xffffffff,0xffffffff,0xffffffff,0xffffffff,0xffffffff,0xffffffff,0xffffffff,0xffffffff,0xffffffff,0xffffffff,0xffffffff,0xffffffff,0xffffffff,0xffffffff,0xffffffff,0xffffffff,0xffffffff,0xffffffff,0xffffffff,0xffffffff,0xffffffff,0xffffffff,0xffffffff,0xffffffff,0xffffffff,0xffffffff,0xffffffff,0xffffffff,0xffffffff,0xffffffff,0xffffffff,0xffffffff,0xffffffff,0xffffffff,0xffffffff,0xffffffff,0xffffffff,0xffffffff,0xffffffff,0xffffffff,0xffffffff,0xffffffff,0xffffffff,0xffffffff,0xffffffff,0xffffffff,0xffffffff,0xffffffff,0xffffffff,0xffffffff,0xffffffff,0xffffffff,0xffffffff,0xffffffff,0xffffffff,0xffffffff,0xffffffff,0xffffffff,0xffffffff,0xffffffff,0xffffffff,0xffffffff,0xffffffff,0xffffffff,0xffffffff,0xffffffff,0xffffffff,0xffffffff,0xffffffff,0xffffffff,0xffffffff,0xffffffff,0xffffffff,0xffffffff,0xffffffff,0xffffffff,0xffffffff,0xffffffff,0xffffffff,0xffffffff,0xffffffff,0xffffffff,0xffffffff,0xffffffff,0xffffffff,0xffffffff,0xffffffff,0xffffffff,0xffffffff,0xffffffff,0xffffffff,0xffffffff,0xffffffff,0xffffffff,0xffffffff,0xffffffff,0xffffffff,0xffffffff,0xffffffff,0xffffffff,0xffffffff,0xffffffff,0xffffffff,0xffffffff,0xffffffff,0xffffffff,0xffffffff,0xffffffff,0xffffffff,0xffffffff,0xffffffff,0xffffffff,0xffffffff,0xffffffff,0xffffffff,0xffffffff,0xffffffff,0xffffffff,0xffffffff,0xffffffff,0xffffffff,0xffffffff,0xffffffff,0xffffffff,0xffffffff,0xffffffff,0xffffffff,0xffffffff,0xffffffff,0xffffffff,0xffffffff,0xffffffff,0xffffffff,0xffffffff,0xffffffff,0xffffffff,0xffffffff,0xffffffff,0xffffffff,0xffffffff,0xffffffff,0xffffffff,0xffffffff,0xffffffff,0xffffffff,0xffffffff,0xffffffff,0xffffffff,0xffffffff,0xffffffff,0xffffffff,0xffffffff,0xffffffff,0xffffffff,0xffffffff,0xffffffff,0xffffffff,0xffffffff,0xffffffff,0xffffffff,0xffffffff,0xffffffff,0xffffffff,0xffffffff,0xffffffff,0xffffffff,0xffffffff,0xffffffff,0xffffffff,0xffffffff,0xffffffff,0xffffffff,0xffffffff,0xffffffff,0xffffffff,0xffffffff,0xffffffff,0xffffffff,0xffffffff,0xffffffff,0xffffffff,0xffffffff,0xffffffff,0xffffffff,0xffffffff,0xffffffff,0xffffffff,0xffffffff,0xffffffff,0xffffffff,0xffffffff,0xffffffff,0xffffffff,0xffffffff,0xffffffff,0xffffffff,0xffffffff,0xffffffff,0xffffffff,0xffffffff,0xffffffff,0xffffffff,0xffffffff,0xffffffff,0xffffffff,0xffffffff,0xffffffff,0xffffffff,0xffffffff,0xffffffff,0xffffffff,0xffffffff,0xffffffff,0xffffffff,0xffffffff,0xffffffff,0xffffffff,0xffffffff,0xffffffff,0xffffffff,0xffffffff,0xffffffff,0xffffffff,0xffffffff,0xffffffff,0xffffffff,0xffffffff,0xffffffff,0xffffffff,0xffffffff,0xffffffff,0xffffffff,0xffffffff,0xffffffff,0xffffffff,0xffffffff,0xffffffff,0xffffffff,0xffffffff,0xffffffff,0xffffffff,0xffffffff,0xffffffff,0xffffffff,0xffffffff,0xffffffff,0xffffffff,0xffffffff,0xffffffff,0xffffffff,0xffffffff,0xffffffff,0xffffffff,0xffffffff,0xffffffff,0xffffffff,0xffffffff,0xffffffff,0xffffffff,0xffffffff,0xffffffff,0xffffffff,0xffffffff,0xffffffff,0xffffffff,0xffffffff,0xffffffff,0xffffffff,0xffffffff,0xffffffff,0xffffffff,0xffffffff,0xffffffff,0xffffffff,0xffffffff,0xffffffff,0xffffffff,0xffffffff,0xffffffff,0xffffffff,0xffffffff,0xffffffff,0xffffffff,0xffffffff,0xffffffff,0xffffffff,0xffffffff,0xffffffff,0xffffffff,0xffffffff,0xffffffff,0xffffffff,0xffffffff,0xffffffff,0xffffffff,0xffffffff,0xffffffff,0xffffffff,0xffffffff,0xffffffff,0xffffffff,0xffffffff,0xffffffff,0xffffffff,0xffffffff,0xffffffff,0xffffffff,0xffffffff,0xffffffff,0xffffffff,0xffffffff,0xffffffff,0xffffffff,0xffffffff,0xffffffff,0xffffffff,0xffffffff,0xffffffff,0xffffffff,0xffffffff,0xffffffff,0xffffffff,0xffffffff,0xffffffff,0xffffffff,0xffffffff,0xffffffff,0xffffffff,0xffffffff,0xffffffff,0xffffffff,0xffffffff,0xffffffff,0xffffffff,0xffffffff,0xffffffff,0xffffffff,0xffffffff,0xffffffff,0xffffffff,0xffffffff,0xffffffff,0xffffffff,0xffffffff,0xffffffff,0xffffffff,0xffffffff,0xffffffff,0xffffffff,0xffffffff,0xffffffff,0xffffffff,0xffffffff,0xffffffff,0xffffffff,0xffffffff,0xffffffff,0xffffffff,0xffffffff,0xffffffff,0xffffffff,0xffffffff,0xffffffff,0xffffffff,0xffffffff,0xffffffff,0xffffffff,0xffffffff,0xffffffff,0xffffffff,0xffffffff,0xffffffff,0xffffffff,0xffffffff,0xffffffff,0xffffffff,0xffffffff,0xffffffff,0xffffffff,0xffffffff,0xffffffff,0xffffffff,0xffffffff,0xffffffff,0xffffffff,0xffffffff,0xffffffff,0xffffffff,0xffffffff,0xffffffff,0xffffffff,0xffffffff,0xffffffff,0xffffffff,0xffffffff,0xffffffff,0xffffffff,0xffffffff,0xffffffff,0xffffffff,0xffffffff,0xffffffff,0xffffffff,0xffffffff,0xffffffff,0xffffffff,0xffffffff,0xffffffff,0xffffffff,0xffffffff,0xffffffff,0xffffffff,0xffffffff,0xffffffff,0xffffffff,0xffffffff,0xffffffff,0xffffffff,0xffffffff,0xffffffff,0xffffffff,0xffffffff,0xffffffff,0xffffffff,0xffffffff,0xffffffff,0xffffffff,0xffffffff,0xffffffff,0xffffffff,0xffffffff,0xffffffff,0xffffffff,0xffffffff,0xffffffff,0xffffffff,0xffffffff,0xffffffff,0xffffffff,0xffffffff,0xffffffff,0xffffffff,0xffffffff,0xffffffff,0xffffffff,0xffffffff,0xffffffff,0xffffffff,0xffffffff,0xffffffff,0xffffffff,0xffffffff,0xffffffff,0xffffffff,0xffffffff,0xffffffff,0xffffffff,0xffffffff,0xffffffff,0xffffffff,0xffffffff,0xffffffff,0xffffffff,0xffffffff,0xffffffff,0xffffffff,0xffffffff,0xffffffff,0xffffffff,0xffffffff,0xffffffff,0xffffffff,0xffffffff,0xffffffff,0xffffffff,0xffffffff,0xffffffff,0xffffffff,0xffffffff,0xffffffff,0xffffffff,0xffffffff,0xffffffff,0xffffffff,0xffffffff,0xffffffff,0xffffffff,0xffffffff,0xffffffff,0xffffffff,0xffffffff,0xffffffff,0xffffffff,0xffffffff,0xffffffff,0xffffffff,0xffffffff,0xffffffff,0xffffffff,0xffffffff,0xffffffff,0xffffffff,0xffffffff,0xffffffff,0xffffffff,0xffffffff,0xffffffff,0xffffffff,0xffffffff,0xffffffff,0xffffffff,0xffffffff,0xffffffff,0xffffffff,0xffffffff,0xffffffff,0xffffffff,0xffffffff,0xffffffff,0xffffffff,0xffffffff,0xffffffff,0xffffffff,0xffffffff,0xffffffff,0xffffffff,0xffffffff,0xffffffff,0xffffffff,0xffffffff,0xffffffff,0xffffffff,0xffffffff,0xffffffff,0xffffffff,0xffffffff,0xffffffff,0xffffffff,0xffffffff,0xffffffff,0xffffffff,0xffffffff,0xffffffff,0xffffffff,0xffffffff,0xffffffff,0xffffffff,0xffffffff,0xffffffff,0xffffffff,0xffffffff,0xffffffff,0xffffffff,0xffffffff,0xffffffff,0xffffffff,0xffffffff,0xffffffff,0xffffffff,0xffffffff,0xffffffff,0xffffffff,0xffffffff,0xffffffff,0xffffffff,0xffffffff,0xffffffff,0xffffffff,0xffffffff,0xffffffff,0xffffffff,0xffffffff,0xffffffff,0xffffffff,0xffffffff,0xffffffff,0xffffffff,0xffffffff,0xffffffff,0xffffffff,0xffffffff,0xffffffff,0xffffffff,0xffffffff,0xffffffff,0xffffffff,0xffffffff,0xffffffff,0xffffffff,0xffffffff,0xffffffff,0xffffffff,0xffffffff,0xffffffff,0xffffffff,0xffffffff,0xffffffff,0xffffffff,0xffffffff,0xffffffff,0xffffffff,0xffffffff,0xffffffff,0xffffffff,0xffffffff,0xffffffff,0xffffffff,0xffffffff,0xffffffff,0xffffffff,0xffffffff,0xffffffff,0xffffffff,0xffffffff,0xffffffff,0xffffffff,0xffffffff,0xffffffff,0xffffffff,0xffffffff,0xffffffff,0xffffffff,0xffffffff,0xffffffff,0xffffffff,0xffffffff,0xffffffff,0xffffffff,0xffffffff,0xffffffff,0xffffffff,0xffffffff,0xffffffff,0xffffffff,0xffffffff,0xffffffff,0xffffffff,0xffffffff,0xffffffff,0xffffffff,0xffffffff,0xffffffff,0xffffffff,0xffffffff,0xffffffff,0xffffffff,0xffffffff,0xffffffff,0xffffffff,0xffffffff,0xffffffff,0xffffffff,0xffffffff,0xffffffff,0xffffffff,0xffffffff,0xffffffff,0xffffffff,0xffffffff,0xffffffff,0xffffffff,0xffffffff,0xffffffff,0xffffffff,0xffffffff,0xffffffff,0xffffffff,0xffffffff,0xffffffff,0xffffffff,0xffffffff,0xffffffff,0xffffffff,0xffffffff,0xffffffff,0xffffffff,0xffffffff,0xffffffff,0xffffffff,0xffffffff,0xffffffff,0xffffffff,0xffffffff,0xffffffff,0xffffffff,0xffffffff,0xffffffff,0xffffffff,0xffffffff,0xffffffff,0xffffffff,0xffffffff,0xffffffff,0xffffffff,0xffffffff,0xffffffff,0xffffffff,0xffffffff,0xffffffff,0xffffffff,0xffffffff,0xffffffff,0xffffffff,0xffffffff,0xffffffff,0xffffffff,0xffffffff,0xffffffff,0xffffffff,0xffffffff,0xffffffff,0xffffffff,0xffffffff,0xffffffff,0xffffffff,0xffffffff,0xffffffff,0xffffffff,0xffffffff,0xffffffff,0xffffffff,0xffffffff,0xffffffff,0xffffffff,0xffffffff,0xffffffff,0xffffffff,0xffffffff,0xffffffff,0xffffffff,0xffffffff,0xffffffff,0xffffffff,0xffffffff,0xffffffff,0xffffffff,0xffffffff,0xffffffff,0xffffffff,0xffffffff,0xffffffff,0xffffffff,0xffffffff,0xffffffff,0xffffffff,0xffffffff,0xffffffff,0xffffffff,0xffffffff,0xffffffff,0xffffffff,0xffffffff,0xffffffff,0xffffffff,0xffffffff,0xffffffff,0xffffffff,0xffffffff,0xffffffff,0xffffffff,0xffffffff,0xffffffff,0xffffffff,0xffffffff,0xffffffff,0xffffffff,0xffffffff,0xffffffff,0xffffffff,0xffffffff,0xffffffff,0xffffffff,0xffffffff,0xffffffff,0xffffffff,0xffffffff,0xffffffff,0xffffffff,0xffffffff,0xffffffff,0xffffffff,0xffffffff,0xffffffff,0xffffffff,0xffffffff,0xffffffff,0xffffffff,0xffffffff,0xffffffff,0xffffffff,0xffffffff,0xffffffff,0xffffffff,0xffffffff,0xffffffff,0xffffffff,0xffffffff,0xffffffff,0xffffffff,0xffffffff,0xffffffff,0xffffffff,0xffffffff,0xffffffff,0xffffffff,0xffffffff,0xffffffff,0xffffffff,0xffffffff,0xffffffff,0xffffffff,0xffffffff,0xffffffff,0xffffffff,0xffffffff,0xffffffff,0xffffffff,0xffffffff,0xffffffff,0xffffffff,0xffffffff,0xffffffff,0xffffffff,0xffffffff,0xffffffff,0xffffffff,0xffffffff,0xffffffff,0xffffffff,0xffffffff,0xffffffff,0xffffffff,0xffffffff,0xffffffff,0xffffffff,0xffffffff,0xffffffff,0xffffffff,0xffffffff,0xffffffff,0xffffffff,0xffffffff,0xffffffff,0xffffffff,0xffffffff,0xffffffff,0xffffffff,0xffffffff,0xffffffff,0xffffffff,0xffffffff,0xffffffff,0xffffffff,0xffffffff,0xffffffff,0xffffffff,0xffffffff,0xffffffff,0xffffffff,0xffffffff,0xffffffff,0xffffffff,0xffffffff,0xffffffff,0xffffffff,0xffffffff,0xffffffff,0xffffffff,0xffffffff,0xffffffff,0xffffffff,0xffffffff,0xffffffff,0xffffffff,0xffffffff,0xffffffff,0xffffffff,0xffffffff,0xffffffff,0xffffffff,0xffffffff,0xffffffff,0xffffffff,0xffffffff,0xffffffff,0xffffffff,0xffffffff,0xffffffff,0xffffffff,0xffffffff,0xffffffff,0xffffffff,0xffffffff,0xffffffff,0xffffffff,0xffffffff,0xffffffff,0xffffffff,0xffffffff,0xffffffff,0xffffffff,0xffffffff,0xffffffff,0xffffffff,0xffffffff,0xffffffff,0xffffffff,0xffffffff,0xffffffff,0xffffffff,0xffffffff,0xffffffff,0xffffffff,0xffffffff,0xffffffff,0xffffffff,0xffffffff,0xffffffff,0xffffffff,0xffffffff,0xffffffff,0xffffffff,0xffffffff,0xffffffff,0xffffffff,0xffffffff,0xffffffff,0xffffffff,0xffffffff,0xffffffff,0xffffffff,0xffffffff,0xffffffff,0xffffffff,0xffffffff,0xffffffff,0xffffffff,0xffffffff,0xffffffff,0xffffffff,0xffffffff,0xffffffff,0xffffffff,0xffffffff,0xffffffff,0xffffffff,0xffffffff,0xffffffff,0xffffffff,0xffffffff,0xffffffff,0xffffffff,0xffffffff,0xffffffff,0xffffffff,0xffffffff,0xffffffff,0xffffffff,0xffffffff,0xffffffff,0xffffffff,0xffffffff,0xffffffff,0xffffffff,0xffffffff,0xffffffff,0xffffffff,0xffffffff,0xffffffff,0xffffffff,0xffffffff,0xffffffff,0xffffffff,0xffffffff,0xffffffff,0xffffffff,0xffffffff,0xffffffff,0xffffffff,0xffffffff,0xffffffff,0xffffffff,0xffffffff,0xffffffff,0xffffffff,0xffffffff,0xffffffff,0xffffffff,0xffffffff,0xffffffff,0xffffffff,0xffffffff,0xffffffff,0xffffffff,0xffffffff,0xffffffff,0xffffffff,0xffffffff,0xffffffff,0xffffffff,0xffffffff,0xffffffff,0xffffffff,0xffffffff,0xffffffff,0xffffffff,0xffffffff,0xffffffff,0xffffffff,0xffffffff,0xffffffff,0xffffffff,0xffffffff,0xffffffff,0xffffffff,0xffffffff,0xffffffff,0xffffffff,0xffffffff,0xffffffff,0xffffffff,0xffffffff,0xffffffff,0xffffffff,0xffffffff,0xffffffff,0xffffffff,0xffffffff,0xffffffff,0xffffffff,0xffffffff,0xffffffff,0xffffffff,0xffffffff,0xffffffff,0xffffffff,0xffffffff,0xffffffff,0xffffffff,0xffffffff,0xffffffff,0xffffffff,0xffffffff,0xffffffff,0xffffffff,0xffffffff,0xffffffff,0xffffffff,0xffffffff,0xffffffff,0xffffffff,0xffffffff,0xffffffff,0xffffffff,0xffffffff,0xffffffff,0xffffffff,0xffffffff,0xffffffff,0xffffffff,0xffffffff,0xffffffff,0xffffffff,0xffffffff,0xffffffff,0xffffffff,0xffffffff,0xffffffff,0xffffffff,0xffffffff,0xffffffff,0xffffffff,0xffffffff,0xffffffff,0xffffffff,0xffffffff,0xffffffff,0xffffffff,0xffffffff,0xffffffff,0xffffffff,0xffffffff,0xffffffff,0xffffffff,0xffffffff,0xffffffff,0xffffffff,0xffffffff,0xffffffff,0xffffffff,0xffffffff,0xffffffff,0xffffffff,0xffffffff,0xffffffff,0xffffffff,0xffffffff,0xffffffff,0xffffffff,0xffffffff,0xffffffff,0xffffffff,0xffffffff,0xffffffff,0xffffffff,0xffffffff,0xffffffff,0xffffffff,0xffffffff,0xffffffff,0xffffffff,0xffffffff,0xffffffff,0xffffffff,0xffffffff,0xffffffff,0xffffffff,0xffffffff,0xffffffff,0xffffffff,0xffffffff,0xffffffff,0xffffffff,0xffffffff,0xffffffff,0xffffffff,0xffffffff,0xffffffff,0xffffffff,0xffffffff,0xffffffff,0xffffffff,0xffffffff,0xffffffff,0xffffffff,0xffffffff,0xffffffff,0xffffffff,0xffffffff,0xffffffff,0xffffffff,0xffffffff,0xffffffff,0xffffffff,0xffffffff,0xffffffff,0xffffffff,0xffffffff,0xffffffff,0xffffffff,0xffffffff,0xffffffff,0xffffffff,0xffffffff,0xffffffff,0xffffffff,0xffffffff,0xffffffff,0xffffffff,0xffffffff,0xffffffff,0xffffffff,0xffffffff,0xffffffff,0xffffffff,0xffffffff,0xffffffff,0xffffffff,0xffffffff,0xffffffff,0xffffffff,0xffffffff,0xffffffff,0xffffffff,0xffffffff,0xffffffff,0xffffffff,0xffffffff,0xffffffff,0xffffffff,0xffffffff,0xffffffff,0xffffffff,0xffffffff,0xffffffff,0xffffffff,0xffffffff,0xffffffff,0xffffffff,0xffffffff,0xffffffff,0xffffffff,0xffffffff,0xffffffff,0xffffffff,0xffffffff,0xffffffff,0xffffffff,0xffffffff,0xffffffff,0xffffffff,0xffffffff,0xffffffff,0xffffffff,0xffffffff,0xffffffff,0xffffffff,0xffffffff,0xffffffff,0xffffffff,0xffffffff,0xffffffff,0xffffffff,0xffffffff,0xffffffff,0xffffffff,0xffffffff,0xffffffff,0xffffffff,0xffffffff,0xffffffff,0xffffffff,0xffffffff,0xffffffff,0xffffffff,0xffffffff,0xffffffff,0xffffffff,0xffffffff,0xffffffff,0xffffffff,0xffffffff,0xffffffff,0xffffffff,0xffffffff,0xffffffff,0xffffffff,0xffffffff,0xffffffff,0xffffffff,0xffffffff,0xffffffff,0xffffffff,0xffffffff,0xffffffff,0xffffffff,0xffffffff,0xffffffff,0xffffffff,0xffffffff,0xffffffff,0xffffffff,0xffffffff,0xffffffff,0xffffffff,0xffffffff,0xffffffff,0xffffffff,0xffffffff,0xffffffff,0xffffffff,0xffffffff,0xffffffff,0xffffffff,0xffffffff,0xffffffff,0xffffffff,0xffffffff,0xffffffff,0xffffffff,0xffffffff,0xffffffff,0xffffffff,0xffffffff,0xffffffff,0xffffffff,0xffffffff,0xffffffff,0xffffffff,0xffffffff,0xffffffff,0xffffffff,0xffffffff,0xffffffff,0xffffffff,0xffffffff,0xffffffff,0xffffffff,0xffffffff,0xffffffff,0xffffffff,0xffffffff,0xffffffff,0xffffffff,0xffffffff,0xffffffff,0xffffffff,0xffffffff,0xffffffff,0xffffffff,0xffffffff,0xffffffff,0xffffffff,0xffffffff,0xffffffff,0xffffffff,0xffffffff,0xffffffff,0xffffffff,0xffffffff,0xffffffff,0xffffffff,0xffffffff,0xffffffff,0xffffffff,0xffffffff,0xffffffff,0xffffffff,0xffffffff,0xffffffff,0xffffffff,0xffffffff,0xffffffff,0xffffffff,0xffffffff,0xffffffff,0xffffffff,0xffffffff,0xffffffff,0xffffffff,0xffffffff,0xffffffff,0xffffffff,0xffffffff,0xffffffff,0xffffffff,0xffffffff,0xffffffff,0xffffffff,0xffffffff,0xffffffff,0xffffffff,0xffffffff,0xffffffff,0xffffffff,0xffffffff,0xffffffff,0xffffffff,0xffffffff,0xffffffff,0xffffffff,0xffffffff,0xffffffff,0xffffffff,0xffffffff,0xffffffff,0xffffffff,0xffffffff,0xffffffff,0xffffffff,0xffffffff,0xffffffff,0xffffffff,0xffffffff,0xffffffff,0xffffffff,0xffffffff,0xffffffff,0xffffffff,0xffffffff,0xffffffff,0xffffffff,0xffffffff,0xffffffff,0xffffffff,0xffffffff,0xffffffff,0xffffffff,0xffffffff,0xffffffff,0xffffffff,0xffffffff,0xffffffff,0xffffffff,0xffffffff,0xffffffff,0xffffffff,0xffffffff,0xffffffff,0xffffffff,0xffffffff,0xffffffff,0xffffffff,0xffffffff,0xffffffff,0xffffffff,0xffffffff,0xffffffff,0xffffffff,0xffffffff,0xffffffff,0xffffffff,0xffffffff,0xffffffff,0xffffffff,0xffffffff,0xffffffff,0xffffffff,0xffffffff,0xffffffff,0xffffffff,0xffffffff,0xffffffff,0xffffffff,0xffffffff,0xffffffff,0xffffffff,0xffffffff,0xffffffff,0xffffffff,0xffffffff,0xffffffff,0xffffffff,0xffffffff,0xffffffff,0xffffffff,0xffffffff,0xffffffff,0xffffffff,0xffffffff,0xffffffff,0xffffffff,0xffffffff,0xffffffff,0xffffffff,0xffffffff,0xffffffff,0xffffffff,0xffffffff,0xffffffff,0xffffffff,0xffffffff,0xffffffff,0xffffffff,0xffffffff,0xffffffff,0xffffffff,0xffffffff,0xffffffff,0xffffffff,0xffffffff,0xffffffff,0xffffffff,0xffffffff,0xffffffff,0xffffffff,0xffffffff,0xffffffff,0xffffffff,0xffffffff,0xffffffff,0xffffffff,0xffffffff,0xffffffff,0xffffffff,0xffffffff,0xffffffff,0xffffffff,0xffffffff,0xffffffff,0xffffffff,0xffffffff,0xffffffff,0xffffffff,0xffffffff,0xffffffff,0xffffffff,0xffffffff,0xffffffff,0xffffffff,0xffffffff,0xffffffff,0xffffffff,0xffffffff,0xffffffff,0xffffffff,0xffffffff,0xffffffff,0xffffffff,0xffffffff,0xffffffff,0xffffffff,0xffffffff,0xffffffff,0xffffffff,0xffffffff,0xffffffff,0xffffffff,0xffffffff,0xffffffff,0xffffffff,0xffffffff,0xffffffff,0xffffffff,0xffffffff,0xffffffff,0xffffffff,0xffffffff,0xffffffff,0xffffffff,0xffffffff,0xffffffff,0xffffffff,0xffffffff,0xffffffff,0xffffffff,0xffffffff,0xffffffff,0xffffffff,0xffffffff,0xffffffff,0xffffffff,0xffffffff,0xffffffff,0xffffffff,0xffffffff,0xffffffff,0xffffffff,0xffffffff,0xffffffff,0xffffffff,0xffffffff,0xffffffff,0xffffffff,0xffffffff,0xffffffff,0xffffffff,0xffffffff,0xffffffff,0xffffffff,0xffffffff,0xffffffff,0xffffffff,0xffffffff,0xffffffff,0xffffffff,0xffffffff,0xffffffff,0xffffffff,0xffffffff,0xffffffff,0xffffffff,0xffffffff,0xffffffff,0xffffffff,0xffffffff,0xffffffff,0xffffffff,0xffffffff,0xffffffff,0xffffffff,0xffffffff,0xffffffff,0xffffffff,0xffffffff,0xffffffff,0xffffffff,0xffffffff,0xffffffff,0xffffffff,0xffffffff,0xffffffff,0xffffffff,0xffffffff,0xffffffff,0xffffffff,0xffffffff,0xffffffff,0xffffffff,0xffffffff,0xffffffff,0xffffffff,0xffffffff,0xffffffff,0xffffffff,0xffffffff,0xffffffff,0xffffffff,0xffffffff,0xffffffff,0xffffffff,0xffffffff,0xffffffff,0xffffffff,0xffffffff,0xffffffff,0xffffffff,0xffffffff,0xffffffff,0xffffffff,0xffffffff,0xffffffff,0xffffffff,0xffffffff,0xffffffff,0xffffffff,0xffffffff,0xffffffff,0xffffffff,0xffffffff,0xffffffff,0xffffffff,0xffffffff,0xffffffff,0xffffffff,0xffffffff,0xffffffff,0xffffffff,0xffffffff,0xffffffff,0xffffffff,0xffffffff,0xffffffff,0xffffffff,0xffffffff,0xffffffff,0xffffffff,0xffffffff,0xffffffff,0xffffffff,0xffffffff,0xffffffff,0xffffffff,0xffffffff,0xffffffff,0xffffffff,0xffffffff,0xffffffff,0xffffffff,0xffffffff,0xffffffff,0xffffffff,0xffffffff,0xffffffff,0xffffffff,0xffffffff,0xffffffff,0xffffffff,0xffffffff,0xffffffff,0xffffffff,0xffffffff,0xffffffff,0xffffffff,0xffffffff,0xffffffff,0xffffffff,0xffffffff,0xffffffff,0xffffffff,0xffffffff,0xffffffff,0xffffffff,0xffffffff,0xffffffff,0xffffffff,0xffffffff,0xffffffff,0xffffffff,0xffffffff,0xffffffff,0xffffffff,0xffffffff,0xffffffff,0xffffffff,0xffffffff,0xffffffff,0xffffffff,0xffffffff,0xffffffff,0xffffffff,0xffffffff,0xffffffff,0xffffffff,0xffffffff,0xffffffff,0xffffffff,0xffffffff,0xffffffff,0xffffffff,0xffffffff,0xffffffff,0xffffffff,0xffffffff,0xffffffff,0xffffffff,0xffffffff,0xffffffff,0xffffffff,0xffffffff,0xffffffff,0xffffffff,0xffffffff,0xffffffff,0xffffffff,0xffffffff,0xffffffff,0xffffffff,0xffffffff,0xffffffff,0xffffffff,0xffffffff,0xffffffff,0xffffffff,0xffffffff,0xffffffff,0xffffffff,0xffffffff,0xffffffff,0xffffffff,0xffffffff,0xffffffff,0xffffffff,0xffffffff,0xffffffff,0xffffffff,0xffffffff,0xffffffff,0xffffffff,0xffffffff,0xffffffff,0xffffffff,0xffffffff,0xffffffff,0xffffffff,0xffffffff,0xffffffff,0xffffffff,0xffffffff,0xffffffff,0xffffffff,0xffffffff,0xffffffff,0xffffffff,0xffffffff,0xffffffff,0xffffffff,0xffffffff,0xffffffff,0xffffffff,0xffffffff,0xffffffff,0xffffffff,0xffffffff,0xffffffff,0xffffffff,0xffffffff,0xffffffff,0xffffffff,0xffffffff,0xffffffff,0xffffffff,0xffffffff,0xffffffff,0xffffffff,0xffffffff,0xffffffff,0xffffffff,0xffffffff,0xffffffff,0xffffffff,0xffffffff,0xffffffff,0xffffffff,0xffffffff,0xffffffff,0xffffffff,0xffffffff,0xffffffff,0xffffffff,0xffffffff,0xffffffff,0xffffffff,0xffffffff,0xffffffff,0xffffffff,0xffffffff,0xffffffff,0xffffffff,0xffffffff,0xffffffff,0xffffffff,0xffffffff,0xffffffff,0xffffffff,0xffffffff,0xffffffff,0xffffffff,0xffffffff,0xffffffff,0xffffffff,0xffffffff,0xffffffff,0xffffffff,0xffffffff,0xffffffff,0xffffffff,0xffffffff,0xffffffff,0xffffffff,0xffffffff,0xffffffff,0xffffffff,0xffffffff,0xffffffff,0xffffffff,0xffffffff,0xffffffff,0xffffffff,0xffffffff,0xffffffff,0xffffffff,0xffffffff,0xffffffff,0xffffffff,0xffffffff,0xffffffff,0xffffffff,0xffffffff,0xffffffff,0xffffffff,0xffffffff,0xffffffff,0xffffffff,0xffffffff,0xffffffff,0xffffffff,0xffffffff,0xffffffff,0xffffffff,0xffffffff,0xffffffff,0xffffffff,0xffffffff,0xffffffff,0xffffffff,0xffffffff,0xffffffff,0xffffffff,0xffffffff,0xffffffff,0xffffffff,0xffffffff,0xffffffff,0xffffffff,0xffffffff,0xffffffff,0xffffffff,0xffffffff,0xffffffff,0xffffffff,0xffffffff,0xffffffff,0xffffffff,0xffffffff,0xffffffff,0xffffffff,0xffffffff,0xffffffff,0xffffffff,0xffffffff,0xffffffff,0xffffffff},
};

enum { T_QKV = 0, T_ATT, T_WO, T_R1, T_F1, T_F2, T_R2, T_MOVE };
struct MainArgs { const u16* x; const u16* Wq; const u16* Wk; const u16* Wv; const u16* Wo; const u16* w1; const u16* w2; u16* out; unsigned char* ws; };

__global__ void __launch_bounds__(512, 2) k_main(MainArgs a) {
  extern __shared__ __attribute__((aligned(16))) unsigned char smem_raw[];
  LAS unsigned char* L = (LAS unsigned char*)smem_raw;
  int wave = __builtin_amdgcn_readfirstlane(threadIdx.x >> 6);
  GAS unsigned* ctl = (GAS unsigned*)a.ws;
  LAS unsigned* box = (LAS unsigned*)(L + 3 * 49152);
  if (threadIdx.x == 0) {
    int cls = (int)(__builtin_amdgcn_s_getreg(20 | (3 << 11)) & 7u);
    unsigned got = __hip_atomic_fetch_add(ctl + (C_SLOT + cls * 128) / 4, 1u, RLX_AGENT);
    for (int k = 0; got >= 32u && k < 8; ++k) { cls = k; got = __hip_atomic_fetch_add(ctl + (C_SLOT + cls * 128) / 4, 1u, RLX_AGENT); }
    *box = (unsigned)cls * 32u + (got & 31u);
  }
  __syncthreads();
  const int slot = __builtin_amdgcn_readfirstlane(*box);
  __syncthreads();
  const int cls = slot >> 5, sl = slot & 31;
  const int role = cls == 0 ? 0 : (cls <= 2 ? 1 + (sl & 1) : cls), idx = cls == 0 ? sl : (cls - 1) * 16 + (sl >> 1);
  if (role <= 2) {
    const int ring = (int)ring_base(a.ws);
    const int par = probe_parity(a.ws, L, ring, role * 32 + idx, wave, threadIdx.x & 63);
    if (threadIdx.x == 0) __hip_atomic_fetch_add(ctl + C_VOTE / 4, 1u + ((unsigned)par << 16), RLX_AGENT);
    if (role == 0) chain_P_asm(a.ws, a.Wq, a.Wk, wave, idx, ring, 0);
    else chain_VM_asm(a.ws, a.Wv, a.Wo, wave, idx, ring, role - 1);
  }
  int grp = role <= 2 ? NGRP - 1 - (CHAIN_SPLIT & idx) : role - 3;
  const rsrc_t rs = __builtin_amdgcn_make_buffer_rsrc(a.ws, 0, (int)WS_END, 0x00020000);
  int budget = SPIN_CAP;
  if (wave == 0 && fresh_lane() == 0) *box = __hip_atomic_fetch_add(ctl + (C_TICK + grp * 128) / 4, 1u, RLX_AGENT);
  __syncthreads();
  for (int flip = 0;; flip ^= 1) {
    asm volatile("" : "+s"(wave));
    const int lane = fresh_lane();
    const bool lead = wave == 0 && lane == 0;
    const unsigned tk = __builtin_amdgcn_readfirstlane(box[flip]);
    const v2u ent = *(const v2u*)&g_tasks[grp][2 * (tk < (unsigned)TL_LEN ? tk : (unsigned)TL_LEN - 1u)];
    const unsigned w0 = ent[0], w1 = ent[1];
    if (w0 == 0xffffffffu) break;
    const int ty = w0 >> 28, p = (w0 >> 20) & 15, nt = (w0 >> 13) & 127, kc = (w0 >> 9) & 15, i = w0 & 511, hf = (w0 >> 26) & 1, nit = w1 & 255;
    const int mode = (w0 >> 24) & 3;
    const bool tq = ty == T_QKV, tw = ty == T_WO, t1 = ty == T_F1, isg = tq | tw | t1 | (ty == T_F2);
    const int col = mode == 0 ? (i & 15) * 256 : i * (mode == 1 ? 128 : 64);
    const u16* Wx = i < 16 ? a.Wq : (i < 32 ? a.Wk : a.Wv);
    const u16* Wy = tw ? a.Wo : (t1 ? a.w1 : a.w2);
    const int abase = tq ? (int)WS_XN : (tw ? (int)WS_O : (t1 ? (int)WS_HN : (int)WS_H1));
    const int aoffs = abase + (p * ((tq | t1) ? 16 : (tw ? 64 : 56)) + kc * nt) * 16384 + hf * 8192;
    const int ldb2 = tq ? HD * 2 : (t1 ? FF * 2 : DM * 2);
    const rsrc_t rB = __builtin_amdgcn_make_buffer_rsrc((void*)(tq ? Wx : Wy), 0, (tq | tw) ? DM * HD * 2 : DM * FF * 2, 0x00020000);
    const int boffs = kc * nt * 64 * ldb2 + col * 2;
    Gemm<0> gb; Gemm<1> gm; Gemm<2> gs;
    if (isg) {
      if (mode == 0) { gb.init(rs, rB, aoffs, boffs, ldb2, nt, wave, lane); gb.prefetch_b(); }
      else if (mode == 1) { gm.init(rs, rB, aoffs, boffs, ldb2, nt, wave, lane); gm.prefetch_b(); }
      else { gs.init(rs, rB, aoffs, boffs, ldb2, nt, wave, lane); gs.prefetch_b(); }
    }
    if (ty == T_MOVE) grp = i;
    if (!tq) {
      if (wave == 0) {
        const int off = ty == T_ATT ? C_QKVF + (p * 48 + (lane < 3 ? lane : 0) * 16 + (i >> 3)) * 64 : C_CNT + ((w1 >> 24) + ((w0 >> 27) & 1 ? lane & 7 : 0)) * 128;
        const unsigned need = (w1 >> 16) & 255;
        while (__builtin_amdgcn_ballot_w64(__hip_atomic_load(ctl + off / 4, RLX_AGENT) < need) != 0ull && --budget > 0) __builtin_amdgcn_s_sleep(2);
      }
      BAR();
    }
    const int c = lane & 15, g = lane >> 4, gp = (g & 1) * 2 + (g >> 1);
    unsigned ntk = 0;
    auto ticket = [&]() { if (wave == 7 && lane == 0) ntk = __hip_atomic_fetch_add(ctl + (C_TICK + grp * 128) / 4, 1u, RLX_AGENT); };
    if (ty >= T_R2 || ty == T_R1) ticket();
    if (ty == T_ATT) {
      if (wave < nit) attn_item(rs, ctl, 4 * p + ((i + wave) & 3), (i + wave) >> 2, wave, lane, budget);
      else if (wave == 7) {
        GAS unsigned* w = ctl + (C_PROG + (i >> 2) * 128) / 4;
        while (__hip_atomic_load(w, RLX_AGENT) <= (unsigned)(4 * p + ((i + nit - 1) & 3)) && --budget > 0) __builtin_amdgcn_s_sleep(8);
      }
      ticket();
    } else if (ty == T_R1) {
      if (wave < nit) reduce_row<false>(rs, a.x, a.out, p * 128 + i + wave, nt, lane);
    } else if (ty == T_R2) {
      if (wave < nit) reduce_row<true>(rs, a.x, a.out, p * 128 + i + wave, nt, lane);
    } else if (ty == T_MOVE) {
    } else if (mode == 1) {
      f32x4 acc[4][4];
      gm.init(rs, rB, aoffs, boffs, ldb2, nt, wave, lane);
      gm.run(acc, lane, ticket);
      const int r0 = ((wave >> 1) & 1) * 64 + (wave >> 2) * 32 + c, wn = wave & 1;
#pragma unroll
      for (int m = 0; m < 2; ++m)
#pragma unroll
        for (int q = 0; q < 2; ++q) {
          const v4u w = pack_pair(acc[m][2 * q], acc[m][2 * q + 1]);
          if (t1) st16(silu8(w), rs, aimg(r0 + 16 * m, 4 * q + gp), (int)WS_H1 + (p * 56 + (col >> 6) + wn) * 16384);
          else st16(w, rs, (r0 + 16 * m) * 2048 + gp * 16, (int)WS_PART + (p * 8 + kc) * 262144 + (col + 64 * wn + 32 * q) * 2);
        }
    } else if (mode == 2) {
      f32x4 acc[2][2];
      gs.init(rs, rB, aoffs, boffs, ldb2, nt, wave, lane);
      gs.run(acc, lane, ticket);
      const int r = hf * 64 + ((wave >> 1) & 1) * 32 + (wave >> 2) * 16 + c, cc = (wave & 1) * 4 + gp;
      const v4u w = pack_pair(acc[0][0], acc[0][1]);
      if (t1) st16(silu8(w), rs, aimg(r, cc), (int)WS_H1 + (p * 56 + (col >> 6)) * 16384);
      else st16(w, rs, r * 2048 + cc * 16, (int)WS_PART + (p * 8 + kc) * 262144 + col * 2);
    } else {
      gb.init(rs, rB, aoffs, boffs, ldb2, nt, wave, lane);
      f32x4 acc[4][4];
      gb.run(acc, lane, ticket);
      const int wm = wave >> 2, wn = wave & 3;
      if (tq) {
        const int which = i >> 4, head = 2 * (i & 15) + (wn >> 1);
        const int vo = which == 2 ? c * 64 + gp * 16 : (gp * 16 + c) * 16;
#pragma unroll
        for (int m = 0; m < 4; ++m)
#pragma unroll
          for (int q = 0; q < 2; ++q) {
            const int blk = (int)WS_QKV + (((4 * p + 2 * wm + (m >> 1)) * NH + head) * 3 + which) * 8192, cg = 2 * (wn & 1) + q;
            st16(pack_pair(acc[m][2 * q], acc[m][2 * q + 1]), rs, vo, blk + (which == 2 ? cg * 2048 + (m & 1) * 1024 : (m & 1) * 4096 + cg * 1024));
          }
      } else if (t1) {
#pragma unroll
        for (int m = 0; m < 4; ++m)
#pragma unroll
          for (int q = 0; q < 2; ++q)
            st16(silu8(pack_pair(acc[m][2 * q], acc[m][2 * q + 1])), rs, aimg(64 * wm + 16 * m + c, 4 * q + gp), (int)WS_H1 + (p * 56 + (col >> 6) + wn) * 16384);
      } else {
#pragma unroll
        for (int m = 0; m < 4; ++m)
#pragma unroll
          for (int q = 0; q < 2; ++q)
            st16(pack_pair(acc[m][2 * q], acc[m][2 * q + 1]), rs, c * 2048 + gp * 16, (int)WS_PART + (p * 8 + kc) * 262144 + (64 * wm + 16 * m) * 2048 + (col + 64 * wn + 32 * q) * 2);
      }
    }
    PH(4);
    if (wave == 7 && lane == 0) box[flip ^ 1] = ntk;
    WAIT_V(0);
    __syncthreads();
    const int done = tq ? C_QKVF + (p * 48 + i) * 64 : C_CNT + ((w1 >> 8) & 255) * 128;
    if (lead) __hip_atomic_fetch_add(ctl + done / 4, (unsigned)nit, RLX_AGENT);
  }
}

extern "C" void kernel_launch(void* const* d_in, const int* in_sizes, int n_in, void* d_out, int out_size, void* d_ws, size_t ws_size,
                              hipStream_t stream) {
  unsigned char* ws = (unsigned char*)d_ws;
  k_init<<<S_TOK / 4, 256, 0, stream>>>((const u16*)d_in[0], (const u16*)d_in[1], ws);
  MainArgs a{(const u16*)d_in[0], (const u16*)d_in[2], (const u16*)d_in[3], (const u16*)d_in[4], (const u16*)d_in[5], (const u16*)d_in[8], (const u16*)d_in[9], (u16*)d_out, ws};
  (void)hipFuncSetAttribute((const void*)k_main, hipFuncAttributeMaxDynamicSharedMemorySize, CHAIN_LDS);
  k_main<<<256, 512, CHAIN_LDS, stream>>>(a);
}
```
